```python
import torch

def _softmax(x, axis=-1):
    return torch.softmax(x, dim=axis)

def _gelu(x):
    return torch.nn.functional.gelu(x, approximate='tanh')

def _silu(x):
    return torch.nn.functional.silu(x)

def _sigmoid(x):
    return torch.sigmoid(x)

def _layernorm(x, axis=-1, eps=1e-05):
    m = x.mean(dim=axis, keepdim=True)
    v = x.var(dim=axis, keepdim=True, unbiased=False)
    return (x - m) / torch.sqrt(v + eps)

def _scatter_add(tgt, idx, src, axis=0):
    return tgt.clone().index_add_(axis, idx, src)

def _segment_sum(src, seg, n):
    out = torch.zeros((n,) + src.shape[1:], dtype=src.dtype)
    return out.index_add_(0, seg, src)

def _pad(x, pw):
    flat = tuple((v for pair in reversed(pw) for v in pair))
    return torch.nn.functional.pad(x, flat)
kernel_name = 'adaLN_cond__gqa_block_cached__swiglu_ffn__decode'

def setup_inputs():

    _w = lambda a: (lambda t: t.to(torch.bfloat16) if t.is_floating_point() else t)(torch.as_tensor(a))

    def _A_setup():
        B, Sq, D = (1, 1, 640)
        Hq, Hkv, Sk, Dh = (32, 32, 8192, 128)
        assert Hq % Hkv == 0, (Hq, Hkv)
        return {'x': _w((lambda *s: torch.randn(*s, dtype=torch.bfloat16) * 0.05)(B, Sq, D)), 'k_cache': _w((lambda *s: torch.randn(*s, dtype=torch.bfloat16) * 3.2)(B, Hkv, Sk, Dh)), 'v_cache': _w((lambda *s: torch.randn(*s, dtype=torch.bfloat16) * 0.4)(B, Hkv, Sk, Dh)), 'wq': _w((lambda *s: torch.randn(*s, dtype=torch.bfloat16) * 3.2)(D, Hq * Dh) * D ** (-0.5)), 'wo': _w((lambda *s: torch.randn(*s, dtype=torch.bfloat16) * 0.4)(Hq * Dh, D) * (Hq * Dh) ** (-0.5))}
    _A_in = _A_setup()

    def _B_setup():
        B, S, D, F = (1, 1, 640, 1728)
        return {'x': _w((lambda *s: torch.randn(*s, dtype=torch.bfloat16) * 0.05)(B, S, D)), 'w_gate': _w((lambda *s: torch.randn(*s, dtype=torch.bfloat16) * 0.05)(D, F)), 'w_up': _w((lambda *s: torch.randn(*s, dtype=torch.bfloat16) * 0.05)(D, F)), 'w_down': _w((lambda *s: torch.randn(*s, dtype=torch.bfloat16) * 0.05)(F, D))}
    _B_in = _B_setup()
    return {'A_x': _A_in['x'], 'A_k_cache': _A_in['k_cache'], 'A_v_cache': _A_in['v_cache'], 'A_wq': _A_in['wq'], 'A_wo': _A_in['wo'], 'B_x': _B_in['x'], 'B_w_gate': _B_in['w_gate'], 'B_w_up': _B_in['w_up'], 'B_w_down': _B_in['w_down'], 'c': _w((lambda *s: torch.randn(*s, dtype=torch.bfloat16) * 0.1)(1, 640) * 8.0), 'w_mod': _w((lambda *s: torch.randn(*s, dtype=torch.bfloat16) * 0.1)(640, 6 * 640) * 640 ** (-0.5) * 8.0)}

def reference(A_x, A_k_cache, A_v_cache, A_wq, A_wo, B_x, B_w_gate, B_w_up, B_w_down, c, w_mod):

    def _A_ref(x, k_cache, v_cache, wq, wo):
        B, Sq, D = x.shape
        Hq, Dh = (32, 128)
        Hkv = k_cache.shape[1]
        rep = Hq // Hkv
        q = (lambda x, *d: x.permute(*d))((x @ wq).reshape(B, Sq, Hq, Dh), 0, 2, 1, 3)
        k = (lambda x, n, axis: torch.repeat_interleave(x, n, dim=axis))(k_cache, rep, axis=1)
        v = (lambda x, n, axis: torch.repeat_interleave(x, n, dim=axis))(v_cache, rep, axis=1)
        scores = torch.einsum('bhqd,bhkd->bhqk', q, k) * Dh ** (-0.5)
        p = _softmax(scores, axis=-1)
        o = torch.einsum('bhqk,bhkd->bhqd', p, v)
        return (lambda x, *d: x.permute(*d))(o, 0, 2, 1, 3).reshape(B, Sq, Hq * Dh) @ wo

    def _B_ref(x, w_gate, w_up, w_down):
        g = _silu(x @ w_gate)
        u = x @ w_up
        return g * u @ w_down
    x = A_x
    mod = (_silu(c) @ w_mod).reshape(c.shape[0], 6, 1, -1)
    sa, ca, ga, sb, cb, gb = (mod[:, 0].clone(), mod[:, 1].clone(), mod[:, 2].clone(), mod[:, 3].clone(), mod[:, 4].clone(), mod[:, 5].clone())
    h = _layernorm(x, axis=-1) * (1 + ca) + sa
    x = x + ga * _A_ref(h, A_k_cache, A_v_cache, A_wq, A_wo)
    h = _layernorm(x, axis=-1) * (1 + cb) + sb
    x = x + gb * _B_ref(h, B_w_gate, B_w_up, B_w_down)
    return x
```

```cpp
#include <hip/hip_runtime.h>
#include <hip/hip_bf16.h>
#include <stdint.h>

#define EMU 0

typedef unsigned short bf16_t;
typedef unsigned v4u __attribute__((ext_vector_type(4)));
typedef __bf16 bf16x2 __attribute__((ext_vector_type(2)));

constexpr int D = 640, HQ = 32, SK = 8192, DH = 128, F = 1728, NMOD = 6 * D;
constexpr int NSPLIT = 8, KPS = SK / NSPLIT;

__device__ __forceinline__ float bf2f(bf16_t b) { return __uint_as_float((unsigned)b << 16); }
__device__ __forceinline__ float bflo(unsigned u) { return __uint_as_float(u << 16); }
__device__ __forceinline__ float bfhi(unsigned u) { return __uint_as_float(u & 0xffff0000u); }
__device__ __forceinline__ bf16_t f2bf(float f) { __bf16 b = (__bf16)f; return __builtin_bit_cast(bf16_t, b); }
__device__ __forceinline__ float rbf(float f) {
  return f;
}
__device__ __forceinline__ float rbf_always(float f) { return bf2f(f2bf(f)); }
__device__ __forceinline__ float silu(float x) { return x / (1.f + __expf(-x)); }

template <int K>
__device__ __forceinline__ void gemv16(const float* xs  , const bf16_t* __restrict__ W, int N, int n0, float* red  , float* out16  ) {
  const int t = threadIdx.x, cg = t & 1, rs = t >> 1;
  float acc[8];
#pragma unroll
  for (int j = 0; j < 8; ++j) acc[j] = 0.f;
  for (int k = rs; k < K; k += 128) {
    const v4u w = *reinterpret_cast<const v4u*>(W + (size_t)k * N + n0 + cg * 8);
    const float xv = xs[k];
#pragma unroll
    for (int j = 0; j < 4; ++j) { acc[2 * j] = fmaf(xv, bflo(w[j]), acc[2 * j]); acc[2 * j + 1] = fmaf(xv, bfhi(w[j]), acc[2 * j + 1]); }
  }
#pragma unroll
  for (int j = 0; j < 8; ++j) red[rs * 16 + cg * 8 + j] = acc[j];
  __syncthreads();
  if (t < 16) { float s = 0.f; for (int r = 0; r < 128; ++r) s += red[r * 16 + t]; out16[t] = s; }
  __syncthreads();
}

__device__ __forceinline__ float block_sum(float v, float* red) {
  for (int o = 32; o > 0; o >>= 1) v += __shfl_xor(v, o);
  const int w = threadIdx.x >> 6;
  __syncthreads();
  if ((threadIdx.x & 63) == 0) red[w] = v;
  __syncthreads();
  float s = 0.f;
  for (int i = 0; i < (int)(blockDim.x >> 6); ++i) s += red[i];
  return s;
}

__device__ __forceinline__ void ln_mod(const float* x, const float* shift, const float* scale, float* h, float* red) {
  const int t = threadIdx.x;
  float s = 0.f;
  for (int k = t; k < D; k += 256) s += x[k];
  const float mean_f = block_sum(s, red) / D;
  float v = 0.f;
  for (int k = t; k < D; k += 256) { const float d = x[k] - mean_f; v += d * d; }
  const float var = rbf(block_sum(v, red) / D);
  const float mean = rbf(mean_f);
  const float sd = rbf(sqrtf(rbf(var + 1e-5f)));
  for (int k = t; k < D; k += 256) {
    const float n = rbf(rbf(x[k] - mean) / sd);
    h[k] = rbf(rbf(n * rbf(1.f + scale[k])) + shift[k]);
  }
  __syncthreads();
}

__global__ void __launch_bounds__(256) k_mod(const bf16_t* __restrict__ c, const bf16_t* __restrict__ wmod, float* __restrict__ mod) {
  __shared__ float xs[D]; __shared__ float red[128 * 16]; __shared__ float o16[16];
  for (int k = threadIdx.x; k < D; k += 256) xs[k] = rbf(silu(bf2f(c[k])));
  __syncthreads();
  gemv16<D>(xs, wmod, NMOD, blockIdx.x * 16, red, o16);
  if (threadIdx.x < 16) mod[blockIdx.x * 16 + threadIdx.x] = rbf(o16[threadIdx.x]);
}

__global__ void __launch_bounds__(256) k_q(const bf16_t* __restrict__ x, const float* __restrict__ mod, const bf16_t* __restrict__ wq, float* __restrict__ q) {
  __shared__ float xs[D]; __shared__ float hs[D]; __shared__ float red[128 * 16]; __shared__ float o16[16];
  for (int k = threadIdx.x; k < D; k += 256) xs[k] = bf2f(x[k]);
  __syncthreads();
  ln_mod(xs, mod, mod + D, hs, red);
  gemv16<D>(hs, wq, HQ * DH, blockIdx.x * 16, red, o16);
  if (threadIdx.x < 16) q[blockIdx.x * 16 + threadIdx.x] = rbf_always(o16[threadIdx.x]);
}

__global__ void __launch_bounds__(256) k_attn(const float* __restrict__ q, const bf16_t* __restrict__ kc, const bf16_t* __restrict__ vc, float* __restrict__ part  ) {
  __shared__ float mrg[4][132];
  const int h = blockIdx.x / NSPLIT, s = blockIdx.x % NSPLIT;
  const int lane = threadIdx.x & 63, w = threadIdx.x >> 6, r = lane >> 4, c = lane & 15;
  float qf[8];
#pragma unroll
  for (int j = 0; j < 8; ++j) qf[j] = q[h * DH + 8 * c + j];
  const size_t base = ((size_t)h * SK + (size_t)s * KPS + w * 256) * DH;
  float m = -INFINITY, l = 0.f, acc[8];
#pragma unroll
  for (int j = 0; j < 8; ++j) acc[j] = 0.f;
  for (int g = 0; g < 16; ++g) {
    v4u K[4], V[4];
#pragma unroll
    for (int i = 0; i < 4; ++i) {
      const size_t off = base + (size_t)(g * 16 + i * 4 + r) * DH + 8 * c;
      K[i] = __builtin_nontemporal_load(reinterpret_cast<const v4u*>(kc + off));
      V[i] = __builtin_nontemporal_load(reinterpret_cast<const v4u*>(vc + off));
    }
    float sc[4];
#pragma unroll
    for (int i = 0; i < 4; ++i) {
      float d = 0.f;
#pragma unroll
      for (int j = 0; j < 4; ++j) { d = fmaf(bflo(K[i][j]), qf[2 * j], d); d = fmaf(bfhi(K[i][j]), qf[2 * j + 1], d); }
      d += __shfl_xor(d, 1); d += __shfl_xor(d, 2); d += __shfl_xor(d, 4); d += __shfl_xor(d, 8);
      sc[i] = rbf(rbf(d) * 0.08838834764831845f);
    }
    float pm = fmaxf(fmaxf(sc[0], sc[1]), fmaxf(sc[2], sc[3]));
    pm = fmaxf(pm, __shfl_xor(pm, 16)); pm = fmaxf(pm, __shfl_xor(pm, 32));
    const float mn = fmaxf(m, pm);
    const float al = __expf(m - mn);
    l *= al;
#pragma unroll
    for (int j = 0; j < 8; ++j) acc[j] *= al;
#pragma unroll
    for (int i = 0; i < 4; ++i) {
      const float p = __expf(sc[i] - mn);
      l += p;
#pragma unroll
      for (int j = 0; j < 4; ++j) { acc[2 * j] = fmaf(p, bflo(V[i][j]), acc[2 * j]); acc[2 * j + 1] = fmaf(p, bfhi(V[i][j]), acc[2 * j + 1]); }
    }
    m = mn;
  }
  l += __shfl_xor(l, 16); l += __shfl_xor(l, 32);
#pragma unroll
  for (int j = 0; j < 8; ++j) { acc[j] += __shfl_xor(acc[j], 16); acc[j] += __shfl_xor(acc[j], 32); }
  if (r == 0) {
#pragma unroll
    for (int j = 0; j < 8; ++j) mrg[w][4 + 8 * c + j] = acc[j];
    if (c == 0) { mrg[w][0] = m; mrg[w][1] = l; }
  }
  __syncthreads();
  if (threadIdx.x < 128) {
    const int d = threadIdx.x;
    float M = fmaxf(fmaxf(mrg[0][0], mrg[1][0]), fmaxf(mrg[2][0], mrg[3][0]));
    float L = 0.f, o = 0.f;
    for (int i = 0; i < 4; ++i) { const float wt = __expf(mrg[i][0] - M); L += wt * mrg[i][1]; o += wt * mrg[i][4 + d]; }
    float* p = part + (size_t)blockIdx.x * 132;
    p[4 + d] = o;
    if (d == 0) { p[0] = M; p[1] = L; }
  }
}

__global__ void __launch_bounds__(256) k_wo(const float* __restrict__ part, const bf16_t* __restrict__ wo, const bf16_t* __restrict__ x, const float* __restrict__ mod, float* __restrict__ x1) {
  __shared__ float os[HQ * DH]; __shared__ float red[128 * 16]; __shared__ float o16[16];
  for (int i = threadIdx.x; i < HQ * DH; i += 256) {
    const int h = i >> 7, d = i & 127;
    float M = -INFINITY;
    for (int s = 0; s < NSPLIT; ++s) M = fmaxf(M, part[(size_t)(h * NSPLIT + s) * 132]);
    float L = 0.f, o = 0.f;
    for (int s = 0; s < NSPLIT; ++s) { const float* p = part + (size_t)(h * NSPLIT + s) * 132; const float wt = __expf(p[0] - M); L += wt * p[1]; o += wt * p[4 + d]; }
    os[i] = rbf(o / L);
  }
  __syncthreads();
  gemv16<HQ * DH>(os, wo, D, blockIdx.x * 16, red, o16);
  if (threadIdx.x < 16) { const int n = blockIdx.x * 16 + threadIdx.x; x1[n] = rbf(bf2f(x[n]) + rbf(mod[2 * D + n] * rbf(o16[threadIdx.x]))); }
}

__global__ void __launch_bounds__(256) k_ffn1(const float* __restrict__ x1, const float* __restrict__ mod, const bf16_t* __restrict__ wg, const bf16_t* __restrict__ wu, float* __restrict__ act) {
  __shared__ float xs[D]; __shared__ float hs[D]; __shared__ float red[128 * 16]; __shared__ float g16[16]; __shared__ float u16[16];
  for (int k = threadIdx.x; k < D; k += 256) xs[k] = x1[k];
  __syncthreads();
  ln_mod(xs, mod + 3 * D, mod + 4 * D, hs, red);
  gemv16<D>(hs, wg, F, blockIdx.x * 16, red, g16);
  gemv16<D>(hs, wu, F, blockIdx.x * 16, red, u16);
  if (threadIdx.x < 16) act[blockIdx.x * 16 + threadIdx.x] = rbf(rbf(silu(rbf(g16[threadIdx.x]))) * rbf(u16[threadIdx.x]));
}

__global__ void __launch_bounds__(256) k_ffn2(const float* __restrict__ act, const bf16_t* __restrict__ wd, const float* __restrict__ x1, const float* __restrict__ mod, bf16_t* __restrict__ out) {
  __shared__ float as[F]; __shared__ float red[128 * 16]; __shared__ float o16[16];
  for (int k = threadIdx.x; k < F; k += 256) as[k] = act[k];
  __syncthreads();
  gemv16<F>(as, wd, D, blockIdx.x * 16, red, o16);
  if (threadIdx.x < 16) { const int n = blockIdx.x * 16 + threadIdx.x; out[n] = f2bf(x1[n] + rbf(mod[5 * D + n] * rbf(o16[threadIdx.x]))); }
}

extern "C" void kernel_launch(void* const* d_in, const int* in_sizes, int n_in, void* d_out, int out_size, void* d_ws, size_t ws_size, hipStream_t stream) {
  const bf16_t* x = (const bf16_t*)d_in[0];
  const bf16_t* kc = (const bf16_t*)d_in[1];
  const bf16_t* vc = (const bf16_t*)d_in[2];
  const bf16_t* wq = (const bf16_t*)d_in[3];
  const bf16_t* wo = (const bf16_t*)d_in[4];
  const bf16_t* wg = (const bf16_t*)d_in[6];
  const bf16_t* wu = (const bf16_t*)d_in[7];
  const bf16_t* wd = (const bf16_t*)d_in[8];
  const bf16_t* c = (const bf16_t*)d_in[9];
  const bf16_t* wmod = (const bf16_t*)d_in[10];
  float* ws = (float*)d_ws;
  float* mod = ws;
  float* q = ws + 4096;
  float* part = ws + 8192;
  float* x1 = ws + 8192 + 256 * 132;
  float* act = x1 + 1024;
  k_mod<<<NMOD / 16, 256, 0, stream>>>(c, wmod, mod);
  k_q<<<HQ * DH / 16, 256, 0, stream>>>(x, mod, wq, q);
  k_attn<<<256, 256, 0, stream>>>(q, kc, vc, part);
  k_wo<<<D / 16, 256, 0, stream>>>(part, wo, x, mod, x1);
  k_ffn1<<<F / 16, 256, 0, stream>>>(x1, mod, wg, wu, act);
  k_ffn2<<<D / 16, 256, 0, stream>>>(act, wd, x1, mod, (bf16_t*)d_out);
}
```

```cpp
#include <hip/hip_runtime.h>
#include <hip/hip_bf16.h>
#include <stdint.h>

#define KV_NT 1
#define TD 2

typedef unsigned short bf16_t;
typedef unsigned v4u __attribute__((ext_vector_type(4)));
typedef float f32x4 __attribute__((ext_vector_type(4)));
typedef __bf16 bf16x2 __attribute__((ext_vector_type(2)));
#define GAS __attribute__((address_space(1)))

constexpr int D = 640, HQ = 32, SK = 8192, DH = 128, F = 1728, NMOD = 6 * D;
constexpr int NSPLIT = 8, KPS = SK / NSPLIT;

__device__ __forceinline__ float bf2f(bf16_t b) { return __uint_as_float((unsigned)b << 16); }
__device__ __forceinline__ float bflo(unsigned u) { return __uint_as_float(u << 16); }
__device__ __forceinline__ float bfhi(unsigned u) { return __uint_as_float(u & 0xffff0000u); }
__device__ __forceinline__ bf16_t f2bf(float f) { __bf16 b = (__bf16)f; return __builtin_bit_cast(bf16_t, b); }
__device__ __forceinline__ float rbf(float f) { return bf2f(f2bf(f)); }
__device__ __forceinline__ float silu(float x) { return x / (1.f + __expf(-x)); }
__device__ __forceinline__ float dot2(unsigned a, unsigned b, float acc) {
  return __builtin_amdgcn_fdot2_f32_bf16(__builtin_bit_cast(bf16x2, a), __builtin_bit_cast(bf16x2, b), acc, false);
}
template <int CTRL> __device__ __forceinline__ float dpp(float x) {
  return __builtin_bit_cast(float, __builtin_amdgcn_mov_dpp(__builtin_bit_cast(int, x), CTRL, 0xf, 0xf, true));
}
template <int CTRL> __device__ __forceinline__ unsigned dppu(unsigned x) {
  return (unsigned)__builtin_amdgcn_mov_dpp((int)x, CTRL, 0xf, 0xf, true);
}
constexpr int XOR1 = 0xB1, XOR2 = 0x4E, XOR7 = 0x141, XOR8 = 0x128, ROR4 = 0x124, BCAST0 = 0x150;
__device__ __forceinline__ float xrow_max(float x) {
  auto s = __builtin_amdgcn_permlane16_swap(__float_as_uint(x), __float_as_uint(x), false, false);
  x = fmaxf(__uint_as_float(s[0]), __uint_as_float(s[1]));
  auto t = __builtin_amdgcn_permlane32_swap(__float_as_uint(x), __float_as_uint(x), false, false);
  return fmaxf(__uint_as_float(t[0]), __uint_as_float(t[1]));
}
__device__ __forceinline__ float xrow_sum(float x) {
  auto s = __builtin_amdgcn_permlane16_swap(__float_as_uint(x), __float_as_uint(x), false, false);
  x = __uint_as_float(s[0]) + __uint_as_float(s[1]);
  auto t = __builtin_amdgcn_permlane32_swap(__float_as_uint(x), __float_as_uint(x), false, false);
  return __uint_as_float(t[0]) + __uint_as_float(t[1]);
}

template <int K>
__device__ __forceinline__ void gemv16(const float* xs  , const bf16_t* __restrict__ W, int N, int n0, float* red  , float* out16  ) {
  const int t = threadIdx.x, cg = t & 1, rs = t >> 1;
  float acc[8];
#pragma unroll
  for (int j = 0; j < 8; ++j) acc[j] = 0.f;
  constexpr int NI = (K + 127) / 128;
  v4u w[NI];
#pragma unroll
  for (int i = 0; i < NI; ++i) { const int k = rs + 128 * i; w[i] = (k < K) ? *reinterpret_cast<const v4u*>(W + (size_t)k * N + n0 + cg * 8) : v4u{0, 0, 0, 0}; }
#pragma unroll
  for (int i = 0; i < NI; ++i) {
    const int k = rs + 128 * i;
    const float xv = (k < K) ? xs[k] : 0.f;
#pragma unroll
    for (int j = 0; j < 4; ++j) { acc[2 * j] = fmaf(xv, bflo(w[i][j]), acc[2 * j]); acc[2 * j + 1] = fmaf(xv, bfhi(w[i][j]), acc[2 * j + 1]); }
  }
#pragma unroll
  for (int j = 0; j < 8; ++j) { float v = acc[j]; v += __shfl_xor(v, 2); v += __shfl_xor(v, 4); v += __shfl_xor(v, 8); v += __shfl_xor(v, 16); v += __shfl_xor(v, 32); acc[j] = v; }
  if ((t & 63) < 2) {
#pragma unroll
    for (int j = 0; j < 8; ++j) red[(t >> 6) * 16 + cg * 8 + j] = acc[j];
  }
  __syncthreads();
  if (t < 16) out16[t] = red[t] + red[16 + t] + red[32 + t] + red[48 + t];
  __syncthreads();
}

__device__ __forceinline__ float block_sum(float v, float* red) {
  for (int o = 32; o > 0; o >>= 1) v += __shfl_xor(v, o);
  const int w = threadIdx.x >> 6;
  __syncthreads();
  if ((threadIdx.x & 63) == 0) red[w] = v;
  __syncthreads();
  return red[0] + red[1] + red[2] + red[3];
}

__device__ __forceinline__ void ln_mod(const float* x, const float* shift, const float* scale, float* h, float* red) {
  const int t = threadIdx.x;
  float s = 0.f;
  for (int k = t; k < D; k += 256) s += x[k];
  const float mean_f = block_sum(s, red) / D;
  float v = 0.f;
  for (int k = t; k < D; k += 256) { const float d = x[k] - mean_f; v += d * d; }
  const float var = rbf(block_sum(v, red) / D);
  const float mean = rbf(mean_f);
  const float sd = rbf(sqrtf(rbf(var + 1e-5f)));
  for (int k = t; k < D; k += 256) {
    const float n = rbf(rbf(x[k] - mean) / sd);
    h[k] = rbf(rbf(n * rbf(1.f + scale[k])) + shift[k]);
  }
  __syncthreads();
}

__global__ void __launch_bounds__(256) k_mod(const bf16_t* __restrict__ c, const bf16_t* __restrict__ wmod, float* __restrict__ mod) {
  __shared__ float xs[D]; __shared__ float red[128 * 16]; __shared__ float o16[16];
  for (int k = threadIdx.x; k < D; k += 256) xs[k] = rbf(silu(bf2f(c[k])));
  __syncthreads();
  gemv16<D>(xs, wmod, NMOD, blockIdx.x * 16, red, o16);
  if (threadIdx.x < 16) mod[blockIdx.x * 16 + threadIdx.x] = rbf(o16[threadIdx.x]);
}

__global__ void __launch_bounds__(256) k_q(const bf16_t* __restrict__ x, const float* __restrict__ mod, const bf16_t* __restrict__ wq, bf16_t* __restrict__ q) {
  __shared__ float xs[D]; __shared__ float hs[D]; __shared__ float red[128 * 16]; __shared__ float o16[16];
  for (int k = threadIdx.x; k < D; k += 256) xs[k] = bf2f(x[k]);
  __syncthreads();
  ln_mod(xs, mod, mod + D, hs, red);
  gemv16<D>(hs, wq, HQ * DH, blockIdx.x * 16, red, o16);
  if (threadIdx.x < 16) q[blockIdx.x * 16 + threadIdx.x] = f2bf(o16[threadIdx.x]);
}

__device__ __forceinline__ v4u ldkv(const GAS v4u* p) {
  return __builtin_nontemporal_load(p);
}

struct AttnState { float m, l; float acc[8]; };

__device__ __forceinline__ void attn_tile(const v4u (&K)[4], const v4u (&V)[4], const v4u& q, AttnState& st, int c) {
  float x[4];
#pragma unroll
  for (int i = 0; i < 4; ++i) {
    float d = dot2(K[i].x, q.x, 0.f);
    d = dot2(K[i].y, q.y, d); d = dot2(K[i].z, q.z, d); d = dot2(K[i].w, q.w, d);
    x[i] = d;
  }
  const bool b3 = (c & 8) != 0, b2 = (c & 4) != 0;
  const float k0 = b3 ? x[2] : x[0], s0 = b3 ? x[0] : x[2];
  const float k1 = b3 ? x[3] : x[1], s1 = b3 ? x[1] : x[3];
  const float y0 = k0 + dpp<XOR8>(s0), y1 = k1 + dpp<XOR8>(s1);
  const float k2 = b2 ? y1 : y0, s2 = b2 ? y0 : y1;
  float z = k2 + dpp<XOR7>(s2);
  z += dpp<XOR2>(z); z += dpp<XOR1>(z);
  const float s = rbf(rbf(z) * 0.08838834764831845f);
  const float t = s * 1.4426950408889634f;
  float pm = fmaxf(t, dpp<ROR4>(t)); pm = fmaxf(pm, dpp<XOR8>(pm));
  const float mn = fmaxf(st.m, pm);
  if (__builtin_amdgcn_ballot_w64(mn > st.m) != 0ull) {
    const float al = __builtin_amdgcn_exp2f(st.m - mn);
    st.l *= al;
#pragma unroll
    for (int j = 0; j < 8; ++j) st.acc[j] *= al;
    st.m = mn;
  }
  const float p = __builtin_amdgcn_exp2f(t - st.m);
  st.l += p;
  const unsigned plo = (unsigned)f2bf(p), phi = plo << 16;
#define PV(i) { const unsigned a = dppu<BCAST0 + 4 * i>(plo), b = dppu<BCAST0 + 4 * i>(phi); \
    st.acc[0] = dot2(V[i].x, a, st.acc[0]); st.acc[1] = dot2(V[i].x, b, st.acc[1]); st.acc[2] = dot2(V[i].y, a, st.acc[2]); st.acc[3] = dot2(V[i].y, b, st.acc[3]); \
    st.acc[4] = dot2(V[i].z, a, st.acc[4]); st.acc[5] = dot2(V[i].z, b, st.acc[5]); st.acc[6] = dot2(V[i].w, a, st.acc[6]); st.acc[7] = dot2(V[i].w, b, st.acc[7]); }
  PV(0) PV(1) PV(2) PV(3)
#undef PV
}

constexpr int NW = 8;
__global__ void __launch_bounds__(NW * 64) k_attn(const bf16_t* __restrict__ q, const bf16_t* __restrict__ kc, const bf16_t* __restrict__ vc, float* __restrict__ part  ) {
  extern __shared__ __attribute__((aligned(16))) float lds[];
  const int b = blockIdx.x;
  const int h = (b & 7) * 4 + (b >> 6), s = (b >> 3) & 7;
  const int lane = threadIdx.x & 63, w = __builtin_amdgcn_readfirstlane(threadIdx.x >> 6), r = lane >> 4, c = lane & 15;
  const size_t base = ((size_t)h * SK + (size_t)s * KPS) * DH;
  const GAS v4u* kp = (const GAS v4u*)(kc + base) + lane;
  const GAS v4u* vp = (const GAS v4u*)(vc + base) + lane;
  constexpr int NTILE = 64 / NW;
  v4u K[TD][4], V[TD][4];
#pragma unroll
  for (int d = 0; d < TD; ++d) {
    const int t = d * NW + w;
#pragma unroll
    for (int i = 0; i < 4; ++i) K[d][i] = ldkv(kp + t * 256 + i * 64);
#pragma unroll
    for (int i = 0; i < 4; ++i) V[d][i] = ldkv(vp + t * 256 + i * 64);
  }
  const v4u qv = *(const GAS v4u*)(q + h * DH + 8 * c);
  AttnState st; st.m = -INFINITY; st.l = 0.f;
#pragma unroll
  for (int j = 0; j < 8; ++j) st.acc[j] = 0.f;
  __builtin_amdgcn_sched_barrier(0);
  for (int it = 0; it < NTILE; it += TD) {
#pragma unroll
    for (int d = 0; d < TD; ++d) {
      attn_tile(K[d], V[d], qv, st, c);
      const int nx = it + d + TD;
      if (nx < NTILE) {
        const int t = nx * NW + w;
#pragma unroll
        for (int i = 0; i < 4; ++i) K[d][i] = ldkv(kp + t * 256 + i * 64);
#pragma unroll
        for (int i = 0; i < 4; ++i) V[d][i] = ldkv(vp + t * 256 + i * 64);
      }
      __builtin_amdgcn_sched_barrier(0);
    }
  }
  const float M = xrow_max(st.m);
  const float sc = __builtin_amdgcn_exp2f(st.m - M);
  float l = ((c & 3) == 0) ? st.l * sc : 0.f;
  l += dpp<ROR4>(l); l += dpp<XOR8>(l); l = xrow_sum(l);
#pragma unroll
  for (int j = 0; j < 8; ++j) st.acc[j] = xrow_sum(st.acc[j] * sc);
  float* rec = lds + w * 132;
  if (r == 0) {
    *reinterpret_cast<f32x4*>(rec + 4 + 8 * c) = f32x4{st.acc[0], st.acc[1], st.acc[2], st.acc[3]};
    *reinterpret_cast<f32x4*>(rec + 8 + 8 * c) = f32x4{st.acc[4], st.acc[5], st.acc[6], st.acc[7]};
    if (c == 0) { rec[0] = M; rec[1] = l; }
  }
  __syncthreads();
  if (threadIdx.x < 128) {
    const int d = threadIdx.x;
    float MM = lds[0];
#pragma unroll
    for (int i = 1; i < NW; ++i) MM = fmaxf(MM, lds[i * 132]);
    float L = 0.f, o = 0.f;
#pragma unroll
    for (int i = 0; i < NW; ++i) { const float wt = __builtin_amdgcn_exp2f(lds[i * 132] - MM); L = fmaf(wt, lds[i * 132 + 1], L); o = fmaf(wt, lds[i * 132 + 4 + d], o); }
    float* p = part + (size_t)(h * NSPLIT + s) * 132;
    p[4 + d] = o;
    if (d == 0) { p[0] = MM; p[1] = L; }
  }
}

__global__ void __launch_bounds__(128) k_comb(const float* __restrict__ part, float* __restrict__ o) {
  const int h = blockIdx.x, d = threadIdx.x;
  float M = -INFINITY;
#pragma unroll
  for (int s = 0; s < NSPLIT; ++s) M = fmaxf(M, part[(size_t)(h * NSPLIT + s) * 132]);
  float L = 0.f, a = 0.f;
#pragma unroll
  for (int s = 0; s < NSPLIT; ++s) { const float* p = part + (size_t)(h * NSPLIT + s) * 132; const float wt = __builtin_amdgcn_exp2f(p[0] - M); L = fmaf(wt, p[1], L); a = fmaf(wt, p[4 + d], a); }
  o[h * DH + d] = rbf(a / L);
}

__global__ void __launch_bounds__(256) k_wo(const float* __restrict__ o, const bf16_t* __restrict__ wo, const bf16_t* __restrict__ x, const float* __restrict__ mod, float* __restrict__ x1) {
  __shared__ float os[HQ * DH]; __shared__ float red[128 * 16]; __shared__ float o16[16];
  for (int i = threadIdx.x; i < HQ * DH; i += 256) os[i] = o[i];
  __syncthreads();
  gemv16<HQ * DH>(os, wo, D, blockIdx.x * 16, red, o16);
  if (threadIdx.x < 16) { const int n = blockIdx.x * 16 + threadIdx.x; x1[n] = rbf(bf2f(x[n]) + rbf(mod[2 * D + n] * rbf(o16[threadIdx.x]))); }
}

__global__ void __launch_bounds__(256) k_ffn1(const float* __restrict__ x1, const float* __restrict__ mod, const bf16_t* __restrict__ wg, const bf16_t* __restrict__ wu, float* __restrict__ act) {
  __shared__ float xs[D]; __shared__ float hs[D]; __shared__ float red[128 * 16]; __shared__ float g16[16]; __shared__ float u16[16];
  for (int k = threadIdx.x; k < D; k += 256) xs[k] = x1[k];
  __syncthreads();
  ln_mod(xs, mod + 3 * D, mod + 4 * D, hs, red);
  gemv16<D>(hs, wg, F, blockIdx.x * 16, red, g16);
  gemv16<D>(hs, wu, F, blockIdx.x * 16, red, u16);
  if (threadIdx.x < 16) act[blockIdx.x * 16 + threadIdx.x] = rbf(rbf(silu(rbf(g16[threadIdx.x]))) * rbf(u16[threadIdx.x]));
}

__global__ void __launch_bounds__(256) k_ffn2(const float* __restrict__ act, const bf16_t* __restrict__ wd, const float* __restrict__ x1, const float* __restrict__ mod, bf16_t* __restrict__ out) {
  __shared__ float as[F]; __shared__ float red[128 * 16]; __shared__ float o16[16];
  for (int k = threadIdx.x; k < F; k += 256) as[k] = act[k];
  __syncthreads();
  gemv16<F>(as, wd, D, blockIdx.x * 16, red, o16);
  if (threadIdx.x < 16) { const int n = blockIdx.x * 16 + threadIdx.x; out[n] = f2bf(x1[n] + rbf(mod[5 * D + n] * rbf(o16[threadIdx.x]))); }
}

extern "C" void kernel_launch(void* const* d_in, const int* in_sizes, int n_in, void* d_out, int out_size, void* d_ws, size_t ws_size, hipStream_t stream) {
  const bf16_t* x = (const bf16_t*)d_in[0];
  const bf16_t* kc = (const bf16_t*)d_in[1];
  const bf16_t* vc = (const bf16_t*)d_in[2];
  const bf16_t* wq = (const bf16_t*)d_in[3];
  const bf16_t* wo = (const bf16_t*)d_in[4];
  const bf16_t* wg = (const bf16_t*)d_in[6];
  const bf16_t* wu = (const bf16_t*)d_in[7];
  const bf16_t* wd = (const bf16_t*)d_in[8];
  const bf16_t* c = (const bf16_t*)d_in[9];
  const bf16_t* wmod = (const bf16_t*)d_in[10];
  float* ws = (float*)d_ws;
  float* mod = ws;
  bf16_t* q = (bf16_t*)(ws + 4096);
  float* part = ws + 8192;
  float* x1 = ws + 8192 + 256 * 132;
  float* act = x1 + 1024;
  float* o = act + 2048;
  k_mod<<<NMOD / 16, 256, 0, stream>>>(c, wmod, mod);
  k_q<<<HQ * DH / 16, 256, 0, stream>>>(x, mod, wq, q);
  k_attn<<<256, NW * 64, 8 * 132 * 4, stream>>>(q, kc, vc, part);
  k_comb<<<HQ, 128, 0, stream>>>(part, o);
  k_wo<<<D / 16, 256, 0, stream>>>(o, wo, x, mod, x1);
  k_ffn1<<<F / 16, 256, 0, stream>>>(x1, mod, wg, wu, act);
  k_ffn2<<<D / 16, 256, 0, stream>>>(act, wd, x1, mod, (bf16_t*)d_out);
}
```

```cpp
#include <hip/hip_runtime.h>
#include <hip/hip_bf16.h>
#include <stdint.h>

#define TD 6
#define PACE 0
#define PACE0 0
#define KVDELAY 0
#define WOLEAD 1
#define GATE 1
#define STAG 12
#define QDELAY 64
#define STAMPS 0
#define NT_Q 54
#define NT_M 64
#define NT_F 42
#define NT_P ((512 - 2 * NT_Q - 2 * NT_M - NT_F) / 3)
static_assert(2 * NT_Q + 2 * NT_M + NT_F + 3 * NT_P == 512, "tiles of a head");
#define NT_G0 54
#define NT_G1 60
#define NT_G2 ((512 - 2 * NT_G0 - 2 * NT_G1) / 4)
static_assert(2 * NT_G0 + 2 * NT_G1 + 4 * NT_G2 == 512, "tiles of a head");
#define NT_H0 52
#define NT_H1 ((512 - 2 * NT_H0) / 6)
static_assert(2 * NT_H0 + 6 * NT_H1 == 512, "tiles of a head");
static_assert(NT_F >= 36, "every stream wave takes at least 9 tiles (the catch-up schedule)");

typedef unsigned short bf16_t;
typedef unsigned long long u64;
typedef unsigned v4u __attribute__((ext_vector_type(4)));
typedef float f32x4 __attribute__((ext_vector_type(4)));
typedef float f32x2 __attribute__((ext_vector_type(2)));
typedef unsigned v2u __attribute__((ext_vector_type(2)));
typedef __bf16 bf16x2 __attribute__((ext_vector_type(2)));
typedef __bf16 bf16x8 __attribute__((ext_vector_type(8)));
typedef short v4s __attribute__((ext_vector_type(4)));
#define GAS __attribute__((address_space(1)))
#define LAS __attribute__((address_space(3)))
typedef GAS u64 gu64;
typedef GAS unsigned gu32;

constexpr int D = 640, HQ = 32, SK = 8192, DH = 128, F = 1728, NMOD = 6 * D;
constexpr int NCU = 256, NTHR = 512, NFFN = 27;
constexpr float LOG2E = 1.4426950408889634f;

constexpr size_t WS_EPOCH = 0;
constexpr size_t WS_XG = 4096;
constexpr size_t WS_MG = 8192;
constexpr size_t WS_QG = 32768;
constexpr size_t WS_YG = 65536;
constexpr size_t WS_DG = 262144;
constexpr size_t WS_PG = 524288;
constexpr int PG_STRIDE = 160;
constexpr size_t WS_STAMP = 64u << 20;

constexpr int L_FLAG_Q = 0, L_CNT_S1 = 1, L_FLAG_S1 = 2, L_FLAG_E = 3, L_E = 4, L_CNT_M = 5, L_FLAG_H = 6, L_CNT_G = 7, L_CNT_W = 8, L_FLAG_L = 9, L_CNT_OV = 10;
constexpr int L_Q = 32;
constexpr int L_REC = 96;
constexpr int L_O = 624;
constexpr int L_MR = 704;
constexpr int L_SC = 960;
constexpr int L_H2 = 1600;
constexpr int L_ST = 1920;
constexpr int L_RGU = 1936;
constexpr int L_ACT = 2448;
constexpr int L_YR = 2576;
constexpr int L_GA = 2704, L_GB = 2736, L_X1 = 2768;
constexpr int L_STAMP = 2816;
constexpr int B_WO = 12288;
constexpr int B_STG = 32768;
constexpr int B_WD = 49152;
constexpr int LDS_BYTES = 131072;

constexpr int CODE_BYTES = 30 * 1024;
constexpr unsigned SPIN_G = 1u << 15;
constexpr unsigned SPIN_L = 1u << 20;

struct Params {
  const bf16_t* x; const bf16_t* kc; const bf16_t* vc; const bf16_t* wq; const bf16_t* wo;
  const bf16_t* wg; const bf16_t* wu; const bf16_t* wd; const bf16_t* c; const bf16_t* wmod;
  bf16_t* out; unsigned char* ws;
};

__device__ __forceinline__ float bf2f(unsigned b) { return __uint_as_float(b << 16); }
__device__ __forceinline__ float bfhi(unsigned b) { return __uint_as_float(b & 0xffff0000u); }
__device__ __forceinline__ float bflo(unsigned b) { return __uint_as_float(b << 16); }
__device__ __forceinline__ unsigned f2bf(float f) { __bf16 b = (__bf16)f; return (unsigned)__builtin_bit_cast(unsigned short, b); }
__device__ __forceinline__ float rbf(float f) { return bf2f(f2bf(f)); }
__device__ __forceinline__ unsigned pk2(float lo, float hi) { return __builtin_bit_cast(unsigned, __builtin_convertvector(f32x2{lo, hi}, bf16x2)); }
__device__ __forceinline__ float silu(float x) { return x * __builtin_amdgcn_rcpf(1.f + __builtin_amdgcn_exp2f(-x * LOG2E)); }
__device__ __forceinline__ float dot2(unsigned a, unsigned b, float acc) {
  return __builtin_amdgcn_fdot2_f32_bf16(__builtin_bit_cast(bf16x2, a), __builtin_bit_cast(bf16x2, b), acc, false);
}
template <int CTRL> __device__ __forceinline__ float dpp(float x) {
  return __builtin_bit_cast(float, __builtin_amdgcn_mov_dpp(__builtin_bit_cast(int, x), CTRL, 0xf, 0xf, true));
}
template <int CTRL> __device__ __forceinline__ unsigned dppu(unsigned x) { return (unsigned)__builtin_amdgcn_mov_dpp((int)x, CTRL, 0xf, 0xf, true); }
constexpr int XOR1 = 0xB1, XOR2 = 0x4E, XOR7 = 0x141, XOR8 = 0x128, ROR4 = 0x124, BCAST0 = 0x150;
__device__ __forceinline__ float swap32_sum(float a, float b) {
  auto t = __builtin_amdgcn_permlane32_swap(__float_as_uint(a), __float_as_uint(b), false, false);
  return __uint_as_float(t[0]) + __uint_as_float(t[1]);
}
__device__ __forceinline__ float swap16_sum(float a, float b) {
  auto t = __builtin_amdgcn_permlane16_swap(__float_as_uint(a), __float_as_uint(b), false, false);
  return __uint_as_float(t[0]) + __uint_as_float(t[1]);
}
__device__ __forceinline__ float xrow_max(float x) {
  auto s = __builtin_amdgcn_permlane16_swap(__float_as_uint(x), __float_as_uint(x), false, false);
  x = fmaxf(__uint_as_float(s[0]), __uint_as_float(s[1]));
  auto t = __builtin_amdgcn_permlane32_swap(__float_as_uint(x), __float_as_uint(x), false, false);
  return fmaxf(__uint_as_float(t[0]), __uint_as_float(t[1]));
}
__device__ __forceinline__ float xrow_sum(float x) { x = swap16_sum(x, x); return swap32_sum(x, x); }
__device__ __forceinline__ float wave_sum(float x) {
  x += dpp<XOR1>(x); x += dpp<XOR2>(x); x += dpp<XOR7>(x); x += dpp<XOR8>(x);
  return xrow_sum(x);
}
__device__ __forceinline__ void reduce_rows(const float* x, float& e0, float& e1) {
  const float y0 = swap32_sum(x[0], x[4]), y1 = swap32_sum(x[1], x[5]), y2 = swap32_sum(x[2], x[6]), y3 = swap32_sum(x[3], x[7]);
  e0 = swap16_sum(y0, y2); e1 = swap16_sum(y1, y3);
}
__device__ __forceinline__ float reduce_oct(const float* x, int lane) {
  float e0, e1; reduce_rows(x, e0, e1);
  const bool b3 = (lane & 8) != 0;
  return (b3 ? e1 : e0) + dpp<XOR8>(b3 ? e0 : e1);
}

__device__ __forceinline__ u64 ldg(const gu64* p) { return __hip_atomic_load(p, __ATOMIC_RELAXED, __HIP_MEMORY_SCOPE_AGENT); }
__device__ __forceinline__ void stg(gu64* p, u64 v) { __hip_atomic_store(p, v, __ATOMIC_RELAXED, __HIP_MEMORY_SCOPE_AGENT); }
constexpr unsigned TAG8 = 0x6Bu;
__device__ __forceinline__ u64 gr_pair(unsigned tag, unsigned pay) { return ((u64)pay << 32) | tag; }
__device__ __forceinline__ u64 gr_f24(unsigned tag, float v) { return ((u64)(((__float_as_uint(v) + 0x80u) & 0xffffff00u) | TAG8) << 32) | tag; }
__device__ __forceinline__ bool ok_pair(u64 g, unsigned tag) { return (unsigned)g == tag; }
__device__ __forceinline__ bool ok_f24(u64 g, unsigned tag) { return ((unsigned)g == tag) & (((unsigned)(g >> 32) & 0xffu) == TAG8); }
__device__ __forceinline__ float val_f24(u64 g) { return __uint_as_float((unsigned)(g >> 32) & 0xffffff00u); }
constexpr unsigned TAG4 = 0xBu;
__device__ __forceinline__ u64 gr_f28(unsigned tag, float v) { return ((u64)(((__float_as_uint(v) + 0x8u) & 0xfffffff0u) | TAG4) << 32) | tag; }
__device__ __forceinline__ bool ok_f28(u64 g, unsigned tag) { return ((unsigned)g == tag) & (((unsigned)(g >> 32) & 0xfu) == TAG4); }
__device__ __forceinline__ float val_f28(u64 g) { return __uint_as_float((unsigned)(g >> 32) & 0xfffffff0u); }
__device__ __forceinline__ bool all_ok(bool ok) { return __builtin_amdgcn_ballot_w64(!ok) == 0ull; }

__device__ __forceinline__ void lds_wait(volatile LAS unsigned* w, unsigned want) {
#define LSLEEP 1
  for (unsigned n = 0; *w != want && n < SPIN_L; ++n) __builtin_amdgcn_s_sleep(LSLEEP);
  asm volatile("" ::: "memory");
}
#define WAIT_LGKM() asm volatile("s_waitcnt lgkmcnt(0)" ::: "memory")
#define BARRIER() do { asm volatile("s_waitcnt lgkmcnt(0)" ::: "memory"); __builtin_amdgcn_s_barrier(); asm volatile("" ::: "memory"); } while (0)

#define STAMP(i) do { } while (0)
#define STAMP_DUMP() do { } while (0)

struct AttnState { float m, l; float acc[8]; };

__device__ __forceinline__ void attn_tile(const v4u (&K)[4], const v4u (&V)[4], const v4u& q, AttnState& st, int c) {
  float x[4];
#pragma unroll
  for (int i = 0; i < 4; ++i) {
    float d = dot2(K[i].x, q.x, 0.f);
    d = dot2(K[i].y, q.y, d); d = dot2(K[i].z, q.z, d); d = dot2(K[i].w, q.w, d);
    x[i] = d;
  }
  const bool b3 = (c & 8) != 0, b2 = (c & 4) != 0;
  const float k0 = b3 ? x[2] : x[0], s0 = b3 ? x[0] : x[2];
  const float k1 = b3 ? x[3] : x[1], s1 = b3 ? x[1] : x[3];
  const float y0 = k0 + dpp<XOR8>(s0), y1 = k1 + dpp<XOR8>(s1);
  const float k2 = b2 ? y1 : y0, s2 = b2 ? y0 : y1;
  float z = k2 + dpp<XOR7>(s2);
  z += dpp<XOR2>(z); z += dpp<XOR1>(z);
  const float s = rbf(rbf(z) * 0.08838834764831845f);
  float pm = fmaxf(s, dpp<ROR4>(s)); pm = fmaxf(pm, dpp<XOR8>(pm));
  const float mn = fmaxf(st.m, pm);
  if (__builtin_amdgcn_ballot_w64(mn > st.m) != 0ull) {
    const float al = __builtin_amdgcn_exp2f((st.m - mn) * LOG2E);
    st.l *= al;
#pragma unroll
    for (int j = 0; j < 8; ++j) st.acc[j] *= al;
    st.m = mn;
  }
  const float pr = __builtin_amdgcn_exp2f((s - st.m) * LOG2E);
  st.l += pr;
  const unsigned plo = f2bf(pr), phi = plo << 16;
#define PV(i) { const unsigned a = dppu<BCAST0 + 4 * i>(plo), b = dppu<BCAST0 + 4 * i>(phi); \
    st.acc[0] = dot2(V[i].x, a, st.acc[0]); st.acc[1] = dot2(V[i].x, b, st.acc[1]); st.acc[2] = dot2(V[i].y, a, st.acc[2]); st.acc[3] = dot2(V[i].y, b, st.acc[3]); \
    st.acc[4] = dot2(V[i].z, a, st.acc[4]); st.acc[5] = dot2(V[i].z, b, st.acc[5]); st.acc[6] = dot2(V[i].w, a, st.acc[6]); st.acc[7] = dot2(V[i].w, b, st.acc[7]); }
  PV(0) PV(1) PV(2) PV(3)
#undef PV
}

__device__ __forceinline__ void interleave(const v4u& r0, const v4u& r1, unsigned* o) {
#pragma unroll
  for (int j = 0; j < 4; ++j) { o[2 * j] = __builtin_amdgcn_perm(r1[j], r0[j], 0x05040100u); o[2 * j + 1] = __builtin_amdgcn_perm(r1[j], r0[j], 0x07060302u); }
}
__device__ __forceinline__ f32x4 mfma(const v4u& a, const v4u& b, const f32x4& c) {
  return __builtin_amdgcn_mfma_f32_16x16x32_bf16(__builtin_bit_cast(bf16x8, a), __builtin_bit_cast(bf16x8, b), c, 0, 0, 0);
}
__device__ __forceinline__ v4u tr_frag(const LAS unsigned char* p, int stride4) {
  const v2u lo = __builtin_bit_cast(v2u, __builtin_amdgcn_ds_read_tr16_b64_v4i16((LAS v4s*)p));
  const v2u hi = __builtin_bit_cast(v2u, __builtin_amdgcn_ds_read_tr16_b64_v4i16((LAS v4s*)(p + stride4)));
  return v4u{lo.x, lo.y, hi.x, hi.y};
}
#define GLDS(gsrc, ldst, aux) __builtin_amdgcn_global_load_lds((const GAS void*)(gsrc), (LAS void*)(ldst), 16, 0, aux)
__device__ __forceinline__ float sel4(int q4, float a, float b, float c, float d) { return q4 == 0 ? a : q4 == 1 ? b : q4 == 2 ? c : d; }
__device__ __forceinline__ v4u ldnt(const bf16_t* p) { return __builtin_nontemporal_load((const GAS v4u*)p); }
#define PIN8(a) asm volatile("" : "+v"((a)[0]), "+v"((a)[1]), "+v"((a)[2]), "+v"((a)[3]), "+v"((a)[4]), "+v"((a)[5]), "+v"((a)[6]), "+v"((a)[7]))

__global__ void __launch_bounds__(NTHR, 2) adaln_block_kernel(Params p) {
  extern __shared__ __attribute__((aligned(16))) unsigned lds_raw[];
  LAS unsigned* L = (LAS unsigned*)lds_raw;
  volatile LAS unsigned* LV = (volatile LAS unsigned*)lds_raw;
  LAS float* LF = (LAS float*)lds_raw;
  LAS unsigned char* LB = (LAS unsigned char*)lds_raw;
  u64 code_base;
  asm volatile("s_getpc_b64 %0" : "=s"(code_base));
  code_base &= ~255ull;
  const int tid = threadIdx.x, lane = tid & 63, wave = __builtin_amdgcn_readfirstlane(tid >> 6);
  const int r = lane >> 4, c = lane & 15;
  const int b = blockIdx.x, X = b & 7, J = b >> 3, sp = J & 7;
  const int head = 4 * X + (J >> 3);
  const bool fh = head < NFFN;
  const bool is_qc = sp < 2, is_ffn = fh && sp == 5;
  const int mu = (sp == 2 || sp == 3) ? 2 * head + sp - 2 : 999;
  const int own = head;
  const bool is_mod = mu < 60, is_own = sp == 4 && head < 20;
  const int cf = fh, cg = !fh && head < 30, ch_ = head >= 30;
  const int nq_ = cf * NT_Q + cg * NT_G0 + ch_ * NT_H0, nm_ = cf * NT_M + cg * NT_G1 + ch_ * NT_H1;
  const int np_ = cf * NT_P + cg * NT_G2 + ch_ * NT_H1, nf_ = cf * NT_F + cg * NT_G2 + ch_ * NT_H1;
  const int t0 = min(sp, 2) * nq_ + min(max(sp - 2, 0), 2) * nm_ + (int)(sp > 4) * np_ + (int)(sp > 5) * nf_ + (int)(sp > 6) * np_;
  const int nt = (int)(sp < 2) * nq_ + (int)(sp == 2 || sp == 3) * nm_ + (int)(sp == 5) * nf_ + (int)(sp == 4 || sp > 5) * np_;
  if (tid < 16) L[tid] = 0u;
  asm volatile("" :: "s"(p.kc), "s"(p.vc), "s"(p.ws), "s"(p.wq), "s"(p.wmod), "s"(p.c), "s"(p.x), "s"(t0), "s"(nt));
  BARRIER();
  gu64* const XG = (gu64*)(p.ws + WS_XG); gu64* const MG = (gu64*)(p.ws + WS_MG); gu64* const YG = (gu64*)(p.ws + WS_YG);
  gu64* const DG = (gu64*)(p.ws + WS_DG); gu64* const QG = (gu64*)(p.ws + WS_QG); gu64* const PG = (gu64*)(p.ws + WS_PG);

  if (__builtin_expect(wave < 4, 1)) {
    const size_t base = ((size_t)head * SK + (size_t)t0 * 16) * DH;
    const __amdgpu_buffer_rsrc_t rk = __builtin_amdgcn_make_buffer_rsrc((void*)(p.kc + base), 0, nt * 4096, 0x00020000);
    const __amdgpu_buffer_rsrc_t rv = __builtin_amdgcn_make_buffer_rsrc((void*)(p.vc + base), 0, nt * 4096, 0x00020000);
    const int voff = lane * 16;
    const int nw = (nt - wave + 3) >> 2;
    v4u K[TD][4], V[TD][4];
#define ISSUE(d, t) do { const int so_ = (wave + 4 * (t)) * 4096; \
    _Pragma("unroll") for (int i_ = 0; i_ < 4; ++i_) K[d][i_] = __builtin_amdgcn_raw_buffer_load_b128(rk, voff + 1024 * i_, so_, 2); \
    _Pragma("unroll") for (int i_ = 0; i_ < 4; ++i_) V[d][i_] = __builtin_amdgcn_raw_buffer_load_b128(rv, voff + 1024 * i_, so_, 2); \
    __builtin_amdgcn_sched_barrier(0); } while (0)
#define PACE_WAIT(n) do { asm volatile("s_waitcnt vmcnt(%0)" :: "n"(n) : "memory"); __builtin_amdgcn_sched_barrier(0); } while (0)
#define QGATE 1
    if (QGATE && is_qc) lds_wait(LV + L_FLAG_H, 1u);
    if (is_mod) lds_wait(LV + L_CNT_G, 1u);
    if (KVDELAY) __builtin_amdgcn_s_sleep(KVDELAY);
    if (wave == 0) STAMP(24);
    ISSUE(0, 0);
#pragma unroll
    for (int d = 1; d < TD; ++d) { PACE_WAIT(PACE0); ISSUE(d, d); }
    const unsigned ov = (unsigned)(size_t)(LB + B_WD + wave * 16384);
#define DMA4(m0v, rsrc, so) do { unsigned keep_; asm volatile("s_mov_b32 %0, m0\n\ts_mov_b32 m0, %1\n\ts_nop 0\n\t" \
      "buffer_load_dwordx4 %2, %3, %4 offen nt lds\n\tbuffer_load_dwordx4 %2, %3, %4 offen offset:1024 nt lds\n\t" \
      "buffer_load_dwordx4 %2, %3, %4 offen offset:2048 nt lds\n\tbuffer_load_dwordx4 %2, %3, %4 offen offset:3072 nt lds\n\t" \
      "s_mov_b32 m0, %0" : "=&s"(keep_) : "s"(m0v), "v"(voff), "s"(rsrc), "s"(so) : "memory"); } while (0)
#define DMA_TILE(e, t) do { const int so_ = (wave + 4 * (t)) * 4096; DMA4(ov + (e) * 8192, rk, so_); DMA4(ov + (e) * 8192 + 4096, rv, so_); __builtin_amdgcn_sched_barrier(0); } while (0)
#define LD_OV(d, e) do { const LAS v4u* src_ = (const LAS v4u*)(LB + B_WD + wave * 16384 + (e) * 8192) + lane; \
      _Pragma("unroll") for (int i_ = 0; i_ < 4; ++i_) { K[d][i_] = src_[64 * i_]; V[d][i_] = src_[256 + 64 * i_]; } __builtin_amdgcn_sched_barrier(0); } while (0)
    PACE_WAIT(PACE0); DMA_TILE(0, 6);
    PACE_WAIT(PACE0); DMA_TILE(1, 7);
    lds_wait(LV + L_FLAG_Q, 1u);
    if (wave == 0) STAMP(1);
    const v4u q = *(const LAS v4u*)(L + L_Q + 4 * c);
    const unsigned tag = L[L_E] + 1u;
    AttnState st; st.m = -INFINITY; st.l = 0.f;
#pragma unroll
    for (int j = 0; j < 8; ++j) st.acc[j] = 0.f;
#define TILE(d) do { attn_tile(K[d], V[d], q, st, c); __builtin_amdgcn_sched_barrier(0); } while (0)
    static_assert(TD == 6, "the catch-up schedule is written for six slots");
    TILE(0); PACE_WAIT(0); ISSUE(0, 8);
    TILE(1); TILE(2); TILE(3); TILE(4);
    LD_OV(4, 0);
    PACE_WAIT(PACE); ISSUE(1, 9);
    TILE(5);
    LD_OV(5, 1);
    WAIT_LGKM();
    if (lane == 0) __hip_atomic_fetch_add((LAS unsigned*)(L + L_CNT_OV), 1u, __ATOMIC_RELAXED, __HIP_MEMORY_SCOPE_WORKGROUP);
    TILE(4); TILE(5);
    TILE(0);
#pragma nounroll
    for (int u = 9; u < nw; u += TD) {
#pragma unroll
      for (int k = 0; k < TD; ++k) {
        if (u + k < nw) {
          PACE_WAIT(PACE);
          ISSUE((k + 2) % TD, u + k + 1);
          if (u + k + 1 + WOLEAD == nw && wave == 0 && lane == 0) LV[L_FLAG_L] = 1u;
          TILE((k + 1) % TD);
        }
      }
    }
#undef TILE
    if (wave == 0 && lane == 0) LV[L_FLAG_L] = 1u;
#undef ISSUE
    if (wave == 0) STAMP(2);
    const float M = xrow_max(st.m);
    const float sc = __builtin_amdgcn_exp2f((st.m - M) * LOG2E);
    float l = ((c & 3) == 0) ? st.l * sc : 0.f;
    l += dpp<ROR4>(l); l += dpp<XOR8>(l); l = xrow_sum(l);
#pragma unroll
    for (int j = 0; j < 8; ++j) st.acc[j] *= sc;
    float e0, e1; reduce_rows(st.acc, e0, e1);
    LAS float* rec = LF + L_REC + wave * 132;
    *(LAS f32x2*)(rec + 4 + 8 * c + 2 * r) = f32x2{e0, e1};
    if (lane == 0) { rec[0] = M; rec[1] = l; }
    WAIT_LGKM();
    unsigned old = 0;
    if (lane == 0) old = __hip_atomic_fetch_add((LAS unsigned*)(L + L_CNT_S1), 1u, __ATOMIC_RELAXED, __HIP_MEMORY_SCOPE_WORKGROUP);
    old = __builtin_amdgcn_readfirstlane(old);
    if (old == 3u) {
      asm volatile("" ::: "memory");
      const LAS float* R = LF + L_REC;
      const float m0 = R[0], m1 = R[132], m2 = R[264], m3 = R[396];
      const float MM = fmaxf(fmaxf(m0, m1), fmaxf(m2, m3));
      const float w0 = __builtin_amdgcn_exp2f((m0 - MM) * LOG2E), w1 = __builtin_amdgcn_exp2f((m1 - MM) * LOG2E);
      const float w2 = __builtin_amdgcn_exp2f((m2 - MM) * LOG2E), w3 = __builtin_amdgcn_exp2f((m3 - MM) * LOG2E);
      const float LL = (w0 * R[1] + w1 * R[133]) + (w2 * R[265] + w3 * R[397]);
      const float oa = (w0 * R[4 + lane] + w1 * R[136 + lane]) + (w2 * R[268 + lane] + w3 * R[400 + lane]);
      const float ob = (w0 * R[68 + lane] + w1 * R[200 + lane]) + (w2 * R[332 + lane] + w3 * R[464 + lane]);
      gu64* rp = PG + (size_t)(head * 8 + sp) * PG_STRIDE;
      stg(rp + lane, gr_f24(tag, oa));
      stg(rp + 64 + lane, gr_f24(tag, ob));
      if (lane < 2) stg(rp + 128 + lane, gr_f24(tag, lane == 0 ? MM : LL));
      if (lane == 0) LV[L_FLAG_S1] = 1u;
      STAMP(3);
    }
    return;
  }

  const int v = wave - 4, t = tid - 256;
  const int r8 = lane >> 3, c8 = lane & 7;
  unsigned tag = 0;
  if (is_qc) {
    const unsigned e_in = __hip_atomic_load((gu32*)(p.ws + WS_EPOCH), __ATOMIC_RELAXED, __HIP_MEMORY_SCOPE_AGENT);
    v4u w[20];
    {
      const bf16_t* src = p.wq + (size_t)(160 * v + 2 * r8) * (HQ * DH) + head * DH + 64 * sp + 8 * c8;
#pragma unroll
      for (int i = 0; i < 10; ++i) { w[2 * i] = ldnt(src + (size_t)(16 * i) * (HQ * DH)); w[2 * i + 1] = ldnt(src + (size_t)(16 * i + 1) * (HQ * DH)); }
    }
    unsigned xs[5];
#pragma unroll
    for (int i = 0; i < 5; ++i) xs[i] = ((const GAS unsigned*)p.x)[lane + 64 * i];
    const unsigned xo0 = ((const GAS unsigned*)p.x)[80 * v + lane], xo1 = ((const GAS unsigned*)p.x)[80 * v + 64 + (lane & 15)];
    __builtin_amdgcn_sched_barrier(0);
    if (v == 0 && lane == 0) { LV[L_E] = e_in; WAIT_LGKM(); LV[L_FLAG_E] = 1u; }
    float n0a, n0b, n1a, n1b;
    {
      float s = 0.f;
#pragma unroll
      for (int i = 0; i < 5; ++i) s += bflo(xs[i]) + bfhi(xs[i]);
      const float mean_f = wave_sum(s) * (1.f / D);
      float vs = 0.f;
#pragma unroll
      for (int i = 0; i < 5; ++i) { const float d0 = bflo(xs[i]) - mean_f, d1 = bfhi(xs[i]) - mean_f; vs = fmaf(d0, d0, vs); vs = fmaf(d1, d1, vs); }
      const float var = rbf(wave_sum(vs) * (1.f / D)), mean = rbf(mean_f);
      const float rs = __builtin_amdgcn_rcpf(rbf(__builtin_amdgcn_sqrtf(rbf(var + 1e-5f))));
      n0a = rbf(rbf(bflo(xo0) - mean) * rs); n0b = rbf(rbf(bfhi(xo0) - mean) * rs);
      n1a = rbf(rbf(bflo(xo1) - mean) * rs); n1b = rbf(rbf(bfhi(xo1) - mean) * rs);
      asm volatile("" : "+v"(n0a), "+v"(n0b), "+v"(n1a), "+v"(n1b));
    }
    unsigned wi[10][8];
#pragma unroll
    for (int i = 0; i < 10; ++i) { interleave(w[2 * i], w[2 * i + 1], wi[i]); PIN8(wi[i]); }
    lds_wait(LV + L_FLAG_E, 1u);
    tag = LV[L_E] + 1u;
    if (v == 0) STAMP(18);
    unsigned s0, c0, s1, c1;
    {
      const gu64* m0 = MG + 80 * v + lane; const gu64* m1 = MG + 80 * v + 64 + (lane & 15);
      for (unsigned n = 0; n < SPIN_G; ++n) {
        const u64 a0 = ldg(m0), b0 = ldg(m0 + 320), a1 = ldg(m1), b1 = ldg(m1 + 320);
        s0 = (unsigned)(a0 >> 32); c0 = (unsigned)(b0 >> 32); s1 = (unsigned)(a1 >> 32); c1 = (unsigned)(b1 >> 32);
        if (all_ok(ok_pair(a0, tag) & ok_pair(b0, tag) & ok_pair(a1, tag) & ok_pair(b1, tag))) break;
      }
    }
    if (v == 0) STAMP(5);
    L[L_H2 + 80 * v + lane] = f2bf(rbf(n0a * rbf(1.f + bflo(c0))) + bflo(s0)) | (f2bf(rbf(n0b * rbf(1.f + bfhi(c0))) + bfhi(s0)) << 16);
    if (lane < 16) L[L_H2 + 80 * v + 64 + lane] = f2bf(rbf(n1a * rbf(1.f + bflo(c1))) + bflo(s1)) | (f2bf(rbf(n1b * rbf(1.f + bfhi(c1))) + bfhi(s1)) << 16);
    WAIT_LGKM();
    float acc[8];
#pragma unroll
    for (int j = 0; j < 8; ++j) acc[j] = 0.f;
#pragma unroll
    for (int i = 0; i < 10; ++i) {
      const unsigned hp = L[L_H2 + 80 * v + 8 * i + r8];
#pragma unroll
      for (int j = 0; j < 8; ++j) acc[j] = dot2(wi[i][j], hp, acc[j]);
    }
    LF[L_MR + 64 * v + 8 * c8 + r8] = reduce_oct(acc, lane);
    WAIT_LGKM();
    unsigned old = 0;
    if (lane == 0) old = __hip_atomic_fetch_add((LAS unsigned*)(L + L_CNT_M), 1u, __ATOMIC_RELAXED, __HIP_MEMORY_SCOPE_WORKGROUP);
    if (__builtin_amdgcn_readfirstlane(old) == 3u) {
      asm volatile("" ::: "memory");
      const unsigned qb = f2bf((LF[L_MR + lane] + LF[L_MR + 64 + lane]) + (LF[L_MR + 128 + lane] + LF[L_MR + 192 + lane]));
      const unsigned up = dppu<XOR1>(qb);
      if ((lane & 1) == 0) stg(QG + head * 64 + 32 * sp + (lane >> 1), gr_pair(tag, qb | (up << 16)));
      if (lane == 0) LV[L_FLAG_H] = 1u;
      STAMP(6);
    }
  } else {
    const unsigned e_in = __hip_atomic_load((gu32*)(p.ws + WS_EPOCH), __ATOMIC_RELAXED, __HIP_MEMORY_SCOPE_AGENT);
    const __amdgpu_buffer_rsrc_t rm = __builtin_amdgcn_make_buffer_rsrc((void*)p.wmod, 0, is_mod ? D * NMOD * 2 : 0, 0x00020000);
    const __amdgpu_buffer_rsrc_t rc = __builtin_amdgcn_make_buffer_rsrc((void*)p.c, 0, is_mod ? D * 2 : 0, 0x00020000);
    const unsigned cv0 = __builtin_amdgcn_raw_buffer_load_b16(rc, (160 * v + lane) * 2, 0, 0), cv1 = __builtin_amdgcn_raw_buffer_load_b16(rc, (160 * v + 64 + lane) * 2, 0, 0),
                   cv2 = __builtin_amdgcn_raw_buffer_load_b16(rc, (160 * v + 128 + (lane & 31)) * 2, 0, 0);
    v4u wm[20];
#pragma unroll
    for (int i = 0; i < 20; ++i) wm[i] = __builtin_amdgcn_raw_buffer_load_b128(rm, ((160 * v + r8) * NMOD + 64 * mu + 8 * c8) * 2, i * (8 * NMOD * 2), 2);
    __builtin_amdgcn_sched_barrier(0);
    if (v == 0 && lane == 0) { LV[L_E] = e_in; WAIT_LGKM(); LV[L_FLAG_E] = 1u; }
    if (is_mod) {
      L[L_SC + 160 * v + lane] = f2bf(silu(bf2f(cv0))); L[L_SC + 160 * v + 64 + lane] = f2bf(silu(bf2f(cv1)));
      if (lane < 32) L[L_SC + 160 * v + 128 + lane] = f2bf(silu(bf2f(cv2)));
      WAIT_LGKM();
      float acc[8];
#pragma unroll
      for (int j = 0; j < 8; ++j) acc[j] = 0.f;
#pragma unroll
      for (int i = 0; i < 20; ++i) {
        const unsigned a = L[L_SC + 160 * v + 8 * i + r8], bb = a << 16;
#pragma unroll
        for (int j = 0; j < 4; ++j) { acc[2 * j] = dot2(wm[i][j], a, acc[2 * j]); acc[2 * j + 1] = dot2(wm[i][j], bb, acc[2 * j + 1]); }
      }
      LF[L_MR + 64 * v + 8 * c8 + r8] = reduce_oct(acc, lane);
      WAIT_LGKM();
      unsigned old = 0;
      if (lane == 0) old = __hip_atomic_fetch_add((LAS unsigned*)(L + L_CNT_M), 1u, __ATOMIC_RELAXED, __HIP_MEMORY_SCOPE_WORKGROUP);
      if (__builtin_amdgcn_readfirstlane(old) == 3u) {
        asm volatile("" ::: "memory");
        const unsigned mb_ = f2bf((LF[L_MR + lane] + LF[L_MR + 64 + lane]) + (LF[L_MR + 128 + lane] + LF[L_MR + 192 + lane]));
        const unsigned up = dppu<XOR1>(mb_);
        lds_wait(LV + L_FLAG_E, 1u);
        if ((lane & 1) == 0) stg(MG + 32 * mu + (lane >> 1), gr_pair(LV[L_E] + 1u, mb_ | (up << 16)));
        if (lane == 0) LV[L_CNT_G] = 1u;
        STAMP(4);
      }
    }
    if (v == 3 && J * 1024 < CODE_BYTES) {
      const v4u cw = ((const GAS v4u*)(code_base + (u64)(J * 1024)))[lane]; asm volatile("" :: "v"(cw));
    }
  }
  if (v == 1) {
    lds_wait(LV + L_FLAG_E, 1u);
    tag = LV[L_E] + 1u;
    const gu64* qp = QG + head * 64 + lane;
    if (!is_qc) __builtin_amdgcn_s_sleep(QDELAY);
    u64 g0 = ldg(qp); __builtin_amdgcn_s_sleep(STAG);
    u64 g1 = ldg(qp); __builtin_amdgcn_s_sleep(STAG);
    u64 g2 = ldg(qp), g;
    for (unsigned n = 0;; ++n) {
      if (all_ok(ok_pair(g0, tag)) || n >= SPIN_G) { g = g0; break; }
      g0 = ldg(qp);
      if (all_ok(ok_pair(g1, tag))) { g = g1; break; }
      g1 = ldg(qp);
      if (all_ok(ok_pair(g2, tag))) { g = g2; break; }
      g2 = ldg(qp);
    }
    L[L_Q + lane] = (unsigned)(g >> 32);
    WAIT_LGKM();
    if (lane == 0) LV[L_FLAG_Q] = 1u;
    STAMP(7);
  }
  lds_wait(LV + L_FLAG_E, 1u);
  tag = LV[L_E] + 1u;

  const int g4 = lane >> 4, tq = (lane & 15) >> 2, tp = lane & 3;
  v4u wf[5][8];
  unsigned mbA[2], mbB[2];
  if (is_ffn) {
#define FPACE 63
    v4u raw[5][8];
    {
      const size_t ro = (size_t)(160 * v + r8) * F + 64 * head + 8 * c8;
#pragma unroll
      for (int kb = 0; kb < 5; ++kb) {
        if (FPACE < 63) asm volatile("s_waitcnt vmcnt(%0)" :: "n"(FPACE) : "memory");
#pragma unroll
        for (int i = 0; i < 4; ++i) { raw[kb][i] = ldnt(p.wg + ro + (size_t)(32 * kb + 8 * i) * F); raw[kb][4 + i] = ldnt(p.wu + ro + (size_t)(32 * kb + 8 * i) * F); }
        __builtin_amdgcn_sched_barrier(0);
      }
    }
    __builtin_amdgcn_sched_barrier(0);
    LAS unsigned char* stg_w = LB + B_STG + v * 4096 + r8 * 128 + c8 * 16;
    const LAS unsigned char* stg_r = LB + B_STG + v * 4096 + (8 * g4 + tq) * 128 + tp * 8;
#pragma unroll
    for (int kb = 0; kb < 5; ++kb)
#pragma unroll
      for (int m = 0; m < 2; ++m) {
#pragma unroll
        for (int i = 0; i < 4; ++i) *(LAS v4u*)(stg_w + i * 1024) = raw[kb][4 * m + i];
        asm volatile("" ::: "memory");
#pragma unroll
        for (int cg = 0; cg < 4; ++cg) wf[kb][4 * m + cg] = tr_frag(stg_r + cg * 32, 512);
        asm volatile("s_waitcnt lgkmcnt(0)" ::: "memory");
      }
    if (v == 0) STAMP(21);
    if (v == 1) STAMP(18);
    lds_wait(LV + L_CNT_OV, 4u);
    if (v == 0) STAMP(22);
#pragma unroll
    for (int i = 0; i < 20; ++i) {
      const int idx = 64 * i + lane, row = idx / 20, ch = idx - 20 * row;
      GLDS(p.wd + (size_t)(64 * head + row) * D + 160 * v + 8 * ch, LB + B_WD + v * 20480 + 1024 * i, 2);
    }
    {
      const gu64* m0 = MG + 80 * v + lane; const gu64* m1 = MG + 80 * v + 64 + (lane & 15);
      for (unsigned n = 0; n < SPIN_G; ++n) {
        const u64 s0 = ldg(m0 + 960), c0 = ldg(m0 + 1280), s1 = ldg(m1 + 960), c1 = ldg(m1 + 1280);
        const unsigned sp0 = (unsigned)(s0 >> 32), cp0 = (unsigned)(c0 >> 32), sp1 = (unsigned)(s1 >> 32), cp1 = (unsigned)(c1 >> 32);
        mbA[0] = (sp0 & 0xffffu) | (f2bf(1.f + bflo(cp0)) << 16); mbB[0] = (sp0 >> 16) | (f2bf(1.f + bfhi(cp0)) << 16);
        mbA[1] = (sp1 & 0xffffu) | (f2bf(1.f + bflo(cp1)) << 16); mbB[1] = (sp1 >> 16) | (f2bf(1.f + bfhi(cp1)) << 16);
        if (all_ok(ok_pair(s0, tag) & ok_pair(c0, tag) & ok_pair(s1, tag) & ok_pair(c1, tag))) break;
        __builtin_amdgcn_s_sleep(8);
      }
    }
  }
  if (is_own && v == 0) {
    const int j = lane & 31, part = lane >> 5;
    const gu64* m = MG + (part ? 1600 : 640) + 16 * own + (j >> 1);
    const float xo = bf2f(((const GAS bf16_t*)p.x)[32 * own + j]);
    unsigned g = 0;
    for (unsigned n = 0; n < SPIN_G; ++n) { const u64 a = ldg(m); g = (unsigned)(a >> 32); if (all_ok(ok_pair(a, tag))) break; __builtin_amdgcn_s_sleep(8); }
    const float gv = (j & 1) ? bfhi(g) : bflo(g);
    if (part == 0) { LF[L_GA + j] = gv; LF[L_X1 + j] = xo; } else LF[L_GB + j] = gv;
  }
  lds_wait(LV + L_FLAG_L, 1u);
#pragma unroll
  for (int i = 0; i < 5; ++i) {
    const int idx = 256 * i + t, row = idx / 10, ch = idx - 10 * row;
    GLDS(p.wo + (size_t)(head * DH + row) * D + 80 * sp + 8 * ch, LB + B_WO + (256 * i + 64 * v) * 16, 0);
  }
  asm volatile("s_waitcnt vmcnt(0)" ::: "memory");
  if (lane == 0) __hip_atomic_fetch_add((LAS unsigned*)(L + L_CNT_W), 1u, __ATOMIC_RELAXED, __HIP_MEMORY_SCOPE_WORKGROUP);
  lds_wait(LV + L_CNT_W, 4u);
  v4u wof[4], wof4[4];
  {
    const LAS unsigned char* src = LB + B_WO + (8 * g4 + tq) * 160 + tp * 8;
#pragma unroll
    for (int kb = 0; kb < 4; ++kb) { wof[kb] = tr_frag(src + kb * 32 * 160 + v * 32, 4 * 160); wof4[kb] = tr_frag(src + kb * 32 * 160 + 4 * 32, 4 * 160); }
  }
  if (v == 0) STAMP(8);

  lds_wait(LV + L_FLAG_S1, 1u);
  if (v == 0) STAMP(9);
  {
    const int hf = lane >> 5, d = 32 * v + (lane & 31);
    const gu64* rp = PG + (size_t)(head * 8) * PG_STRIDE;
    const gu64* op = rp + hf * PG_STRIDE + d;
    const gu64* mp = rp + (((lane & 15) >> 1) ^ hf) * PG_STRIDE + 128 + (lane & 1);
    float o[4], ml = 0.f;
    for (unsigned n = 0; n < SPIN_G; ++n) {
      bool ok = true;
#pragma unroll
      for (int j = 0; j < 4; ++j) { const u64 g = ldg(op + 2 * j * PG_STRIDE); o[j] = val_f24(g); ok &= ok_f24(g, tag); }
      const u64 g = ldg(mp); ml = val_f24(g); ok &= ok_f24(g, tag);
      if (all_ok(ok)) break;
    }
    const float mm = (lane & 1) ? -INFINITY : ml;
    float M = fmaxf(mm, dpp<XOR2>(mm)); M = fmaxf(M, dpp<XOR7>(M)); M = fmaxf(M, dpp<XOR8>(M)); M = fmaxf(M, dpp<XOR1>(M));
    const float wgt = __builtin_amdgcn_exp2f((mm - M) * LOG2E);
    const float wn = dpp<XOR1>(wgt);
    const float wl = (lane & 1) ? wn * ml : 0.f;
    float Ls = wl + dpp<XOR1>(wl); Ls += dpp<XOR2>(Ls); Ls += dpp<XOR7>(Ls); Ls += dpp<XOR8>(Ls);
    float acc = dpp<BCAST0>(wgt) * o[0];
    acc = fmaf(dpp<BCAST0 + 4>(wgt), o[1], acc); acc = fmaf(dpp<BCAST0 + 8>(wgt), o[2], acc); acc = fmaf(dpp<BCAST0 + 12>(wgt), o[3], acc);
    acc = swap32_sum(acc, acc);
    if (lane < 32) ((LAS bf16_t*)(L + L_O))[d] = (bf16_t)f2bf(acc * __builtin_amdgcn_rcpf(Ls));
  }
  BARRIER();
  if (v == 0) STAMP(10);
  {
    v4u A[4];
#pragma unroll
    for (int kb = 0; kb < 4; ++kb) A[kb] = *(const LAS v4u*)(L + L_O + 16 * kb + 4 * g4);
    f32x4 y = {0.f, 0.f, 0.f, 0.f}, y4 = {0.f, 0.f, 0.f, 0.f};
#pragma unroll
    for (int kb = 0; kb < 4; ++kb) y = mfma(A[kb], wof[kb], y);
    if (v == 0) {
#pragma unroll
      for (int kb = 0; kb < 4; ++kb) y4 = mfma(A[kb], wof4[kb], y4);
    }
    const int n = 80 * sp + (lane < 16 ? 16 * v : 64) + c;
    if (lane < 16 || (v == 0 && lane < 32)) stg(YG + (size_t)(n >> 5) * 1024 + head * 32 + (n & 31), gr_f24(tag, lane < 16 ? y[0] : y4[0]));
  }
  if (v == 0) STAMP(11);
  if (is_own) {
    const int hf = lane >> 5, col = lane & 31;
    float y[4];
    {
      const gu64* yp = YG + (size_t)own * 1024 + (8 * v + hf) * 32 + col;
      for (unsigned n = 0; n < SPIN_G; ++n) {
        bool ok = true;
#pragma unroll
        for (int j = 0; j < 4; ++j) { const u64 g = ldg(yp + 64 * j); y[j] = val_f24(g); ok &= ok_f24(g, tag); }
        if (all_ok(ok)) break;
      }
    }
    if (v == 0) STAMP(30);
    float s = (y[0] + y[1]) + (y[2] + y[3]);
    s = swap32_sum(s, s);
    if (lane < 32) LF[L_YR + 32 * v + col] = s;
    BARRIER();
    if (v == 0) STAMP(31);
    float x1 = 0.f;
    if (v == 0) {
      const float a = (LF[L_YR + col] + LF[L_YR + 32 + col]) + (LF[L_YR + 64 + col] + LF[L_YR + 96 + col]);
      x1 = rbf(LF[L_X1 + col] + rbf(LF[L_GA + col] * rbf(a)));
      const unsigned xb = f2bf(x1), pair = xb | (dppu<XOR1>(xb) << 16);
      if (lane < 32 && (lane & 1) == 0) stg(XG + 16 * own + (lane >> 1), gr_pair(tag, pair));
      STAMP(12);
    }
    {
      const gu64* dp = DG + (size_t)own * 1024 + (7 * v + hf) * 32 + col;
      for (unsigned n = 0; n < SPIN_G; ++n) {
        bool ok = true;
#pragma unroll
        for (int j = 0; j < 4; ++j) {
          const bool live = 2 * j + hf < 7 && 7 * v + 2 * j + hf < NFFN;
          const u64 g = ldg(dp + (live ? 64 * j : 0)); y[j] = live ? val_f24(g) : 0.f; ok &= ok_f24(g, tag);
        }
        if (all_ok(ok)) break;
        __builtin_amdgcn_s_sleep(1);
      }
    }
    if (v == 0) STAMP(15);
    s = (y[0] + y[1]) + (y[2] + y[3]);
    s = swap32_sum(s, s);
    if (lane < 32) LF[L_YR + 32 * v + col] = s;
    BARRIER();
    if (v == 0) {
      const float a = (LF[L_YR + col] + LF[L_YR + 32 + col]) + (LF[L_YR + 64 + col] + LF[L_YR + 96 + col]);
      if (lane < 32) ((GAS bf16_t*)p.out)[32 * own + col] = (bf16_t)f2bf(x1 + rbf(LF[L_GB + col] * rbf(a)));
      if (own == 0 && lane == 0) __hip_atomic_store((gu32*)(p.ws + WS_EPOCH), tag, __ATOMIC_RELAXED, __HIP_MEMORY_SCOPE_AGENT);
      STAMP(16);
    }
  }
  if (is_ffn) {
    unsigned xo0, xo1;
    for (unsigned n = 0; n < SPIN_G; ++n) {
      const u64 g0 = ldg(XG + 80 * v + lane), g1 = ldg(XG + 80 * v + 64 + (lane & 15));
      xo0 = (unsigned)(g0 >> 32); xo1 = (unsigned)(g1 >> 32);
      if (all_ok(ok_pair(g0, tag) & ok_pair(g1, tag))) break;
    }
    if (v == 0) STAMP(13);
    {
      const unsigned xm = lane < 16 ? xo1 : 0u;
      float s1 = dot2(xm, 0x3f803f80u, dot2(xo0, 0x3f803f80u, 0.f)), s2 = dot2(xm, xm, dot2(xo0, xo0, 0.f));
      s1 += dpp<XOR1>(s1); s2 += dpp<XOR1>(s2); s1 += dpp<XOR2>(s1); s2 += dpp<XOR2>(s2);
      s1 += dpp<XOR7>(s1); s2 += dpp<XOR7>(s2); s1 += dpp<XOR8>(s1); s2 += dpp<XOR8>(s2);
      s1 = xrow_sum(s1); s2 = xrow_sum(s2);
      if (lane == 0) *(LAS f32x2*)(LF + L_ST + 2 * v) = f32x2{s1, s2};
    }
    if (v == 0) STAMP(25);
    BARRIER();
    if (v == 0) STAMP(26);
    {
      const f32x4 sa = *(const LAS f32x4*)(LF + L_ST), sb = *(const LAS f32x4*)(LF + L_ST + 4);
      const float mean_f = ((sa[0] + sa[2]) + (sb[0] + sb[2])) * (1.f / D);
      const float ex2 = ((sa[1] + sa[3]) + (sb[1] + sb[3])) * (1.f / D);
      float var_f = fmaf(-mean_f, mean_f, ex2);
      if (__builtin_amdgcn_readfirstlane(__float_as_uint(var_f * 256.f < ex2 ? 1.f : 0.f)) != 0u) {
        float vs = 0.f;
#pragma unroll
        for (int i = 0; i < 5; ++i) { const unsigned xp = (unsigned)(ldg(XG + lane + 64 * i) >> 32); const float d0 = bflo(xp) - mean_f, d1 = bfhi(xp) - mean_f; vs = fmaf(d0, d0, vs); vs = fmaf(d1, d1, vs); }
        var_f = wave_sum(vs) * (1.f / D);
      }
      const float var = rbf(var_f), mean = rbf(mean_f);
      const float rs = __builtin_amdgcn_rcpf(rbf(__builtin_amdgcn_sqrtf(rbf(var + 1e-5f))));
#define LNPAIR(xp, ma, mb_) ({ const unsigned d_ = pk2(bflo(xp) - mean, bfhi(xp) - mean); const unsigned n_ = pk2(bflo(d_) * rs, bfhi(d_) * rs); \
        const unsigned m_ = pk2(bflo(n_) * bfhi(ma), bfhi(n_) * bfhi(mb_)); pk2(bflo(m_) + bflo(ma), bfhi(m_) + bflo(mb_)); })
      L[L_H2 + 80 * v + lane] = LNPAIR(xo0, mbA[0], mbB[0]);
      const unsigned h1 = LNPAIR(xo1, mbA[1], mbB[1]);
      if (lane < 16) L[L_H2 + 80 * v + 64 + lane] = h1;
#undef LNPAIR
    }
    if (v == 0) STAMP(27);
    v4u wd[2][10];
    {
      v4u A[5];
#pragma unroll
      for (int kb = 0; kb < 5; ++kb) A[kb] = *(const LAS v4u*)(L + L_H2 + 80 * v + 16 * kb + 4 * g4);
      f32x4 acc[8];
#pragma unroll
      for (int cg = 0; cg < 8; ++cg) acc[cg] = f32x4{0.f, 0.f, 0.f, 0.f};
      const LAS unsigned char* src = LB + B_WD + v * 20480 + (8 * g4 + tq) * 320 + tp * 8;
#pragma unroll
      for (int kb = 0; kb < 5; ++kb) {
#pragma unroll
        for (int cg = 0; cg < 8; ++cg) acc[cg] = mfma(A[kb], wf[kb][cg], acc[cg]);
        __builtin_amdgcn_sched_barrier(0);
        if (kb < 4) {
#pragma unroll
          for (int j = 0; j < 5; ++j) { const int f = 5 * kb + j; wd[f / 10][f % 10] = tr_frag(src + (f / 10) * 32 * 320 + (f % 10) * 32, 4 * 320); }
        }
        __builtin_amdgcn_sched_barrier(0);
      }
      LF[L_RGU + v * 128 + lane] = sel4(g4, acc[0][0], acc[1][0], acc[2][0], acc[3][0]);
      LF[L_RGU + v * 128 + 64 + lane] = sel4(g4, acc[4][0], acc[5][0], acc[6][0], acc[7][0]);
    }
    if (v == 0) STAMP(28);
    BARRIER();
    if (v == 0) STAMP(29);
    {
      const float G = rbf((LF[L_RGU + lane] + LF[L_RGU + 128 + lane]) + (LF[L_RGU + 256 + lane] + LF[L_RGU + 384 + lane]));
      const float U = rbf((LF[L_RGU + 64 + lane] + LF[L_RGU + 192 + lane]) + (LF[L_RGU + 320 + lane] + LF[L_RGU + 448 + lane]));
      ((LAS bf16_t*)(L + L_ACT + 32 * v))[lane] = (bf16_t)f2bf(rbf(silu(G)) * U);
      if (v == 0) STAMP(14);
      const v4u A0 = *(const LAS v4u*)(L + L_ACT + 32 * v + 4 * g4), A1 = *(const LAS v4u*)(L + L_ACT + 32 * v + 16 + 4 * g4);
      f32x4 acc[10];
#pragma unroll
      for (int cg = 0; cg < 10; ++cg) { acc[cg] = mfma(A0, wd[0][cg], f32x4{0.f, 0.f, 0.f, 0.f}); acc[cg] = mfma(A1, wd[1][cg], acc[cg]); }
      gu64* dp = DG + head * 32;
      const int n0 = 160 * v + 16 * g4 + c;
      stg(dp + (size_t)(n0 >> 5) * 1024 + (n0 & 31), gr_f24(tag, sel4(g4, acc[0][0], acc[1][0], acc[2][0], acc[3][0])));
      const int n1 = n0 + 64;
      stg(dp + (size_t)(n1 >> 5) * 1024 + (n1 & 31), gr_f24(tag, sel4(g4, acc[4][0], acc[5][0], acc[6][0], acc[7][0])));
      const int n2 = n0 + 128;
      if (lane < 32) stg(dp + (size_t)(n2 >> 5) * 1024 + (n2 & 31), gr_f24(tag, g4 == 0 ? acc[8][0] : acc[9][0]));
    }
    if (v == 0) STAMP(17);
  }
  if (v == 0) STAMP_DUMP();
}

extern "C" void kernel_launch(void* const* d_in, const int* in_sizes, int n_in, void* d_out, int out_size, void* d_ws, size_t ws_size, hipStream_t stream) {
  Params p;
  p.x = (const bf16_t*)d_in[0]; p.kc = (const bf16_t*)d_in[1]; p.vc = (const bf16_t*)d_in[2]; p.wq = (const bf16_t*)d_in[3]; p.wo = (const bf16_t*)d_in[4];
  p.wg = (const bf16_t*)d_in[6]; p.wu = (const bf16_t*)d_in[7]; p.wd = (const bf16_t*)d_in[8]; p.c = (const bf16_t*)d_in[9]; p.wmod = (const bf16_t*)d_in[10];
  p.out = (bf16_t*)d_out; p.ws = (unsigned char*)d_ws;
  (void)hipFuncSetAttribute((const void*)adaln_block_kernel, hipFuncAttributeMaxDynamicSharedMemorySize, LDS_BYTES);
  adaln_block_kernel<<<NCU, NTHR, LDS_BYTES, stream>>>(p);
}
```

```cpp
#include <hip/hip_runtime.h>
#include <hip/hip_bf16.h>
#include <stdint.h>

#define TD 6
#define PACE 0
#define PACE0 0
#define KVDELAY 0
#define PSLEEP 0
#define WOLEAD 1
#define GATE 1
#define STAG 12
#define QDELAY 64
#define STAMPS 0
#define NT_Q 54
#define NT_M 64
#define NT_F 42
#define NT_P ((512 - 2 * NT_Q - 2 * NT_M - NT_F) / 3)
static_assert(2 * NT_Q + 2 * NT_M + NT_F + 3 * NT_P == 512, "tiles of a head");
#define NT_G0 54
#define NT_G1 60
#define NT_G2 ((512 - 2 * NT_G0 - 2 * NT_G1) / 4)
static_assert(2 * NT_G0 + 2 * NT_G1 + 4 * NT_G2 == 512, "tiles of a head");
#define NT_H0 52
#define NT_H1 ((512 - 2 * NT_H0) / 6)
static_assert(2 * NT_H0 + 6 * NT_H1 == 512, "tiles of a head");
static_assert(NT_F >= 36, "every stream wave takes at least 9 tiles (the catch-up schedule)");

typedef unsigned short bf16_t;
typedef unsigned long long u64;
typedef unsigned v4u __attribute__((ext_vector_type(4)));
typedef float f32x4 __attribute__((ext_vector_type(4)));
typedef float f32x2 __attribute__((ext_vector_type(2)));
typedef unsigned v2u __attribute__((ext_vector_type(2)));
typedef __bf16 bf16x2 __attribute__((ext_vector_type(2)));
typedef __bf16 bf16x8 __attribute__((ext_vector_type(8)));
typedef short v4s __attribute__((ext_vector_type(4)));
#define GAS __attribute__((address_space(1)))
#define LAS __attribute__((address_space(3)))
typedef GAS u64 gu64;
typedef GAS unsigned gu32;

constexpr int D = 640, HQ = 32, SK = 8192, DH = 128, F = 1728, NMOD = 6 * D;
constexpr int NCU = 256, NTHR = 512, NFFN = 27;
constexpr float LOG2E = 1.4426950408889634f;

constexpr size_t WS_EPOCH = 0;
constexpr size_t WS_XG = 393216 + 32768;
constexpr size_t WS_MG = 8192;
constexpr size_t WS_QG = 32768;
constexpr size_t WS_YG = 65536;
constexpr size_t WS_DG = 262144;
constexpr size_t WS_PG = 524288;
constexpr int PG_STRIDE = 160;
constexpr size_t WS_STAMP = 64u << 20;

constexpr int L_FLAG_Q = 0, L_CNT_S1 = 1, L_FLAG_S1 = 2, L_FLAG_E = 3, L_E = 4, L_CNT_M = 5, L_FLAG_H = 6, L_CNT_G = 7, L_CNT_W = 8, L_FLAG_L = 9, L_CNT_OV = 10, L_CNT_Q = 11;
constexpr int L_Q = 32;
constexpr int L_REC = 96;
constexpr int L_O = 624;
constexpr int L_MR = 704;
constexpr int L_SC = 960;
constexpr int L_H2 = 1600;
constexpr int L_ST = 1920;
constexpr int L_RGU = 1936;
constexpr int L_ACT = 2448;
constexpr int L_STAMP = 2816;
constexpr int B_WO = 12288;
constexpr int B_STG = 32768;
constexpr int B_WD = 49152;
constexpr int LDS_BYTES = 131072;

constexpr int CODE_BYTES = 29 * 1024;
constexpr unsigned SPIN_G = 1u << 15;
constexpr unsigned SPIN_L = 1u << 20;

struct Params {
  const bf16_t* x; const bf16_t* kc; const bf16_t* vc; const bf16_t* wq; const bf16_t* wo;
  const bf16_t* wg; const bf16_t* wu; const bf16_t* wd; const bf16_t* c; const bf16_t* wmod;
  bf16_t* out; unsigned char* ws;
};

__device__ __forceinline__ float bf2f(unsigned b) { return __uint_as_float(b << 16); }
__device__ __forceinline__ float bfhi(unsigned b) { return __uint_as_float(b & 0xffff0000u); }
__device__ __forceinline__ float bflo(unsigned b) { return __uint_as_float(b << 16); }
__device__ __forceinline__ unsigned f2bf(float f) { __bf16 b = (__bf16)f; return (unsigned)__builtin_bit_cast(unsigned short, b); }
__device__ __forceinline__ float rbf(float f) { return bf2f(f2bf(f)); }
__device__ __forceinline__ unsigned pk2(float lo, float hi) { return __builtin_bit_cast(unsigned, __builtin_convertvector(f32x2{lo, hi}, bf16x2)); }
__device__ __forceinline__ float silu(float x) { return x * __builtin_amdgcn_rcpf(1.f + __builtin_amdgcn_exp2f(-x * LOG2E)); }
__device__ __forceinline__ float dot2(unsigned a, unsigned b, float acc) {
  return __builtin_amdgcn_fdot2_f32_bf16(__builtin_bit_cast(bf16x2, a), __builtin_bit_cast(bf16x2, b), acc, false);
}
template <int CTRL> __device__ __forceinline__ float dpp(float x) {
  return __builtin_bit_cast(float, __builtin_amdgcn_mov_dpp(__builtin_bit_cast(int, x), CTRL, 0xf, 0xf, true));
}
template <int CTRL> __device__ __forceinline__ unsigned dppu(unsigned x) { return (unsigned)__builtin_amdgcn_mov_dpp((int)x, CTRL, 0xf, 0xf, true); }
#define MAX_DPP(x, ctrl) ({ float r_; asm("s_nop 1\n\tv_max_f32_dpp %0, %1, %1 " ctrl " row_mask:0xf bank_mask:0xf bound_ctrl:1" : "=v"(r_) : "v"(x)); r_; })
constexpr int XOR1 = 0xB1, XOR2 = 0x4E, XOR7 = 0x141, XOR8 = 0x128, ROR4 = 0x124, BCAST0 = 0x150;
__device__ __forceinline__ float swap32_sum(float a, float b) {
  auto t = __builtin_amdgcn_permlane32_swap(__float_as_uint(a), __float_as_uint(b), false, false);
  return __uint_as_float(t[0]) + __uint_as_float(t[1]);
}
__device__ __forceinline__ float swap16_sum(float a, float b) {
  auto t = __builtin_amdgcn_permlane16_swap(__float_as_uint(a), __float_as_uint(b), false, false);
  return __uint_as_float(t[0]) + __uint_as_float(t[1]);
}
__device__ __forceinline__ float xrow_max(float x) {
  auto s = __builtin_amdgcn_permlane16_swap(__float_as_uint(x), __float_as_uint(x), false, false);
  x = fmaxf(__uint_as_float(s[0]), __uint_as_float(s[1]));
  auto t = __builtin_amdgcn_permlane32_swap(__float_as_uint(x), __float_as_uint(x), false, false);
  return fmaxf(__uint_as_float(t[0]), __uint_as_float(t[1]));
}
__device__ __forceinline__ float xrow_sum(float x) { x = swap16_sum(x, x); return swap32_sum(x, x); }
__device__ __forceinline__ float wave_sum(float x) {
  x += dpp<XOR1>(x); x += dpp<XOR2>(x); x += dpp<XOR7>(x); x += dpp<XOR8>(x);
  return xrow_sum(x);
}
__device__ __forceinline__ void reduce_rows(const float* x, float& e0, float& e1) {
  const float y0 = swap32_sum(x[0], x[4]), y1 = swap32_sum(x[1], x[5]), y2 = swap32_sum(x[2], x[6]), y3 = swap32_sum(x[3], x[7]);
  e0 = swap16_sum(y0, y2); e1 = swap16_sum(y1, y3);
}
__device__ __forceinline__ float reduce_oct(const float* x, int lane) {
  float e0, e1; reduce_rows(x, e0, e1);
  const bool b3 = (lane & 8) != 0;
  return (b3 ? e1 : e0) + dpp<XOR8>(b3 ? e0 : e1);
}

__device__ __forceinline__ u64 ldg(const gu64* p) { return __hip_atomic_load(p, __ATOMIC_RELAXED, __HIP_MEMORY_SCOPE_AGENT); }
__device__ __forceinline__ void stg(gu64* p, u64 v) { __hip_atomic_store(p, v, __ATOMIC_RELAXED, __HIP_MEMORY_SCOPE_AGENT); }
__device__ __forceinline__ u64 gr_pair(unsigned tag, unsigned pay) { return ((u64)pay << 32) | tag; }
__device__ __forceinline__ u64 gr_f24(unsigned tag, float v) { return ((u64)((__float_as_uint(v) + 0x80u) & 0xffffff00u) << 32) | tag; }
__device__ __forceinline__ unsigned bad_pair(u64 g, unsigned tag) { return (unsigned)g ^ tag; }
__device__ __forceinline__ unsigned bad_f24(u64 g, unsigned tag) { return ((unsigned)g ^ tag) | ((unsigned)(g >> 32) & 0xffu); }
__device__ __forceinline__ float val_f24(u64 g) { return __uint_as_float((unsigned)(g >> 32)); }
__device__ __forceinline__ bool none_bad(unsigned bad) { return __builtin_amdgcn_ballot_w64(bad != 0u) == 0ull; }
__device__ __forceinline__ bool ok_pair(u64 g, unsigned tag) { return (unsigned)g == tag; }
__device__ __forceinline__ bool all_ok(bool ok) { return __builtin_amdgcn_ballot_w64(!ok) == 0ull; }
#define PINP(ptr) asm volatile("" : "+v"(ptr))

__device__ __forceinline__ void lds_wait(volatile LAS unsigned* w, unsigned want) {
#define LSLEEP 1
  for (unsigned n = 0; *w != want && n < SPIN_L; ++n) __builtin_amdgcn_s_sleep(LSLEEP);
  asm volatile("" ::: "memory");
}
#define WAIT_LGKM() asm volatile("s_waitcnt lgkmcnt(0)" ::: "memory")
#define BARRIER() do { asm volatile("s_waitcnt lgkmcnt(0)" ::: "memory"); __builtin_amdgcn_s_barrier(); asm volatile("" ::: "memory"); } while (0)

#define STAMP(i) do { } while (0)
#define STAMP_DUMP() do { } while (0)

struct AttnState { float m, l; float acc[8]; };

__device__ __forceinline__ void attn_tile(const v4u (&K)[4], const v4u (&V)[4], const v4u& q, AttnState& st, int c) {
  float x[4];
#pragma unroll
  for (int i = 0; i < 4; ++i) {
    float d = dot2(K[i].x, q.x, 0.f);
    d = dot2(K[i].y, q.y, d); d = dot2(K[i].z, q.z, d); d = dot2(K[i].w, q.w, d);
    x[i] = d;
  }
  const bool b3 = (c & 8) != 0, b2 = (c & 4) != 0;
  const float k0 = b3 ? x[2] : x[0], s0 = b3 ? x[0] : x[2];
  const float k1 = b3 ? x[3] : x[1], s1 = b3 ? x[1] : x[3];
  const float y0 = k0 + dpp<XOR8>(s0), y1 = k1 + dpp<XOR8>(s1);
  const float k2 = b2 ? y1 : y0, s2 = b2 ? y0 : y1;
  float z = k2 + dpp<XOR7>(s2);
  z += dpp<XOR2>(z); z += dpp<XOR1>(z);
  const float s = rbf(rbf(z) * 0.08838834764831845f);
  float pm = MAX_DPP(s, "row_ror:4"); pm = MAX_DPP(pm, "row_ror:8");
  const float mn = fmaxf(st.m, pm);
  if (__builtin_amdgcn_ballot_w64(mn > st.m) != 0ull) {
    const float al = __builtin_amdgcn_exp2f((st.m - mn) * LOG2E);
    st.l *= al;
#pragma unroll
    for (int j = 0; j < 8; ++j) st.acc[j] *= al;
    st.m = mn;
  }
  const float pr = __builtin_amdgcn_exp2f((s - st.m) * LOG2E);
  st.l += pr;
  const unsigned plo = f2bf(pr), phi = plo << 16;
#define PV(i) { const unsigned a = dppu<BCAST0 + 4 * i>(plo), b = dppu<BCAST0 + 4 * i>(phi); \
    st.acc[0] = dot2(V[i].x, a, st.acc[0]); st.acc[1] = dot2(V[i].x, b, st.acc[1]); st.acc[2] = dot2(V[i].y, a, st.acc[2]); st.acc[3] = dot2(V[i].y, b, st.acc[3]); \
    st.acc[4] = dot2(V[i].z, a, st.acc[4]); st.acc[5] = dot2(V[i].z, b, st.acc[5]); st.acc[6] = dot2(V[i].w, a, st.acc[6]); st.acc[7] = dot2(V[i].w, b, st.acc[7]); }
  PV(0) PV(1) PV(2) PV(3)
#undef PV
}

__device__ __forceinline__ void interleave(const v4u& r0, const v4u& r1, unsigned* o) {
#pragma unroll
  for (int j = 0; j < 4; ++j) { o[2 * j] = __builtin_amdgcn_perm(r1[j], r0[j], 0x05040100u); o[2 * j + 1] = __builtin_amdgcn_perm(r1[j], r0[j], 0x07060302u); }
}
__device__ __forceinline__ f32x4 mfma(const v4u& a, const v4u& b, const f32x4& c) {
  return __builtin_amdgcn_mfma_f32_16x16x32_bf16(__builtin_bit_cast(bf16x8, a), __builtin_bit_cast(bf16x8, b), c, 0, 0, 0);
}
__device__ __forceinline__ v4u tr_frag(const LAS unsigned char* p, int stride4) {
  const v2u lo = __builtin_bit_cast(v2u, __builtin_amdgcn_ds_read_tr16_b64_v4i16((LAS v4s*)p));
  const v2u hi = __builtin_bit_cast(v2u, __builtin_amdgcn_ds_read_tr16_b64_v4i16((LAS v4s*)(p + stride4)));
  return v4u{lo.x, lo.y, hi.x, hi.y};
}
#define GLDS(gsrc, ldst, aux) __builtin_amdgcn_global_load_lds((const GAS void*)(gsrc), (LAS void*)(ldst), 16, 0, aux)
__device__ __forceinline__ float sel4(int q4, float a, float b, float c, float d) { return q4 == 0 ? a : q4 == 1 ? b : q4 == 2 ? c : d; }
__device__ __forceinline__ v4u ldnt(const bf16_t* p) { return __builtin_nontemporal_load((const GAS v4u*)p); }
#define PIN8(a) asm volatile("" : "+v"((a)[0]), "+v"((a)[1]), "+v"((a)[2]), "+v"((a)[3]), "+v"((a)[4]), "+v"((a)[5]), "+v"((a)[6]), "+v"((a)[7]))

__global__ void __launch_bounds__(NTHR, 2) adaln_block_kernel(Params p) {
  extern __shared__ __attribute__((aligned(16))) unsigned lds_raw[];
  LAS unsigned* L = (LAS unsigned*)lds_raw;
  volatile LAS unsigned* LV = (volatile LAS unsigned*)lds_raw;
  LAS float* LF = (LAS float*)lds_raw;
  LAS unsigned char* LB = (LAS unsigned char*)lds_raw;
  u64 code_base;
  asm volatile("s_getpc_b64 %0" : "=s"(code_base));
  code_base &= ~255ull;
  const int tid = threadIdx.x, lane = tid & 63, wave = __builtin_amdgcn_readfirstlane(tid >> 6);
  const int r = lane >> 4, c = lane & 15;
  const int b = blockIdx.x, X = b & 7, J = b >> 3, sp = J & 7;
  const int head = 4 * X + (J >> 3);
  const bool fh = head < NFFN;
  const bool is_qc = sp < 2, is_ffn = fh && sp == 5;
  const int mu = (sp == 2 || sp == 3) ? 2 * head + sp - 2 : 999;
  const int own = head;
  const bool is_mod = mu < 60, is_own = sp == 4 && head < 10;
  const int cf = fh, cg = !fh && head < 30, ch_ = head >= 30;
  const int nq_ = cf * NT_Q + cg * NT_G0 + ch_ * NT_H0, nm_ = cf * NT_M + cg * NT_G1 + ch_ * NT_H1;
  const int np_ = cf * NT_P + cg * NT_G2 + ch_ * NT_H1, nf_ = cf * NT_F + cg * NT_G2 + ch_ * NT_H1;
  const int t0 = min(sp, 2) * nq_ + min(max(sp - 2, 0), 2) * nm_ + (int)(sp > 4) * np_ + (int)(sp > 5) * nf_ + (int)(sp > 6) * np_;
  const int nt = (int)(sp < 2) * nq_ + (int)(sp == 2 || sp == 3) * nm_ + (int)(sp == 5) * nf_ + (int)(sp == 4 || sp > 5) * np_;
  if (tid < 16) L[tid] = 0u;
  asm volatile("" :: "s"(p.kc), "s"(p.vc), "s"(p.ws), "s"(p.wq), "s"(p.wmod), "s"(p.c), "s"(p.x), "s"(t0), "s"(nt));
  BARRIER();
  gu64* const XG = (gu64*)(p.ws + WS_XG); gu64* const MG = (gu64*)(p.ws + WS_MG); gu64* const YG = (gu64*)(p.ws + WS_YG);
  gu64* const DG = (gu64*)(p.ws + WS_DG); gu64* const QG = (gu64*)(p.ws + WS_QG); gu64* const PG = (gu64*)(p.ws + WS_PG);

  if (__builtin_expect(wave < 4, 1)) {
    const size_t base = ((size_t)head * SK + (size_t)t0 * 16) * DH;
    const __amdgpu_buffer_rsrc_t rk = __builtin_amdgcn_make_buffer_rsrc((void*)(p.kc + base), 0, nt * 4096, 0x00020000);
    const __amdgpu_buffer_rsrc_t rv = __builtin_amdgcn_make_buffer_rsrc((void*)(p.vc + base), 0, nt * 4096, 0x00020000);
    const int voff = lane * 16;
    const int nw = (nt - wave + 3) >> 2;
    v4u K[TD][4], V[TD][4];
#define ISSUE(d, t) do { const int so_ = (wave + 4 * (t)) * 4096; \
    _Pragma("unroll") for (int i_ = 0; i_ < 4; ++i_) K[d][i_] = __builtin_amdgcn_raw_buffer_load_b128(rk, voff + 1024 * i_, so_, 2); \
    _Pragma("unroll") for (int i_ = 0; i_ < 4; ++i_) V[d][i_] = __builtin_amdgcn_raw_buffer_load_b128(rv, voff + 1024 * i_, so_, 2); \
    __builtin_amdgcn_sched_barrier(0); } while (0)
#define PACE_WAIT(n) do { asm volatile("s_waitcnt vmcnt(%0)" :: "n"(n) : "memory"); __builtin_amdgcn_sched_barrier(0); } while (0)
#define QGATE 1
    if (QGATE == 1 && is_qc) lds_wait(LV + L_FLAG_H, 1u);
    if (QGATE == 2 && is_qc) lds_wait(LV + L_CNT_Q, 4u);
    if (is_mod) lds_wait(LV + L_CNT_G, 1u);
    if (KVDELAY) __builtin_amdgcn_s_sleep(KVDELAY);
    if (wave == 0) STAMP(24);
    ISSUE(0, 0);
#pragma unroll
    for (int d = 1; d < TD; ++d) { PACE_WAIT(PACE0); ISSUE(d, d); }
    const unsigned ov = (unsigned)(size_t)(LB + B_WD + wave * 16384);
#define DMA4(m0v, rsrc, so) do { unsigned keep_; asm volatile("s_mov_b32 %0, m0\n\ts_mov_b32 m0, %1\n\ts_nop 0\n\t" \
      "buffer_load_dwordx4 %2, %3, %4 offen nt lds\n\tbuffer_load_dwordx4 %2, %3, %4 offen offset:1024 nt lds\n\t" \
      "buffer_load_dwordx4 %2, %3, %4 offen offset:2048 nt lds\n\tbuffer_load_dwordx4 %2, %3, %4 offen offset:3072 nt lds\n\t" \
      "s_mov_b32 m0, %0" : "=&s"(keep_) : "s"(m0v), "v"(voff), "s"(rsrc), "s"(so) : "memory"); } while (0)
#define DMA_TILE(e, t) do { const int so_ = (wave + 4 * (t)) * 4096; DMA4(ov + (e) * 8192, rk, so_); DMA4(ov + (e) * 8192 + 4096, rv, so_); __builtin_amdgcn_sched_barrier(0); } while (0)
#define LD_OV(d, e) do { const LAS v4u* src_ = (const LAS v4u*)(LB + B_WD + wave * 16384 + (e) * 8192) + lane; \
      _Pragma("unroll") for (int i_ = 0; i_ < 4; ++i_) { K[d][i_] = src_[64 * i_]; V[d][i_] = src_[256 + 64 * i_]; } __builtin_amdgcn_sched_barrier(0); } while (0)
    PACE_WAIT(PACE0); DMA_TILE(0, 6);
    PACE_WAIT(PACE0); DMA_TILE(1, 7);
    lds_wait(LV + L_FLAG_Q, 1u);
    if (wave == 0) STAMP(1);
    const v4u q = *(const LAS v4u*)(L + L_Q + 4 * c);
    const unsigned tag = L[L_E] + 1u;
    AttnState st; st.m = -INFINITY; st.l = 0.f;
#pragma unroll
    for (int j = 0; j < 8; ++j) st.acc[j] = 0.f;
#define TILE(d) do { attn_tile(K[d], V[d], q, st, c); __builtin_amdgcn_sched_barrier(0); } while (0)
    static_assert(TD == 6, "the catch-up schedule is written for six slots");
    TILE(0); PACE_WAIT(0); ISSUE(0, 8);
    TILE(1); TILE(2); TILE(3); TILE(4);
    LD_OV(4, 0);
    PACE_WAIT(PACE); ISSUE(1, 9);
    TILE(5);
    LD_OV(5, 1);
    WAIT_LGKM();
    if (lane == 0) __hip_atomic_fetch_add((LAS unsigned*)(L + L_CNT_OV), 1u, __ATOMIC_RELAXED, __HIP_MEMORY_SCOPE_WORKGROUP);
    TILE(4); TILE(5);
    TILE(0);
#pragma nounroll
    for (int u = 9; u < nw; u += TD) {
#pragma unroll
      for (int k = 0; k < TD; ++k) {
        if (u + k < nw) {
          PACE_WAIT(PACE);
          ISSUE((k + 2) % TD, u + k + 1);
          if (u + k + 1 + WOLEAD == nw && wave == 0 && lane == 0) LV[L_FLAG_L] = 1u;
          TILE((k + 1) % TD);
        }
      }
    }
#undef TILE
    if (wave == 0 && lane == 0) LV[L_FLAG_L] = 1u;
#undef ISSUE
    if (wave == 0) STAMP(2);
    const float M = xrow_max(st.m);
    const float sc = __builtin_amdgcn_exp2f((st.m - M) * LOG2E);
    float l = ((c & 3) == 0) ? st.l * sc : 0.f;
    l += dpp<ROR4>(l); l += dpp<XOR8>(l); l = xrow_sum(l);
#pragma unroll
    for (int j = 0; j < 8; ++j) st.acc[j] *= sc;
    float e0, e1; reduce_rows(st.acc, e0, e1);
    LAS float* rec = LF + L_REC + wave * 132;
    *(LAS f32x2*)(rec + 4 + 8 * c + 2 * r) = f32x2{e0, e1};
    if (lane == 0) { rec[0] = M; rec[1] = l; }
    WAIT_LGKM();
    unsigned old = 0;
    if (lane == 0) old = __hip_atomic_fetch_add((LAS unsigned*)(L + L_CNT_S1), 1u, __ATOMIC_RELAXED, __HIP_MEMORY_SCOPE_WORKGROUP);
    old = __builtin_amdgcn_readfirstlane(old);
    if (old == 3u) {
      asm volatile("" ::: "memory");
      const LAS float* R = LF + L_REC;
      const float m0 = R[0], m1 = R[132], m2 = R[264], m3 = R[396];
      const float MM = fmaxf(fmaxf(m0, m1), fmaxf(m2, m3));
      const float w0 = __builtin_amdgcn_exp2f((m0 - MM) * LOG2E), w1 = __builtin_amdgcn_exp2f((m1 - MM) * LOG2E);
      const float w2 = __builtin_amdgcn_exp2f((m2 - MM) * LOG2E), w3 = __builtin_amdgcn_exp2f((m3 - MM) * LOG2E);
      const float LL = (w0 * R[1] + w1 * R[133]) + (w2 * R[265] + w3 * R[397]);
      const float oa = (w0 * R[4 + lane] + w1 * R[136 + lane]) + (w2 * R[268 + lane] + w3 * R[400 + lane]);
      const float ob = (w0 * R[68 + lane] + w1 * R[200 + lane]) + (w2 * R[332 + lane] + w3 * R[464 + lane]);
      gu64* rp = PG + (size_t)(head * 8 + sp) * PG_STRIDE;
      stg(rp + lane, gr_f24(tag, oa));
      stg(rp + 64 + lane, gr_f24(tag, ob));
      if (lane < 2) stg(rp + 128 + lane, gr_f24(tag, lane == 0 ? MM : LL));
      if (lane == 0) LV[L_FLAG_S1] = 1u;
      STAMP(3);
    }
    return;
  }

  const int v = wave - 4, t = tid - 256;
  const int r8 = lane >> 3, c8 = lane & 7;
  unsigned tag = 0;
  if (is_qc) {
    const unsigned e_in = __hip_atomic_load((gu32*)(p.ws + WS_EPOCH), __ATOMIC_RELAXED, __HIP_MEMORY_SCOPE_AGENT);
    v4u w[20];
    {
      const bf16_t* src = p.wq + (size_t)(160 * v + 2 * r8) * (HQ * DH) + head * DH + 64 * sp + 8 * c8;
#pragma unroll
      for (int i = 0; i < 10; ++i) { w[2 * i] = ldnt(src + (size_t)(16 * i) * (HQ * DH)); w[2 * i + 1] = ldnt(src + (size_t)(16 * i + 1) * (HQ * DH)); }
    }
    unsigned xs[5];
#pragma unroll
    for (int i = 0; i < 5; ++i) xs[i] = ((const GAS unsigned*)p.x)[lane + 64 * i];
    const unsigned xo0 = ((const GAS unsigned*)p.x)[80 * v + lane], xo1 = ((const GAS unsigned*)p.x)[80 * v + 64 + (lane & 15)];
    __builtin_amdgcn_sched_barrier(0);
    if (v == 0 && lane == 0) { LV[L_E] = e_in; WAIT_LGKM(); LV[L_FLAG_E] = 1u; }
    float n0a, n0b, n1a, n1b;
    {
      float s = 0.f;
#pragma unroll
      for (int i = 0; i < 5; ++i) s += bflo(xs[i]) + bfhi(xs[i]);
      const float mean_f = wave_sum(s) * (1.f / D);
      float vs = 0.f;
#pragma unroll
      for (int i = 0; i < 5; ++i) { const float d0 = bflo(xs[i]) - mean_f, d1 = bfhi(xs[i]) - mean_f; vs = fmaf(d0, d0, vs); vs = fmaf(d1, d1, vs); }
      const float var = rbf(wave_sum(vs) * (1.f / D)), mean = rbf(mean_f);
      const float rs = __builtin_amdgcn_rcpf(rbf(__builtin_amdgcn_sqrtf(rbf(var + 1e-5f))));
      n0a = rbf(rbf(bflo(xo0) - mean) * rs); n0b = rbf(rbf(bfhi(xo0) - mean) * rs);
      n1a = rbf(rbf(bflo(xo1) - mean) * rs); n1b = rbf(rbf(bfhi(xo1) - mean) * rs);
      asm volatile("" : "+v"(n0a), "+v"(n0b), "+v"(n1a), "+v"(n1b));
    }
    unsigned wi[10][8];
#pragma unroll
    for (int i = 0; i < 10; ++i) { interleave(w[2 * i], w[2 * i + 1], wi[i]); PIN8(wi[i]); }
    if (lane == 0) __hip_atomic_fetch_add((LAS unsigned*)(L + L_CNT_Q), 1u, __ATOMIC_RELAXED, __HIP_MEMORY_SCOPE_WORKGROUP);
    lds_wait(LV + L_FLAG_E, 1u);
    tag = LV[L_E] + 1u;
    if (v == 0) STAMP(18);
    unsigned s0, c0, s1, c1;
    {
      const gu64* m0 = MG + 80 * v + lane; const gu64* m1 = MG + 80 * v + 64 + (lane & 15);
      for (unsigned n = 0; n < SPIN_G; ++n) {
        const u64 a0 = ldg(m0), b0 = ldg(m0 + 320), a1 = ldg(m1), b1 = ldg(m1 + 320);
        s0 = (unsigned)(a0 >> 32); c0 = (unsigned)(b0 >> 32); s1 = (unsigned)(a1 >> 32); c1 = (unsigned)(b1 >> 32);
        if (all_ok(ok_pair(a0, tag) & ok_pair(b0, tag) & ok_pair(a1, tag) & ok_pair(b1, tag))) break;
      }
    }
    if (v == 0) STAMP(5);
    L[L_H2 + 80 * v + lane] = f2bf(rbf(n0a * rbf(1.f + bflo(c0))) + bflo(s0)) | (f2bf(rbf(n0b * rbf(1.f + bfhi(c0))) + bfhi(s0)) << 16);
    if (lane < 16) L[L_H2 + 80 * v + 64 + lane] = f2bf(rbf(n1a * rbf(1.f + bflo(c1))) + bflo(s1)) | (f2bf(rbf(n1b * rbf(1.f + bfhi(c1))) + bfhi(s1)) << 16);
    WAIT_LGKM();
    float acc[8];
#pragma unroll
    for (int j = 0; j < 8; ++j) acc[j] = 0.f;
#pragma unroll
    for (int i = 0; i < 10; ++i) {
      const unsigned hp = L[L_H2 + 80 * v + 8 * i + r8];
#pragma unroll
      for (int j = 0; j < 8; ++j) acc[j] = dot2(wi[i][j], hp, acc[j]);
    }
    LF[L_MR + 64 * v + 8 * c8 + r8] = reduce_oct(acc, lane);
    WAIT_LGKM();
    unsigned old = 0;
    if (lane == 0) old = __hip_atomic_fetch_add((LAS unsigned*)(L + L_CNT_M), 1u, __ATOMIC_RELAXED, __HIP_MEMORY_SCOPE_WORKGROUP);
    if (__builtin_amdgcn_readfirstlane(old) == 3u) {
      asm volatile("" ::: "memory");
      const unsigned qb = f2bf((LF[L_MR + lane] + LF[L_MR + 64 + lane]) + (LF[L_MR + 128 + lane] + LF[L_MR + 192 + lane]));
      const unsigned up = dppu<XOR1>(qb);
      if ((lane & 1) == 0) stg(QG + head * 64 + 32 * sp + (lane >> 1), gr_pair(tag, qb | (up << 16)));
      if (lane == 0) LV[L_FLAG_H] = 1u;
      STAMP(6);
    }
  } else {
    const unsigned e_in = __hip_atomic_load((gu32*)(p.ws + WS_EPOCH), __ATOMIC_RELAXED, __HIP_MEMORY_SCOPE_AGENT);
    const __amdgpu_buffer_rsrc_t rm = __builtin_amdgcn_make_buffer_rsrc((void*)p.wmod, 0, is_mod ? D * NMOD * 2 : 0, 0x00020000);
    const __amdgpu_buffer_rsrc_t rc = __builtin_amdgcn_make_buffer_rsrc((void*)p.c, 0, is_mod ? D * 2 : 0, 0x00020000);
    const unsigned cv0 = __builtin_amdgcn_raw_buffer_load_b16(rc, (160 * v + lane) * 2, 0, 0), cv1 = __builtin_amdgcn_raw_buffer_load_b16(rc, (160 * v + 64 + lane) * 2, 0, 0),
                   cv2 = __builtin_amdgcn_raw_buffer_load_b16(rc, (160 * v + 128 + (lane & 31)) * 2, 0, 0);
    v4u wm[20];
#pragma unroll
    for (int i = 0; i < 20; ++i) wm[i] = __builtin_amdgcn_raw_buffer_load_b128(rm, ((160 * v + r8) * NMOD + 64 * mu + 8 * c8) * 2, i * (8 * NMOD * 2), 2);
    __builtin_amdgcn_sched_barrier(0);
    if (v == 0 && lane == 0) { LV[L_E] = e_in; WAIT_LGKM(); LV[L_FLAG_E] = 1u; }
    if (is_mod) {
      L[L_SC + 160 * v + lane] = f2bf(silu(bf2f(cv0))); L[L_SC + 160 * v + 64 + lane] = f2bf(silu(bf2f(cv1)));
      if (lane < 32) L[L_SC + 160 * v + 128 + lane] = f2bf(silu(bf2f(cv2)));
      WAIT_LGKM();
      float acc[8];
#pragma unroll
      for (int j = 0; j < 8; ++j) acc[j] = 0.f;
#pragma unroll
      for (int i = 0; i < 20; ++i) {
        const unsigned a = L[L_SC + 160 * v + 8 * i + r8], bb = a << 16;
#pragma unroll
        for (int j = 0; j < 4; ++j) { acc[2 * j] = dot2(wm[i][j], a, acc[2 * j]); acc[2 * j + 1] = dot2(wm[i][j], bb, acc[2 * j + 1]); }
      }
      LF[L_MR + 64 * v + 8 * c8 + r8] = reduce_oct(acc, lane);
      WAIT_LGKM();
      unsigned old = 0;
      if (lane == 0) old = __hip_atomic_fetch_add((LAS unsigned*)(L + L_CNT_M), 1u, __ATOMIC_RELAXED, __HIP_MEMORY_SCOPE_WORKGROUP);
      if (__builtin_amdgcn_readfirstlane(old) == 3u) {
        asm volatile("" ::: "memory");
        const unsigned mb_ = f2bf((LF[L_MR + lane] + LF[L_MR + 64 + lane]) + (LF[L_MR + 128 + lane] + LF[L_MR + 192 + lane]));
        const unsigned up = dppu<XOR1>(mb_);
        lds_wait(LV + L_FLAG_E, 1u);
        if ((lane & 1) == 0) stg(MG + 32 * mu + (lane >> 1), gr_pair(LV[L_E] + 1u, mb_ | (up << 16)));
        if (lane == 0) LV[L_CNT_G] = 1u;
        STAMP(4);
      }
    }
    if (v == 3 && J * 1024 < CODE_BYTES) {
      const v4u cw = ((const GAS v4u*)(code_base + (u64)(J * 1024)))[lane]; asm volatile("" :: "v"(cw));
    }
  }
  if (v == 1) {
    lds_wait(LV + L_FLAG_E, 1u);
    tag = LV[L_E] + 1u;
    const gu64* qp = QG + head * 64 + lane;
    if (!is_qc) __builtin_amdgcn_s_sleep(QDELAY);
    u64 g0 = ldg(qp); __builtin_amdgcn_s_sleep(STAG);
    u64 g1 = ldg(qp); __builtin_amdgcn_s_sleep(STAG);
    u64 g2 = ldg(qp), g;
    for (unsigned n = 0;; ++n) {
      if (all_ok(ok_pair(g0, tag)) || n >= SPIN_G) { g = g0; break; }
      g0 = ldg(qp);
      if (all_ok(ok_pair(g1, tag))) { g = g1; break; }
      g1 = ldg(qp);
      if (all_ok(ok_pair(g2, tag))) { g = g2; break; }
      g2 = ldg(qp);
    }
    L[L_Q + lane] = (unsigned)(g >> 32);
    WAIT_LGKM();
    if (lane == 0) LV[L_FLAG_Q] = 1u;
    STAMP(7);
  }
  lds_wait(LV + L_FLAG_E, 1u);
  tag = LV[L_E] + 1u;

  const int g4 = lane >> 4, tq = (lane & 15) >> 2, tp = lane & 3;
  v4u wf[5][8];
  unsigned mbA[2], mbB[2];
  if (is_ffn) {
#define FPACE 63
    v4u raw[5][8];
    {
      const size_t ro = (size_t)(160 * v + r8) * F + 64 * head + 8 * c8;
#pragma unroll
      for (int kb = 0; kb < 5; ++kb) {
        if (FPACE < 63) asm volatile("s_waitcnt vmcnt(%0)" :: "n"(FPACE) : "memory");
#pragma unroll
        for (int i = 0; i < 4; ++i) { raw[kb][i] = ldnt(p.wg + ro + (size_t)(32 * kb + 8 * i) * F); raw[kb][4 + i] = ldnt(p.wu + ro + (size_t)(32 * kb + 8 * i) * F); }
        __builtin_amdgcn_sched_barrier(0);
      }
    }
    __builtin_amdgcn_sched_barrier(0);
    LAS unsigned char* stg_w = LB + B_STG + v * 4096 + r8 * 128 + c8 * 16;
    const LAS unsigned char* stg_r = LB + B_STG + v * 4096 + (8 * g4 + tq) * 128 + tp * 8;
#pragma unroll
    for (int kb = 0; kb < 5; ++kb)
#pragma unroll
      for (int m = 0; m < 2; ++m) {
#pragma unroll
        for (int i = 0; i < 4; ++i) *(LAS v4u*)(stg_w + i * 1024) = raw[kb][4 * m + i];
        asm volatile("" ::: "memory");
#pragma unroll
        for (int cg = 0; cg < 4; ++cg) wf[kb][4 * m + cg] = tr_frag(stg_r + cg * 32, 512);
        asm volatile("s_waitcnt lgkmcnt(0)" ::: "memory");
      }
    if (v == 0) STAMP(21);
    if (v == 1) STAMP(18);
    lds_wait(LV + L_CNT_OV, 4u);
    if (v == 0) STAMP(22);
#pragma unroll
    for (int i = 0; i < 20; ++i) {
      const int idx = 64 * i + lane, row = idx / 20, ch = idx - 20 * row;
      GLDS(p.wd + (size_t)(64 * head + row) * D + 160 * v + 8 * ch, LB + B_WD + v * 20480 + 1024 * i, 2);
    }
    {
      const gu64* m0 = MG + 80 * v + lane; const gu64* m1 = MG + 80 * v + 64 + (lane & 15);
      for (unsigned n = 0; n < SPIN_G; ++n) {
        const u64 s0 = ldg(m0 + 960), c0 = ldg(m0 + 1280), s1 = ldg(m1 + 960), c1 = ldg(m1 + 1280);
        const unsigned sp0 = (unsigned)(s0 >> 32), cp0 = (unsigned)(c0 >> 32), sp1 = (unsigned)(s1 >> 32), cp1 = (unsigned)(c1 >> 32);
        mbA[0] = (sp0 & 0xffffu) | (f2bf(1.f + bflo(cp0)) << 16); mbB[0] = (sp0 >> 16) | (f2bf(1.f + bfhi(cp0)) << 16);
        mbA[1] = (sp1 & 0xffffu) | (f2bf(1.f + bflo(cp1)) << 16); mbB[1] = (sp1 >> 16) | (f2bf(1.f + bfhi(cp1)) << 16);
        if (all_ok(ok_pair(s0, tag) & ok_pair(c0, tag) & ok_pair(s1, tag) & ok_pair(c1, tag))) break;
        __builtin_amdgcn_s_sleep(8);
      }
    }
  }
  float own_x = 0.f, own_ga = 0.f, own_gb = 0.f;
  if (is_own) {
    const int col = 64 * own + 16 * v + c;
    const gu64* m = MG + (lane < 32 ? 640 : 1600) + (col >> 1);
    own_x = bf2f(((const GAS bf16_t*)p.x)[col]);
    unsigned g = 0;
    for (unsigned n = SPIN_G; n != 0u; --n) { const u64 a = ldg(m); g = (unsigned)(a >> 32); if (none_bad(bad_pair(a, tag))) break; __builtin_amdgcn_s_sleep(8); }
    const float gv = (c & 1) ? bfhi(g) : bflo(g);
    const float ga_h = lane < 32 ? gv : 0.f, gb_h = lane < 32 ? 0.f : gv;
    own_ga = swap32_sum(ga_h, ga_h); own_gb = swap32_sum(gb_h, gb_h);
  }
  lds_wait(LV + L_FLAG_L, 1u);
#pragma unroll
  for (int i = 0; i < 5; ++i) {
    const int idx = 256 * i + t, row = idx / 10, ch = idx - 10 * row;
    GLDS(p.wo + (size_t)(head * DH + row) * D + 80 * sp + 8 * ch, LB + B_WO + (256 * i + 64 * v) * 16, 0);
  }
  asm volatile("s_waitcnt vmcnt(0)" ::: "memory");
  if (lane == 0) __hip_atomic_fetch_add((LAS unsigned*)(L + L_CNT_W), 1u, __ATOMIC_RELAXED, __HIP_MEMORY_SCOPE_WORKGROUP);
  lds_wait(LV + L_CNT_W, 4u);
  v4u wof[4], wof4[4];
  {
    const LAS unsigned char* src = LB + B_WO + (8 * g4 + tq) * 160 + tp * 8;
#pragma unroll
    for (int kb = 0; kb < 4; ++kb) { wof[kb] = tr_frag(src + kb * 32 * 160 + v * 32, 4 * 160); wof4[kb] = tr_frag(src + kb * 32 * 160 + 4 * 32, 4 * 160); }
  }
  if (v == 0) STAMP(8);

  const int hf = lane >> 5;
  const gu64* hop = PG + (size_t)(head * 8 + hf) * PG_STRIDE + 32 * v + (lane & 31);
  const gu64* hmp = PG + (size_t)(head * 8 + (((lane & 15) >> 1) ^ hf)) * PG_STRIDE + 128 + (lane & 1);
  gu64* ysp = YG + (size_t)(5 * sp + (lane < 16 ? v : 4)) * 512 + head * 16 + c;
  LAS bf16_t* odst = (LAS bf16_t*)(L + L_O) + 32 * v + (lane & 31);
  PINP(hop); PINP(hmp); PINP(ysp);
  lds_wait(LV + L_FLAG_S1, 1u);
  if (v == 0) STAMP(9);
  {
    u64 g0, g1, g2, g3, gm;
    for (unsigned n = SPIN_G; n != 0u; --n) {
      g0 = ldg(hop); g1 = ldg(hop + 2 * PG_STRIDE); g2 = ldg(hop + 4 * PG_STRIDE); g3 = ldg(hop + 6 * PG_STRIDE); gm = ldg(hmp);
      if (none_bad((bad_f24(g0, tag) | bad_f24(g1, tag) | bad_f24(g2, tag)) | (bad_f24(g3, tag) | bad_f24(gm, tag)))) break;
      if (PSLEEP) __builtin_amdgcn_s_sleep(PSLEEP);
    }
    const float ml = val_f24(gm);
    const float mm = (lane & 1) ? -INFINITY : ml;
    float M = MAX_DPP(mm, "quad_perm:[2,3,0,1]"); M = MAX_DPP(M, "row_half_mirror"); M = MAX_DPP(M, "row_ror:8"); M = MAX_DPP(M, "quad_perm:[1,0,3,2]");
    const float wgt = __builtin_amdgcn_exp2f((mm - M) * LOG2E);
    const float wn = dpp<XOR1>(wgt);
    const float wl = (lane & 1) ? wn * ml : 0.f;
    float Ls = wl + dpp<XOR1>(wl); Ls += dpp<XOR2>(Ls); Ls += dpp<XOR7>(Ls); Ls += dpp<XOR8>(Ls);
    float acc = dpp<BCAST0>(wgt) * val_f24(g0);
    acc = fmaf(dpp<BCAST0 + 4>(wgt), val_f24(g1), acc); acc = fmaf(dpp<BCAST0 + 8>(wgt), val_f24(g2), acc); acc = fmaf(dpp<BCAST0 + 12>(wgt), val_f24(g3), acc);
    acc = swap32_sum(acc, acc);
    *odst = (bf16_t)f2bf(acc * __builtin_amdgcn_rcpf(Ls));
  }
  BARRIER();
  if (v == 0) STAMP(10);
  {
    v4u A[4];
#pragma unroll
    for (int kb = 0; kb < 4; ++kb) A[kb] = *(const LAS v4u*)(L + L_O + 16 * kb + 4 * g4);
    f32x4 y = {0.f, 0.f, 0.f, 0.f}, y4 = {0.f, 0.f, 0.f, 0.f};
#pragma unroll
    for (int kb = 0; kb < 4; ++kb) y = mfma(A[kb], wof[kb], y);
    if (v == 0) {
#pragma unroll
      for (int kb = 0; kb < 4; ++kb) y4 = mfma(A[kb], wof4[kb], y4);
    }
    if (lane < 16 || (v == 0 && lane < 32)) stg(ysp, gr_f24(tag, lane < 16 ? y[0] : y4[0]));
  }
  if (v == 0) STAMP(11);
  if (is_own) {
    const int og = 4 * own + v;
    const gu64* yp = YG + (size_t)og * 512 + lane;
    const gu64* dp = DG + (size_t)og * 512 + lane;
    const gu64* dp6 = dp + (lane < 48 ? 384 : 0);
    gu64* xp = XG + 16 * og + (c >> 1);
    GAS bf16_t* op = (GAS bf16_t*)p.out + 16 * og + c;
    PINP(yp); PINP(dp); PINP(dp6); PINP(xp); PINP(op);
    u64 g[8];
    for (unsigned n = SPIN_G; n != 0u; --n) {
      unsigned bad = 0;
#pragma unroll
      for (int i = 0; i < 8; ++i) { g[i] = ldg(yp + 64 * i); bad |= bad_f24(g[i], tag); }
      if (none_bad(bad)) break;
      if (PSLEEP) __builtin_amdgcn_s_sleep(PSLEEP);
    }
    if (v == 0) STAMP(30);
    float s = ((val_f24(g[0]) + val_f24(g[1])) + (val_f24(g[2]) + val_f24(g[3]))) + ((val_f24(g[4]) + val_f24(g[5])) + (val_f24(g[6]) + val_f24(g[7])));
    s = xrow_sum(s);
    const float x1 = rbf(own_x + rbf(own_ga * rbf(s)));
    const unsigned xb = f2bf(x1), pair = xb | (dppu<XOR1>(xb) << 16);
    if (lane < 16 && (lane & 1) == 0) stg(xp, gr_pair(tag, pair));
    if (v == 0) STAMP(12);
    for (unsigned n = SPIN_G; n != 0u; --n) {
      unsigned bad = 0;
#pragma unroll
      for (int i = 0; i < 6; ++i) { g[i] = ldg(dp + 64 * i); bad |= bad_f24(g[i], tag); }
      g[6] = ldg(dp6); bad |= bad_f24(g[6], tag);
      if (none_bad(bad)) break;
      if (PSLEEP) __builtin_amdgcn_s_sleep(PSLEEP);
    }
    if (v == 0) STAMP(15);
    s = ((val_f24(g[0]) + val_f24(g[1])) + (val_f24(g[2]) + val_f24(g[3]))) + ((val_f24(g[4]) + val_f24(g[5])) + (lane < 48 ? val_f24(g[6]) : 0.f));
    s = xrow_sum(s);
    if (lane < 16) *op = (bf16_t)f2bf(x1 + rbf(own_gb * rbf(s)));
    if (og == 0 && lane == 0) __hip_atomic_store((gu32*)(p.ws + WS_EPOCH), tag, __ATOMIC_RELAXED, __HIP_MEMORY_SCOPE_AGENT);
    if (v == 0) STAMP(16);
  }
  if (is_ffn) {
    const int ua = 80 * v + lane, ub = 80 * v + 64 + (lane & 15);
    const gu64* xa = XG + (ua >> 3) * 16 + (ua & 7); const gu64* xb = XG + (ub >> 3) * 16 + (ub & 7);
    gu64* dq = DG + (size_t)(10 * v + g4) * 512 + head * 16 + c;
    PINP(xa); PINP(xb); PINP(dq);
    unsigned xo0, xo1;
    for (unsigned n = SPIN_G; n != 0u; --n) {
      const u64 g0 = ldg(xa), g1 = ldg(xb);
      xo0 = (unsigned)(g0 >> 32); xo1 = (unsigned)(g1 >> 32);
      if (none_bad(bad_pair(g0, tag) | bad_pair(g1, tag))) break;
      if (PSLEEP) __builtin_amdgcn_s_sleep(PSLEEP);
    }
    if (v == 0) STAMP(13);
    {
      const unsigned xm = lane < 16 ? xo1 : 0u;
      float s1 = dot2(xm, 0x3f803f80u, dot2(xo0, 0x3f803f80u, 0.f)), s2 = dot2(xm, xm, dot2(xo0, xo0, 0.f));
      s1 += dpp<XOR1>(s1); s2 += dpp<XOR1>(s2); s1 += dpp<XOR2>(s1); s2 += dpp<XOR2>(s2);
      s1 += dpp<XOR7>(s1); s2 += dpp<XOR7>(s2); s1 += dpp<XOR8>(s1); s2 += dpp<XOR8>(s2);
      s1 = xrow_sum(s1); s2 = xrow_sum(s2);
      if (lane == 0) *(LAS f32x2*)(LF + L_ST + 2 * v) = f32x2{s1, s2};
    }
    if (v == 0) STAMP(25);
    BARRIER();
    if (v == 0) STAMP(26);
    {
      const f32x4 sa = *(const LAS f32x4*)(LF + L_ST), sb = *(const LAS f32x4*)(LF + L_ST + 4);
      const float mean_f = ((sa[0] + sa[2]) + (sb[0] + sb[2])) * (1.f / D);
      const float ex2 = ((sa[1] + sa[3]) + (sb[1] + sb[3])) * (1.f / D);
      float var_f = fmaf(-mean_f, mean_f, ex2);
      if (__builtin_amdgcn_ballot_w64(var_f * 256.f < ex2) != 0ull) {
        float vs = 0.f;
#pragma unroll
        for (int i = 0; i < 5; ++i) { const int u_ = lane + 64 * i; const unsigned xp = (unsigned)(ldg(XG + (u_ >> 3) * 16 + (u_ & 7)) >> 32); const float d0 = bflo(xp) - mean_f, d1 = bfhi(xp) - mean_f; vs = fmaf(d0, d0, vs); vs = fmaf(d1, d1, vs); }
        var_f = wave_sum(vs) * (1.f / D);
      }
      const float var = rbf(var_f), mean = rbf(mean_f);
      const float rs = __builtin_amdgcn_rcpf(rbf(__builtin_amdgcn_sqrtf(rbf(var + 1e-5f))));
#define LNPAIR(xp, ma, mb_) ({ const unsigned d_ = pk2(bflo(xp) - mean, bfhi(xp) - mean); const unsigned n_ = pk2(bflo(d_) * rs, bfhi(d_) * rs); \
        const unsigned m_ = pk2(bflo(n_) * bfhi(ma), bfhi(n_) * bfhi(mb_)); pk2(bflo(m_) + bflo(ma), bfhi(m_) + bflo(mb_)); })
      L[L_H2 + 80 * v + lane] = LNPAIR(xo0, mbA[0], mbB[0]);
      const unsigned h1 = LNPAIR(xo1, mbA[1], mbB[1]);
      if (lane < 16) L[L_H2 + 80 * v + 64 + lane] = h1;
#undef LNPAIR
    }
    if (v == 0) STAMP(27);
    v4u wd[2][10];
    {
      v4u A[5];
#pragma unroll
      for (int kb = 0; kb < 5; ++kb) A[kb] = *(const LAS v4u*)(L + L_H2 + 80 * v + 16 * kb + 4 * g4);
      f32x4 acc[8];
#pragma unroll
      for (int cg = 0; cg < 8; ++cg) acc[cg] = f32x4{0.f, 0.f, 0.f, 0.f};
      const LAS unsigned char* src = LB + B_WD + v * 20480 + (8 * g4 + tq) * 320 + tp * 8;
#pragma unroll
      for (int kb = 0; kb < 5; ++kb) {
#pragma unroll
        for (int cg = 0; cg < 8; ++cg) acc[cg] = mfma(A[kb], wf[kb][cg], acc[cg]);
        __builtin_amdgcn_sched_barrier(0);
        if (kb < 4) {
#pragma unroll
          for (int j = 0; j < 5; ++j) { const int f = 5 * kb + j; wd[f / 10][f % 10] = tr_frag(src + (f / 10) * 32 * 320 + (f % 10) * 32, 4 * 320); }
        }
        __builtin_amdgcn_sched_barrier(0);
      }
      LF[L_RGU + v * 128 + lane] = sel4(g4, acc[0][0], acc[1][0], acc[2][0], acc[3][0]);
      LF[L_RGU + v * 128 + 64 + lane] = sel4(g4, acc[4][0], acc[5][0], acc[6][0], acc[7][0]);
    }
    if (v == 0) STAMP(28);
    BARRIER();
    if (v == 0) STAMP(29);
    {
      const float G = rbf((LF[L_RGU + lane] + LF[L_RGU + 128 + lane]) + (LF[L_RGU + 256 + lane] + LF[L_RGU + 384 + lane]));
      const float U = rbf((LF[L_RGU + 64 + lane] + LF[L_RGU + 192 + lane]) + (LF[L_RGU + 320 + lane] + LF[L_RGU + 448 + lane]));
      ((LAS bf16_t*)(L + L_ACT + 32 * v))[lane] = (bf16_t)f2bf(rbf(silu(G)) * U);
      if (v == 0) STAMP(14);
      const v4u A0 = *(const LAS v4u*)(L + L_ACT + 32 * v + 4 * g4), A1 = *(const LAS v4u*)(L + L_ACT + 32 * v + 16 + 4 * g4);
      f32x4 acc[10];
#pragma unroll
      for (int cg = 0; cg < 10; ++cg) { acc[cg] = mfma(A0, wd[0][cg], f32x4{0.f, 0.f, 0.f, 0.f}); acc[cg] = mfma(A1, wd[1][cg], acc[cg]); }
      stg(dq, gr_f24(tag, sel4(g4, acc[0][0], acc[1][0], acc[2][0], acc[3][0])));
      stg(dq + 4 * 512, gr_f24(tag, sel4(g4, acc[4][0], acc[5][0], acc[6][0], acc[7][0])));
      if (lane < 32) stg(dq + 8 * 512, gr_f24(tag, g4 == 0 ? acc[8][0] : acc[9][0]));
    }
    if (v == 0) STAMP(17);
  }
  if (v == 0) STAMP_DUMP();
}

extern "C" void kernel_launch(void* const* d_in, const int* in_sizes, int n_in, void* d_out, int out_size, void* d_ws, size_t ws_size, hipStream_t stream) {
  Params p;
  p.x = (const bf16_t*)d_in[0]; p.kc = (const bf16_t*)d_in[1]; p.vc = (const bf16_t*)d_in[2]; p.wq = (const bf16_t*)d_in[3]; p.wo = (const bf16_t*)d_in[4];
  p.wg = (const bf16_t*)d_in[6]; p.wu = (const bf16_t*)d_in[7]; p.wd = (const bf16_t*)d_in[8]; p.c = (const bf16_t*)d_in[9]; p.wmod = (const bf16_t*)d_in[10];
  p.out = (bf16_t*)d_out; p.ws = (unsigned char*)d_ws;
  (void)hipFuncSetAttribute((const void*)adaln_block_kernel, hipFuncAttributeMaxDynamicSharedMemorySize, LDS_BYTES);
  adaln_block_kernel<<<NCU, NTHR, LDS_BYTES, stream>>>(p);
}
```

```cpp
#include <hip/hip_runtime.h>
#include <hip/hip_bf16.h>
#include <stdint.h>

#define TD 6
#define PACE 0
#define PACE0 0
#define KVDELAY 0
#define PSLEEP 0
#define TLBWARM 1
#define WOLEAD 1
#define GATE 1
#define STAG 12
#define QDELAY 64
#define STAMPS 0
#define NT_Q 54
#define NT_M 64
#define NT_F 42
#define NT_P ((512 - 2 * NT_Q - 2 * NT_M - NT_F) / 3)
static_assert(2 * NT_Q + 2 * NT_M + NT_F + 3 * NT_P == 512, "tiles of a head");
#define NT_G0 54
#define NT_G1 60
#define NT_G2 ((512 - 2 * NT_G0 - 2 * NT_G1) / 4)
static_assert(2 * NT_G0 + 2 * NT_G1 + 4 * NT_G2 == 512, "tiles of a head");
#define NT_H0 52
#define NT_H1 ((512 - 2 * NT_H0) / 6)
static_assert(2 * NT_H0 + 6 * NT_H1 == 512, "tiles of a head");
static_assert(NT_F >= 36, "every stream wave takes at least 9 tiles (the catch-up schedule)");

typedef unsigned short bf16_t;
typedef unsigned long long u64;
typedef unsigned v4u __attribute__((ext_vector_type(4)));
typedef float f32x4 __attribute__((ext_vector_type(4)));
typedef float f32x2 __attribute__((ext_vector_type(2)));
typedef unsigned v2u __attribute__((ext_vector_type(2)));
typedef __bf16 bf16x2 __attribute__((ext_vector_type(2)));
typedef __bf16 bf16x8 __attribute__((ext_vector_type(8)));
typedef short v4s __attribute__((ext_vector_type(4)));
#define GAS __attribute__((address_space(1)))
#define LAS __attribute__((address_space(3)))
typedef GAS u64 gu64;
typedef GAS unsigned gu32;

constexpr int D = 640, HQ = 32, SK = 8192, DH = 128, F = 1728, NMOD = 6 * D;
constexpr int NCU = 256, NTHR = 512, NFFN = 27;
constexpr float LOG2E = 1.4426950408889634f;

constexpr size_t WS_EPOCH = 0;
constexpr size_t WS_XG = 393216 + 32768;
constexpr size_t WS_MG = 8192;
constexpr size_t WS_QG = 32768;
constexpr size_t WS_YG = 65536;
constexpr size_t WS_DG = 262144;
constexpr size_t WS_PG = 524288;
constexpr int PG_STRIDE = 160;
constexpr size_t WS_STAMP = 64u << 20;

constexpr int L_FLAG_Q = 0, L_CNT_S1 = 1, L_FLAG_S1 = 2, L_FLAG_E = 3, L_E = 4, L_CNT_M = 5, L_FLAG_H = 6, L_CNT_G = 7, L_CNT_W = 8, L_FLAG_L = 9, L_CNT_OV = 10, L_CNT_Q = 11;
constexpr int L_Q = 32;
constexpr int L_REC = 96;
constexpr int L_O = 624;
constexpr int L_MR = 704;
constexpr int L_SC = 960;
constexpr int L_H2 = 1600;
constexpr int L_ST = 1920;
constexpr int L_RGU = 1936;
constexpr int L_ACT = 2448;
constexpr int L_STAMP = 2816;
constexpr int B_WO = 12288;
constexpr int B_STG = 32768;
constexpr int B_WD = 49152;
constexpr int LDS_BYTES = 131072;

constexpr int CODE_BYTES = 29 * 1024;
constexpr unsigned SPIN_G = 1u << 15;
constexpr unsigned SPIN_L = 1u << 20;

struct Params {
  const bf16_t* x; const bf16_t* kc; const bf16_t* vc; const bf16_t* wq; const bf16_t* wo;
  const bf16_t* wg; const bf16_t* wu; const bf16_t* wd; const bf16_t* c; const bf16_t* wmod;
  bf16_t* out; unsigned char* ws;
};

__device__ __forceinline__ float bf2f(unsigned b) { return __uint_as_float(b << 16); }
__device__ __forceinline__ float bfhi(unsigned b) { return __uint_as_float(b & 0xffff0000u); }
__device__ __forceinline__ float bflo(unsigned b) { return __uint_as_float(b << 16); }
__device__ __forceinline__ unsigned f2bf(float f) { __bf16 b = (__bf16)f; return (unsigned)__builtin_bit_cast(unsigned short, b); }
__device__ __forceinline__ float rbf(float f) { return bf2f(f2bf(f)); }
__device__ __forceinline__ unsigned pk2(float lo, float hi) { return __builtin_bit_cast(unsigned, __builtin_convertvector(f32x2{lo, hi}, bf16x2)); }
__device__ __forceinline__ float silu(float x) { return x * __builtin_amdgcn_rcpf(1.f + __builtin_amdgcn_exp2f(-x * LOG2E)); }
__device__ __forceinline__ float dot2(unsigned a, unsigned b, float acc) {
  return __builtin_amdgcn_fdot2_f32_bf16(__builtin_bit_cast(bf16x2, a), __builtin_bit_cast(bf16x2, b), acc, false);
}
template <int CTRL> __device__ __forceinline__ float dpp(float x) {
  return __builtin_bit_cast(float, __builtin_amdgcn_mov_dpp(__builtin_bit_cast(int, x), CTRL, 0xf, 0xf, true));
}
template <int CTRL> __device__ __forceinline__ unsigned dppu(unsigned x) { return (unsigned)__builtin_amdgcn_mov_dpp((int)x, CTRL, 0xf, 0xf, true); }
#define MAX_DPP(x, ctrl) ({ float r_; asm("s_nop 1\n\tv_max_f32_dpp %0, %1, %1 " ctrl " row_mask:0xf bank_mask:0xf bound_ctrl:1" : "=v"(r_) : "v"(x)); r_; })
constexpr int XOR1 = 0xB1, XOR2 = 0x4E, XOR7 = 0x141, XOR8 = 0x128, ROR4 = 0x124, BCAST0 = 0x150;
__device__ __forceinline__ float swap32_sum(float a, float b) {
  auto t = __builtin_amdgcn_permlane32_swap(__float_as_uint(a), __float_as_uint(b), false, false);
  return __uint_as_float(t[0]) + __uint_as_float(t[1]);
}
__device__ __forceinline__ float swap16_sum(float a, float b) {
  auto t = __builtin_amdgcn_permlane16_swap(__float_as_uint(a), __float_as_uint(b), false, false);
  return __uint_as_float(t[0]) + __uint_as_float(t[1]);
}
__device__ __forceinline__ float xrow_max(float x) {
  auto s = __builtin_amdgcn_permlane16_swap(__float_as_uint(x), __float_as_uint(x), false, false);
  x = fmaxf(__uint_as_float(s[0]), __uint_as_float(s[1]));
  auto t = __builtin_amdgcn_permlane32_swap(__float_as_uint(x), __float_as_uint(x), false, false);
  return fmaxf(__uint_as_float(t[0]), __uint_as_float(t[1]));
}
__device__ __forceinline__ float xrow_sum(float x) { x = swap16_sum(x, x); return swap32_sum(x, x); }
__device__ __forceinline__ float wave_sum(float x) {
  x += dpp<XOR1>(x); x += dpp<XOR2>(x); x += dpp<XOR7>(x); x += dpp<XOR8>(x);
  return xrow_sum(x);
}
__device__ __forceinline__ void reduce_rows(const float* x, float& e0, float& e1) {
  const float y0 = swap32_sum(x[0], x[4]), y1 = swap32_sum(x[1], x[5]), y2 = swap32_sum(x[2], x[6]), y3 = swap32_sum(x[3], x[7]);
  e0 = swap16_sum(y0, y2); e1 = swap16_sum(y1, y3);
}
__device__ __forceinline__ float reduce_oct(const float* x, int lane) {
  float e0, e1; reduce_rows(x, e0, e1);
  const bool b3 = (lane & 8) != 0;
  return (b3 ? e1 : e0) + dpp<XOR8>(b3 ? e0 : e1);
}

__device__ __forceinline__ u64 ldg(const gu64* p) { return __hip_atomic_load(p, __ATOMIC_RELAXED, __HIP_MEMORY_SCOPE_AGENT); }
__device__ __forceinline__ void stg(gu64* p, u64 v) { __hip_atomic_store(p, v, __ATOMIC_RELAXED, __HIP_MEMORY_SCOPE_AGENT); }
__device__ __forceinline__ u64 gr_pair(unsigned tag, unsigned pay) { return ((u64)pay << 32) | tag; }
__device__ __forceinline__ u64 gr_f24(unsigned tag, float v) { return ((u64)((__float_as_uint(v) + 0x80u) & 0xffffff00u) << 32) | tag; }
__device__ __forceinline__ unsigned bad_pair(u64 g, unsigned tag) { return (unsigned)g ^ tag; }
__device__ __forceinline__ unsigned bad_f24(u64 g, unsigned tag) { return ((unsigned)g ^ tag) | ((unsigned)(g >> 32) & 0xffu); }
__device__ __forceinline__ float val_f24(u64 g) { return __uint_as_float((unsigned)(g >> 32)); }
__device__ __forceinline__ bool none_bad(unsigned bad) { return __builtin_amdgcn_ballot_w64(bad != 0u) == 0ull; }
__device__ __forceinline__ bool ok_pair(u64 g, unsigned tag) { return (unsigned)g == tag; }
__device__ __forceinline__ bool all_ok(bool ok) { return __builtin_amdgcn_ballot_w64(!ok) == 0ull; }
#define PINP(ptr) asm volatile("" : "+v"(ptr))

__device__ __forceinline__ void lds_wait(volatile LAS unsigned* w, unsigned want) {
#define LSLEEP 1
  for (unsigned n = 0; *w != want && n < SPIN_L; ++n) __builtin_amdgcn_s_sleep(LSLEEP);
  asm volatile("" ::: "memory");
}
#define WAIT_LGKM() asm volatile("s_waitcnt lgkmcnt(0)" ::: "memory")
#define BARRIER() do { asm volatile("s_waitcnt lgkmcnt(0)" ::: "memory"); __builtin_amdgcn_s_barrier(); asm volatile("" ::: "memory"); } while (0)

#define STAMP(i) do { } while (0)
#define STAMP_DUMP() do { } while (0)

struct AttnState { float m, l; float acc[8]; };

__device__ __forceinline__ void attn_tile(const v4u (&K)[4], const v4u (&V)[4], const v4u& q, AttnState& st, int c) {
  float x[4];
#pragma unroll
  for (int i = 0; i < 4; ++i) {
    float d = dot2(K[i].x, q.x, 0.f);
    d = dot2(K[i].y, q.y, d); d = dot2(K[i].z, q.z, d); d = dot2(K[i].w, q.w, d);
    x[i] = d;
  }
  const bool b3 = (c & 8) != 0, b2 = (c & 4) != 0;
  const float k0 = b3 ? x[2] : x[0], s0 = b3 ? x[0] : x[2];
  const float k1 = b3 ? x[3] : x[1], s1 = b3 ? x[1] : x[3];
  const float y0 = k0 + dpp<XOR8>(s0), y1 = k1 + dpp<XOR8>(s1);
  const float k2 = b2 ? y1 : y0, s2 = b2 ? y0 : y1;
  float z = k2 + dpp<XOR7>(s2);
  z += dpp<XOR2>(z); z += dpp<XOR1>(z);
  const float s = rbf(rbf(z) * 0.08838834764831845f);
  float pm = MAX_DPP(s, "row_ror:4"); pm = MAX_DPP(pm, "row_ror:8");
  const float mn = fmaxf(st.m, pm);
  if (__builtin_amdgcn_ballot_w64(mn > st.m) != 0ull) {
    const float al = __builtin_amdgcn_exp2f((st.m - mn) * LOG2E);
    st.l *= al;
#pragma unroll
    for (int j = 0; j < 8; ++j) st.acc[j] *= al;
    st.m = mn;
  }
  const float pr = __builtin_amdgcn_exp2f((s - st.m) * LOG2E);
  st.l += pr;
  const unsigned plo = f2bf(pr), phi = plo << 16;
#define PV(i) { const unsigned a = dppu<BCAST0 + 4 * i>(plo), b = dppu<BCAST0 + 4 * i>(phi); \
    st.acc[0] = dot2(V[i].x, a, st.acc[0]); st.acc[1] = dot2(V[i].x, b, st.acc[1]); st.acc[2] = dot2(V[i].y, a, st.acc[2]); st.acc[3] = dot2(V[i].y, b, st.acc[3]); \
    st.acc[4] = dot2(V[i].z, a, st.acc[4]); st.acc[5] = dot2(V[i].z, b, st.acc[5]); st.acc[6] = dot2(V[i].w, a, st.acc[6]); st.acc[7] = dot2(V[i].w, b, st.acc[7]); }
  PV(0) PV(1) PV(2) PV(3)
#undef PV
}

__device__ __forceinline__ void interleave(const v4u& r0, const v4u& r1, unsigned* o) {
#pragma unroll
  for (int j = 0; j < 4; ++j) { o[2 * j] = __builtin_amdgcn_perm(r1[j], r0[j], 0x05040100u); o[2 * j + 1] = __builtin_amdgcn_perm(r1[j], r0[j], 0x07060302u); }
}
__device__ __forceinline__ f32x4 mfma(const v4u& a, const v4u& b, const f32x4& c) {
  return __builtin_amdgcn_mfma_f32_16x16x32_bf16(__builtin_bit_cast(bf16x8, a), __builtin_bit_cast(bf16x8, b), c, 0, 0, 0);
}
__device__ __forceinline__ v4u tr_frag(const LAS unsigned char* p, int stride4) {
  const v2u lo = __builtin_bit_cast(v2u, __builtin_amdgcn_ds_read_tr16_b64_v4i16((LAS v4s*)p));
  const v2u hi = __builtin_bit_cast(v2u, __builtin_amdgcn_ds_read_tr16_b64_v4i16((LAS v4s*)(p + stride4)));
  return v4u{lo.x, lo.y, hi.x, hi.y};
}
#define GLDS(gsrc, ldst, aux) __builtin_amdgcn_global_load_lds((const GAS void*)(gsrc), (LAS void*)(ldst), 16, 0, aux)
__device__ __forceinline__ float sel4(int q4, float a, float b, float c, float d) { return q4 == 0 ? a : q4 == 1 ? b : q4 == 2 ? c : d; }
__device__ __forceinline__ v4u ldnt(const bf16_t* p) { return __builtin_nontemporal_load((const GAS v4u*)p); }
#define PIN8(a) asm volatile("" : "+v"((a)[0]), "+v"((a)[1]), "+v"((a)[2]), "+v"((a)[3]), "+v"((a)[4]), "+v"((a)[5]), "+v"((a)[6]), "+v"((a)[7]))

__global__ void __launch_bounds__(NTHR, 2) adaln_block_kernel(Params p) {
  extern __shared__ __attribute__((aligned(16))) unsigned lds_raw[];
  LAS unsigned* L = (LAS unsigned*)lds_raw;
  volatile LAS unsigned* LV = (volatile LAS unsigned*)lds_raw;
  LAS float* LF = (LAS float*)lds_raw;
  LAS unsigned char* LB = (LAS unsigned char*)lds_raw;
  u64 code_base;
  asm volatile("s_getpc_b64 %0" : "=s"(code_base));
  code_base &= ~255ull;
  const int tid = threadIdx.x, lane = tid & 63, wave = __builtin_amdgcn_readfirstlane(tid >> 6);
  const int r = lane >> 4, c = lane & 15;
  const int b = blockIdx.x, X = b & 7, J = b >> 3, sp = J & 7;
  const int head = 4 * X + (J >> 3);
  const bool fh = head < NFFN;
  const bool is_qc = sp < 2, is_ffn = fh && sp == 5;
  const int mu = (sp == 2 || sp == 3) ? 2 * head + sp - 2 : 999;
  const int own = head;
  const bool is_mod = mu < 60, is_own = sp == 4 && head < 10;
  const int cf = fh, cg = !fh && head < 30, ch_ = head >= 30;
  const int nq_ = cf * NT_Q + cg * NT_G0 + ch_ * NT_H0, nm_ = cf * NT_M + cg * NT_G1 + ch_ * NT_H1;
  const int np_ = cf * NT_P + cg * NT_G2 + ch_ * NT_H1, nf_ = cf * NT_F + cg * NT_G2 + ch_ * NT_H1;
  const int t0 = min(sp, 2) * nq_ + min(max(sp - 2, 0), 2) * nm_ + (int)(sp > 4) * np_ + (int)(sp > 5) * nf_ + (int)(sp > 6) * np_;
  const int nt = (int)(sp < 2) * nq_ + (int)(sp == 2 || sp == 3) * nm_ + (int)(sp == 5) * nf_ + (int)(sp == 4 || sp > 5) * np_;
  if (tid < 16) L[tid] = 0u;
  asm volatile("" :: "s"(p.kc), "s"(p.vc), "s"(p.ws), "s"(p.wq), "s"(p.wmod), "s"(p.c), "s"(p.x), "s"(t0), "s"(nt));
  BARRIER();
  gu64* const XG = (gu64*)(p.ws + WS_XG); gu64* const MG = (gu64*)(p.ws + WS_MG); gu64* const YG = (gu64*)(p.ws + WS_YG);
  gu64* const DG = (gu64*)(p.ws + WS_DG); gu64* const QG = (gu64*)(p.ws + WS_QG); gu64* const PG = (gu64*)(p.ws + WS_PG);

  if (__builtin_expect(wave < 4, 1)) {
    const size_t base = ((size_t)head * SK + (size_t)t0 * 16) * DH;
    const __amdgpu_buffer_rsrc_t rk = __builtin_amdgcn_make_buffer_rsrc((void*)(p.kc + base), 0, nt * 4096, 0x00020000);
    const __amdgpu_buffer_rsrc_t rv = __builtin_amdgcn_make_buffer_rsrc((void*)(p.vc + base), 0, nt * 4096, 0x00020000);
    const int voff = lane * 16;
    const int nw = (nt - wave + 3) >> 2;
    v4u K[TD][4], V[TD][4];
#define ISSUE(d, t) do { const int so_ = (wave + 4 * (t)) * 4096; \
    _Pragma("unroll") for (int i_ = 0; i_ < 4; ++i_) K[d][i_] = __builtin_amdgcn_raw_buffer_load_b128(rk, voff + 1024 * i_, so_, 2); \
    _Pragma("unroll") for (int i_ = 0; i_ < 4; ++i_) V[d][i_] = __builtin_amdgcn_raw_buffer_load_b128(rv, voff + 1024 * i_, so_, 2); \
    __builtin_amdgcn_sched_barrier(0); } while (0)
#define PACE_WAIT(n) do { asm volatile("s_waitcnt vmcnt(%0)" :: "n"(n) : "memory"); __builtin_amdgcn_sched_barrier(0); } while (0)
#define QGATE 1
    if (QGATE == 1 && is_qc) lds_wait(LV + L_FLAG_H, 1u);
    if (QGATE == 2 && is_qc) lds_wait(LV + L_CNT_Q, 4u);
    if (is_mod) lds_wait(LV + L_CNT_G, 1u);
    if (KVDELAY) __builtin_amdgcn_s_sleep(KVDELAY);
    if (wave == 0) STAMP(24);
    ISSUE(0, 0);
#pragma unroll
    for (int d = 1; d < TD; ++d) { PACE_WAIT(PACE0); ISSUE(d, d); }
    const unsigned ov = (unsigned)(size_t)(LB + B_WD + wave * 16384);
#define DMA4(m0v, rsrc, so) do { unsigned keep_; asm volatile("s_mov_b32 %0, m0\n\ts_mov_b32 m0, %1\n\ts_nop 0\n\t" \
      "buffer_load_dwordx4 %2, %3, %4 offen nt lds\n\tbuffer_load_dwordx4 %2, %3, %4 offen offset:1024 nt lds\n\t" \
      "buffer_load_dwordx4 %2, %3, %4 offen offset:2048 nt lds\n\tbuffer_load_dwordx4 %2, %3, %4 offen offset:3072 nt lds\n\t" \
      "s_mov_b32 m0, %0" : "=&s"(keep_) : "s"(m0v), "v"(voff), "s"(rsrc), "s"(so) : "memory"); } while (0)
#define DMA_TILE(e, t) do { const int so_ = (wave + 4 * (t)) * 4096; DMA4(ov + (e) * 8192, rk, so_); DMA4(ov + (e) * 8192 + 4096, rv, so_); __builtin_amdgcn_sched_barrier(0); } while (0)
#define LD_OV(d, e) do { const LAS v4u* src_ = (const LAS v4u*)(LB + B_WD + wave * 16384 + (e) * 8192) + lane; \
      _Pragma("unroll") for (int i_ = 0; i_ < 4; ++i_) { K[d][i_] = src_[64 * i_]; V[d][i_] = src_[256 + 64 * i_]; } __builtin_amdgcn_sched_barrier(0); } while (0)
    PACE_WAIT(PACE0); DMA_TILE(0, 6);
    PACE_WAIT(PACE0); DMA_TILE(1, 7);
    lds_wait(LV + L_FLAG_Q, 1u);
    if (wave == 0) STAMP(1);
    const v4u q = *(const LAS v4u*)(L + L_Q + 4 * c);
    const unsigned tag = L[L_E] + 1u;
    AttnState st; st.m = -INFINITY; st.l = 0.f;
#pragma unroll
    for (int j = 0; j < 8; ++j) st.acc[j] = 0.f;
#define TILE(d) do { attn_tile(K[d], V[d], q, st, c); __builtin_amdgcn_sched_barrier(0); } while (0)
    static_assert(TD == 6, "the catch-up schedule is written for six slots");
    TILE(0); PACE_WAIT(0); ISSUE(0, 8);
    TILE(1); TILE(2); TILE(3); TILE(4);
    LD_OV(4, 0);
    PACE_WAIT(PACE); ISSUE(1, 9);
    TILE(5);
    LD_OV(5, 1);
    WAIT_LGKM();
    if (lane == 0) __hip_atomic_fetch_add((LAS unsigned*)(L + L_CNT_OV), 1u, __ATOMIC_RELAXED, __HIP_MEMORY_SCOPE_WORKGROUP);
    TILE(4); TILE(5);
    TILE(0);
#pragma nounroll
    for (int u = 9; u < nw; u += TD) {
#pragma unroll
      for (int k = 0; k < TD; ++k) {
        if (u + k < nw) {
          PACE_WAIT(PACE);
          ISSUE((k + 2) % TD, u + k + 1);
          if (u + k + 1 + WOLEAD == nw && wave == 0 && lane == 0) LV[L_FLAG_L] = 1u;
          TILE((k + 1) % TD);
        }
      }
    }
#undef TILE
    if (wave == 0 && lane == 0) LV[L_FLAG_L] = 1u;
#undef ISSUE
    if (wave == 0) STAMP(2);
    const float M = xrow_max(st.m);
    const float sc = __builtin_amdgcn_exp2f((st.m - M) * LOG2E);
    float l = ((c & 3) == 0) ? st.l * sc : 0.f;
    l += dpp<ROR4>(l); l += dpp<XOR8>(l); l = xrow_sum(l);
#pragma unroll
    for (int j = 0; j < 8; ++j) st.acc[j] *= sc;
    float e0, e1; reduce_rows(st.acc, e0, e1);
    LAS float* rec = LF + L_REC + wave * 132;
    *(LAS f32x2*)(rec + 4 + 8 * c + 2 * r) = f32x2{e0, e1};
    if (lane == 0) { rec[0] = M; rec[1] = l; }
    WAIT_LGKM();
    unsigned old = 0;
    if (lane == 0) old = __hip_atomic_fetch_add((LAS unsigned*)(L + L_CNT_S1), 1u, __ATOMIC_RELAXED, __HIP_MEMORY_SCOPE_WORKGROUP);
    old = __builtin_amdgcn_readfirstlane(old);
    if (old == 3u) {
      asm volatile("" ::: "memory");
      const LAS float* R = LF + L_REC;
      const float m0 = R[0], m1 = R[132], m2 = R[264], m3 = R[396];
      const float MM = fmaxf(fmaxf(m0, m1), fmaxf(m2, m3));
      const float w0 = __builtin_amdgcn_exp2f((m0 - MM) * LOG2E), w1 = __builtin_amdgcn_exp2f((m1 - MM) * LOG2E);
      const float w2 = __builtin_amdgcn_exp2f((m2 - MM) * LOG2E), w3 = __builtin_amdgcn_exp2f((m3 - MM) * LOG2E);
      const float LL = (w0 * R[1] + w1 * R[133]) + (w2 * R[265] + w3 * R[397]);
      const float oa = (w0 * R[4 + lane] + w1 * R[136 + lane]) + (w2 * R[268 + lane] + w3 * R[400 + lane]);
      const float ob = (w0 * R[68 + lane] + w1 * R[200 + lane]) + (w2 * R[332 + lane] + w3 * R[464 + lane]);
      gu64* rp = PG + (size_t)(head * 8 + sp) * PG_STRIDE;
      stg(rp + lane, gr_f24(tag, oa));
      stg(rp + 64 + lane, gr_f24(tag, ob));
      if (lane < 2) stg(rp + 128 + lane, gr_f24(tag, lane == 0 ? MM : LL));
      if (lane == 0) LV[L_FLAG_S1] = 1u;
      STAMP(3);
    }
    return;
  }

  const int v = wave - 4, t = tid - 256;
  const int r8 = lane >> 3, c8 = lane & 7;
  unsigned tag = 0;
  if (is_qc) {
    const unsigned e_in = __hip_atomic_load((gu32*)(p.ws + WS_EPOCH), __ATOMIC_RELAXED, __HIP_MEMORY_SCOPE_AGENT);
    v4u w[20];
    {
      const bf16_t* src = p.wq + (size_t)(160 * v + 2 * r8) * (HQ * DH) + head * DH + 64 * sp + 8 * c8;
#pragma unroll
      for (int i = 0; i < 10; ++i) { w[2 * i] = ldnt(src + (size_t)(16 * i) * (HQ * DH)); w[2 * i + 1] = ldnt(src + (size_t)(16 * i + 1) * (HQ * DH)); }
    }
    unsigned xs[5];
#pragma unroll
    for (int i = 0; i < 5; ++i) xs[i] = ((const GAS unsigned*)p.x)[lane + 64 * i];
    const unsigned xo0 = ((const GAS unsigned*)p.x)[80 * v + lane], xo1 = ((const GAS unsigned*)p.x)[80 * v + 64 + (lane & 15)];
    __builtin_amdgcn_sched_barrier(0);
    if (v == 0 && lane == 0) { LV[L_E] = e_in; WAIT_LGKM(); LV[L_FLAG_E] = 1u; }
    float n0a, n0b, n1a, n1b;
    {
      float s = 0.f;
#pragma unroll
      for (int i = 0; i < 5; ++i) s += bflo(xs[i]) + bfhi(xs[i]);
      const float mean_f = wave_sum(s) * (1.f / D);
      float vs = 0.f;
#pragma unroll
      for (int i = 0; i < 5; ++i) { const float d0 = bflo(xs[i]) - mean_f, d1 = bfhi(xs[i]) - mean_f; vs = fmaf(d0, d0, vs); vs = fmaf(d1, d1, vs); }
      const float var = rbf(wave_sum(vs) * (1.f / D)), mean = rbf(mean_f);
      const float rs = __builtin_amdgcn_rcpf(rbf(__builtin_amdgcn_sqrtf(rbf(var + 1e-5f))));
      n0a = rbf(rbf(bflo(xo0) - mean) * rs); n0b = rbf(rbf(bfhi(xo0) - mean) * rs);
      n1a = rbf(rbf(bflo(xo1) - mean) * rs); n1b = rbf(rbf(bfhi(xo1) - mean) * rs);
      asm volatile("" : "+v"(n0a), "+v"(n0b), "+v"(n1a), "+v"(n1b));
    }
    unsigned wi[10][8];
#pragma unroll
    for (int i = 0; i < 10; ++i) { interleave(w[2 * i], w[2 * i + 1], wi[i]); PIN8(wi[i]); }
    if (lane == 0) __hip_atomic_fetch_add((LAS unsigned*)(L + L_CNT_Q), 1u, __ATOMIC_RELAXED, __HIP_MEMORY_SCOPE_WORKGROUP);
    lds_wait(LV + L_FLAG_E, 1u);
    tag = LV[L_E] + 1u;
    if (v == 0) STAMP(18);
    unsigned s0, c0, s1, c1;
    {
      const gu64* m0 = MG + 80 * v + lane; const gu64* m1 = MG + 80 * v + 64 + (lane & 15);
      for (unsigned n = 0; n < SPIN_G; ++n) {
        const u64 a0 = ldg(m0), b0 = ldg(m0 + 320), a1 = ldg(m1), b1 = ldg(m1 + 320);
        s0 = (unsigned)(a0 >> 32); c0 = (unsigned)(b0 >> 32); s1 = (unsigned)(a1 >> 32); c1 = (unsigned)(b1 >> 32);
        if (all_ok(ok_pair(a0, tag) & ok_pair(b0, tag) & ok_pair(a1, tag) & ok_pair(b1, tag))) break;
      }
    }
    if (v == 0) STAMP(5);
    L[L_H2 + 80 * v + lane] = f2bf(rbf(n0a * rbf(1.f + bflo(c0))) + bflo(s0)) | (f2bf(rbf(n0b * rbf(1.f + bfhi(c0))) + bfhi(s0)) << 16);
    if (lane < 16) L[L_H2 + 80 * v + 64 + lane] = f2bf(rbf(n1a * rbf(1.f + bflo(c1))) + bflo(s1)) | (f2bf(rbf(n1b * rbf(1.f + bfhi(c1))) + bfhi(s1)) << 16);
    WAIT_LGKM();
    float acc[8];
#pragma unroll
    for (int j = 0; j < 8; ++j) acc[j] = 0.f;
#pragma unroll
    for (int i = 0; i < 10; ++i) {
      const unsigned hp = L[L_H2 + 80 * v + 8 * i + r8];
#pragma unroll
      for (int j = 0; j < 8; ++j) acc[j] = dot2(wi[i][j], hp, acc[j]);
    }
    LF[L_MR + 64 * v + 8 * c8 + r8] = reduce_oct(acc, lane);
    WAIT_LGKM();
    unsigned old = 0;
    if (lane == 0) old = __hip_atomic_fetch_add((LAS unsigned*)(L + L_CNT_M), 1u, __ATOMIC_RELAXED, __HIP_MEMORY_SCOPE_WORKGROUP);
    if (__builtin_amdgcn_readfirstlane(old) == 3u) {
      asm volatile("" ::: "memory");
      const unsigned qb = f2bf((LF[L_MR + lane] + LF[L_MR + 64 + lane]) + (LF[L_MR + 128 + lane] + LF[L_MR + 192 + lane]));
      const unsigned up = dppu<XOR1>(qb);
      if ((lane & 1) == 0) stg(QG + head * 64 + 32 * sp + (lane >> 1), gr_pair(tag, qb | (up << 16)));
      if (lane == 0) LV[L_FLAG_H] = 1u;
      STAMP(6);
    }
  } else {
    const unsigned e_in = __hip_atomic_load((gu32*)(p.ws + WS_EPOCH), __ATOMIC_RELAXED, __HIP_MEMORY_SCOPE_AGENT);
    const __amdgpu_buffer_rsrc_t rm = __builtin_amdgcn_make_buffer_rsrc((void*)p.wmod, 0, is_mod ? D * NMOD * 2 : 0, 0x00020000);
    const __amdgpu_buffer_rsrc_t rc = __builtin_amdgcn_make_buffer_rsrc((void*)p.c, 0, is_mod ? D * 2 : 0, 0x00020000);
    const unsigned cv0 = __builtin_amdgcn_raw_buffer_load_b16(rc, (160 * v + lane) * 2, 0, 0), cv1 = __builtin_amdgcn_raw_buffer_load_b16(rc, (160 * v + 64 + lane) * 2, 0, 0),
                   cv2 = __builtin_amdgcn_raw_buffer_load_b16(rc, (160 * v + 128 + (lane & 31)) * 2, 0, 0);
    v4u wm[20];
#pragma unroll
    for (int i = 0; i < 20; ++i) wm[i] = __builtin_amdgcn_raw_buffer_load_b128(rm, ((160 * v + r8) * NMOD + 64 * mu + 8 * c8) * 2, i * (8 * NMOD * 2), 2);
    __builtin_amdgcn_sched_barrier(0);
    if (v == 0 && lane == 0) { LV[L_E] = e_in; WAIT_LGKM(); LV[L_FLAG_E] = 1u; }
    if (is_mod) {
      L[L_SC + 160 * v + lane] = f2bf(silu(bf2f(cv0))); L[L_SC + 160 * v + 64 + lane] = f2bf(silu(bf2f(cv1)));
      if (lane < 32) L[L_SC + 160 * v + 128 + lane] = f2bf(silu(bf2f(cv2)));
      WAIT_LGKM();
      float acc[8];
#pragma unroll
      for (int j = 0; j < 8; ++j) acc[j] = 0.f;
#pragma unroll
      for (int i = 0; i < 20; ++i) {
        const unsigned a = L[L_SC + 160 * v + 8 * i + r8], bb = a << 16;
#pragma unroll
        for (int j = 0; j < 4; ++j) { acc[2 * j] = dot2(wm[i][j], a, acc[2 * j]); acc[2 * j + 1] = dot2(wm[i][j], bb, acc[2 * j + 1]); }
      }
      LF[L_MR + 64 * v + 8 * c8 + r8] = reduce_oct(acc, lane);
      WAIT_LGKM();
      unsigned old = 0;
      if (lane == 0) old = __hip_atomic_fetch_add((LAS unsigned*)(L + L_CNT_M), 1u, __ATOMIC_RELAXED, __HIP_MEMORY_SCOPE_WORKGROUP);
      if (__builtin_amdgcn_readfirstlane(old) == 3u) {
        asm volatile("" ::: "memory");
        const unsigned mb_ = f2bf((LF[L_MR + lane] + LF[L_MR + 64 + lane]) + (LF[L_MR + 128 + lane] + LF[L_MR + 192 + lane]));
        const unsigned up = dppu<XOR1>(mb_);
        lds_wait(LV + L_FLAG_E, 1u);
        if ((lane & 1) == 0) stg(MG + 32 * mu + (lane >> 1), gr_pair(LV[L_E] + 1u, mb_ | (up << 16)));
        if (lane == 0) LV[L_CNT_G] = 1u;
        STAMP(4);
      }
    }
    if (v == 3 && J * 1024 < CODE_BYTES) {
      const v4u cw = ((const GAS v4u*)(code_base + (u64)(J * 1024)))[lane]; asm volatile("" :: "v"(cw));
    }
  }
  if (v == 2) {
      const int k = lane & 15;
      const GAS unsigned* a = k == 0 ? (const GAS unsigned*)(p.wo + (size_t)(head * DH) * D + 80 * sp)
                            : k == 1 ? (const GAS unsigned*)(p.wo + (size_t)(head * DH + DH - 1) * D + 80 * sp)
                            : k == 2 ? (const GAS unsigned*)p.out
                            : k == 3 ? (const GAS unsigned*)(p.ws + WS_PG + (size_t)(head * 8) * PG_STRIDE * 8)
                            : k == 4 ? (const GAS unsigned*)(p.ws + WS_YG + (size_t)(5 * sp) * 4096)
                            : k == 5 ? (const GAS unsigned*)(p.ws + WS_DG)
                            : k == 6 ? (const GAS unsigned*)(p.ws + WS_DG + 131072)
                            : k == 7 ? (const GAS unsigned*)(p.ws + WS_XG)
                            : k == 8 ? (const GAS unsigned*)(p.kc + ((size_t)head * SK + (size_t)(t0 + nt) * 16 - 1) * DH)
                            : k == 9 ? (const GAS unsigned*)(p.vc + ((size_t)head * SK + (size_t)(t0 + nt) * 16 - 1) * DH)
                            : (const GAS unsigned*)(p.ws + WS_QG);
      const unsigned w_ = *a; asm volatile("" :: "v"(w_));
  }
  if (v == 1) {
    lds_wait(LV + L_FLAG_E, 1u);
    tag = LV[L_E] + 1u;
    const gu64* qp = QG + head * 64 + lane;
    if (!is_qc) __builtin_amdgcn_s_sleep(QDELAY);
    u64 g0 = ldg(qp); __builtin_amdgcn_s_sleep(STAG);
    u64 g1 = ldg(qp); __builtin_amdgcn_s_sleep(STAG);
    u64 g2 = ldg(qp), g;
    for (unsigned n = 0;; ++n) {
      if (all_ok(ok_pair(g0, tag)) || n >= SPIN_G) { g = g0; break; }
      g0 = ldg(qp);
      if (all_ok(ok_pair(g1, tag))) { g = g1; break; }
      g1 = ldg(qp);
      if (all_ok(ok_pair(g2, tag))) { g = g2; break; }
      g2 = ldg(qp);
    }
    L[L_Q + lane] = (unsigned)(g >> 32);
    WAIT_LGKM();
    if (lane == 0) LV[L_FLAG_Q] = 1u;
    STAMP(7);
  }
  lds_wait(LV + L_FLAG_E, 1u);
  tag = LV[L_E] + 1u;

  const int g4 = lane >> 4, tq = (lane & 15) >> 2, tp = lane & 3;
  v4u wf[5][8];
  unsigned mbA[2], mbB[2];
  if (is_ffn) {
#define FPACE 63
    v4u raw[5][8];
    {
      const size_t ro = (size_t)(160 * v + r8) * F + 64 * head + 8 * c8;
#pragma unroll
      for (int kb = 0; kb < 5; ++kb) {
        if (FPACE < 63) asm volatile("s_waitcnt vmcnt(%0)" :: "n"(FPACE) : "memory");
#pragma unroll
        for (int i = 0; i < 4; ++i) { raw[kb][i] = ldnt(p.wg + ro + (size_t)(32 * kb + 8 * i) * F); raw[kb][4 + i] = ldnt(p.wu + ro + (size_t)(32 * kb + 8 * i) * F); }
        __builtin_amdgcn_sched_barrier(0);
      }
    }
    __builtin_amdgcn_sched_barrier(0);
    LAS unsigned char* stg_w = LB + B_STG + v * 4096 + r8 * 128 + c8 * 16;
    const LAS unsigned char* stg_r = LB + B_STG + v * 4096 + (8 * g4 + tq) * 128 + tp * 8;
#pragma unroll
    for (int kb = 0; kb < 5; ++kb)
#pragma unroll
      for (int m = 0; m < 2; ++m) {
#pragma unroll
        for (int i = 0; i < 4; ++i) *(LAS v4u*)(stg_w + i * 1024) = raw[kb][4 * m + i];
        asm volatile("" ::: "memory");
#pragma unroll
        for (int cg = 0; cg < 4; ++cg) wf[kb][4 * m + cg] = tr_frag(stg_r + cg * 32, 512);
        asm volatile("s_waitcnt lgkmcnt(0)" ::: "memory");
      }
    if (v == 0) STAMP(21);
    if (v == 1) STAMP(18);
    lds_wait(LV + L_CNT_OV, 4u);
    if (v == 0) STAMP(22);
#pragma unroll
    for (int i = 0; i < 20; ++i) {
      const int idx = 64 * i + lane, row = idx / 20, ch = idx - 20 * row;
      GLDS(p.wd + (size_t)(64 * head + row) * D + 160 * v + 8 * ch, LB + B_WD + v * 20480 + 1024 * i, 2);
    }
    {
      const gu64* m0 = MG + 80 * v + lane; const gu64* m1 = MG + 80 * v + 64 + (lane & 15);
      for (unsigned n = 0; n < SPIN_G; ++n) {
        const u64 s0 = ldg(m0 + 960), c0 = ldg(m0 + 1280), s1 = ldg(m1 + 960), c1 = ldg(m1 + 1280);
        const unsigned sp0 = (unsigned)(s0 >> 32), cp0 = (unsigned)(c0 >> 32), sp1 = (unsigned)(s1 >> 32), cp1 = (unsigned)(c1 >> 32);
        mbA[0] = (sp0 & 0xffffu) | (f2bf(1.f + bflo(cp0)) << 16); mbB[0] = (sp0 >> 16) | (f2bf(1.f + bfhi(cp0)) << 16);
        mbA[1] = (sp1 & 0xffffu) | (f2bf(1.f + bflo(cp1)) << 16); mbB[1] = (sp1 >> 16) | (f2bf(1.f + bfhi(cp1)) << 16);
        if (all_ok(ok_pair(s0, tag) & ok_pair(c0, tag) & ok_pair(s1, tag) & ok_pair(c1, tag))) break;
        __builtin_amdgcn_s_sleep(8);
      }
    }
  }
  float own_x = 0.f, own_ga = 0.f, own_gb = 0.f;
  if (is_own) {
    const int col = 64 * own + 16 * v + c;
    const gu64* m = MG + (lane < 32 ? 640 : 1600) + (col >> 1);
    own_x = bf2f(((const GAS bf16_t*)p.x)[col]);
    unsigned g = 0;
    for (unsigned n = SPIN_G; n != 0u; --n) { const u64 a = ldg(m); g = (unsigned)(a >> 32); if (none_bad(bad_pair(a, tag))) break; __builtin_amdgcn_s_sleep(8); }
    const float gv = (c & 1) ? bfhi(g) : bflo(g);
    const float ga_h = lane < 32 ? gv : 0.f, gb_h = lane < 32 ? 0.f : gv;
    own_ga = swap32_sum(ga_h, ga_h); own_gb = swap32_sum(gb_h, gb_h);
  }
  lds_wait(LV + L_FLAG_L, 1u);
#pragma unroll
  for (int i = 0; i < 5; ++i) {
    const int idx = 256 * i + t, row = idx / 10, ch = idx - 10 * row;
    GLDS(p.wo + (size_t)(head * DH + row) * D + 80 * sp + 8 * ch, LB + B_WO + (256 * i + 64 * v) * 16, 0);
  }
  asm volatile("s_waitcnt vmcnt(0)" ::: "memory");
  if (lane == 0) __hip_atomic_fetch_add((LAS unsigned*)(L + L_CNT_W), 1u, __ATOMIC_RELAXED, __HIP_MEMORY_SCOPE_WORKGROUP);
  lds_wait(LV + L_CNT_W, 4u);
  v4u wof[4], wof4[4];
  {
    const LAS unsigned char* src = LB + B_WO + (8 * g4 + tq) * 160 + tp * 8;
#pragma unroll
    for (int kb = 0; kb < 4; ++kb) { wof[kb] = tr_frag(src + kb * 32 * 160 + v * 32, 4 * 160); wof4[kb] = tr_frag(src + kb * 32 * 160 + 4 * 32, 4 * 160); }
  }
  if (v == 0) STAMP(8);

  const int hf = lane >> 5;
  const gu64* hop = PG + (size_t)(head * 8 + hf) * PG_STRIDE + 32 * v + (lane & 31);
  const gu64* hmp = PG + (size_t)(head * 8 + (((lane & 15) >> 1) ^ hf)) * PG_STRIDE + 128 + (lane & 1);
  gu64* ysp = YG + (size_t)(5 * sp + (lane < 16 ? v : 4)) * 512 + head * 16 + c;
  LAS bf16_t* odst = (LAS bf16_t*)(L + L_O) + 32 * v + (lane & 31);
  PINP(hop); PINP(hmp); PINP(ysp);
  lds_wait(LV + L_FLAG_S1, 1u);
  if (v == 0) STAMP(9);
  {
    u64 g0, g1, g2, g3, gm;
    for (unsigned n = SPIN_G; n != 0u; --n) {
      g0 = ldg(hop); g1 = ldg(hop + 2 * PG_STRIDE); g2 = ldg(hop + 4 * PG_STRIDE); g3 = ldg(hop + 6 * PG_STRIDE); gm = ldg(hmp);
      if (none_bad((bad_f24(g0, tag) | bad_f24(g1, tag) | bad_f24(g2, tag)) | (bad_f24(g3, tag) | bad_f24(gm, tag)))) break;
      if (PSLEEP) __builtin_amdgcn_s_sleep(PSLEEP);
    }
    const float ml = val_f24(gm);
    const float mm = (lane & 1) ? -INFINITY : ml;
    float M = MAX_DPP(mm, "quad_perm:[2,3,0,1]"); M = MAX_DPP(M, "row_half_mirror"); M = MAX_DPP(M, "row_ror:8"); M = MAX_DPP(M, "quad_perm:[1,0,3,2]");
    const float wgt = __builtin_amdgcn_exp2f((mm - M) * LOG2E);
    const float wn = dpp<XOR1>(wgt);
    const float wl = (lane & 1) ? wn * ml : 0.f;
    float Ls = wl + dpp<XOR1>(wl); Ls += dpp<XOR2>(Ls); Ls += dpp<XOR7>(Ls); Ls += dpp<XOR8>(Ls);
    float acc = dpp<BCAST0>(wgt) * val_f24(g0);
    acc = fmaf(dpp<BCAST0 + 4>(wgt), val_f24(g1), acc); acc = fmaf(dpp<BCAST0 + 8>(wgt), val_f24(g2), acc); acc = fmaf(dpp<BCAST0 + 12>(wgt), val_f24(g3), acc);
    acc = swap32_sum(acc, acc);
    *odst = (bf16_t)f2bf(acc * __builtin_amdgcn_rcpf(Ls));
  }
  BARRIER();
  if (v == 0) STAMP(10);
  {
    v4u A[4];
#pragma unroll
    for (int kb = 0; kb < 4; ++kb) A[kb] = *(const LAS v4u*)(L + L_O + 16 * kb + 4 * g4);
    f32x4 y = {0.f, 0.f, 0.f, 0.f}, y4 = {0.f, 0.f, 0.f, 0.f};
#pragma unroll
    for (int kb = 0; kb < 4; ++kb) y = mfma(A[kb], wof[kb], y);
    if (v == 0) {
#pragma unroll
      for (int kb = 0; kb < 4; ++kb) y4 = mfma(A[kb], wof4[kb], y4);
    }
    if (lane < 16 || (v == 0 && lane < 32)) stg(ysp, gr_f24(tag, lane < 16 ? y[0] : y4[0]));
  }
  if (v == 0) STAMP(11);
  if (is_own) {
    const int og = 4 * own + v;
    const gu64* yp = YG + (size_t)og * 512 + lane;
    const gu64* dp = DG + (size_t)og * 512 + lane;
    const gu64* dp6 = dp + (lane < 48 ? 384 : 0);
    gu64* xp = XG + 16 * og + (c >> 1);
    GAS bf16_t* op = (GAS bf16_t*)p.out + 16 * og + c;
    PINP(yp); PINP(dp); PINP(dp6); PINP(xp); PINP(op);
    u64 g[8];
    for (unsigned n = SPIN_G; n != 0u; --n) {
      unsigned bad = 0;
#pragma unroll
      for (int i = 0; i < 8; ++i) { g[i] = ldg(yp + 64 * i); bad |= bad_f24(g[i], tag); }
      if (none_bad(bad)) break;
      if (PSLEEP) __builtin_amdgcn_s_sleep(PSLEEP);
    }
    if (v == 0) STAMP(30);
    float s = ((val_f24(g[0]) + val_f24(g[1])) + (val_f24(g[2]) + val_f24(g[3]))) + ((val_f24(g[4]) + val_f24(g[5])) + (val_f24(g[6]) + val_f24(g[7])));
    s = xrow_sum(s);
    const float x1 = rbf(own_x + rbf(own_ga * rbf(s)));
    const unsigned xb = f2bf(x1), pair = xb | (dppu<XOR1>(xb) << 16);
    if (lane < 16 && (lane & 1) == 0) stg(xp, gr_pair(tag, pair));
    if (v == 0) STAMP(12);
    for (unsigned n = SPIN_G; n != 0u; --n) {
      unsigned bad = 0;
#pragma unroll
      for (int i = 0; i < 6; ++i) { g[i] = ldg(dp + 64 * i); bad |= bad_f24(g[i], tag); }
      g[6] = ldg(dp6); bad |= bad_f24(g[6], tag);
      if (none_bad(bad)) break;
      if (PSLEEP) __builtin_amdgcn_s_sleep(PSLEEP);
    }
    if (v == 0) STAMP(15);
    s = ((val_f24(g[0]) + val_f24(g[1])) + (val_f24(g[2]) + val_f24(g[3]))) + ((val_f24(g[4]) + val_f24(g[5])) + (lane < 48 ? val_f24(g[6]) : 0.f));
    s = xrow_sum(s);
    if (lane < 16) *op = (bf16_t)f2bf(x1 + rbf(own_gb * rbf(s)));
    if (og == 0 && lane == 0) __hip_atomic_store((gu32*)(p.ws + WS_EPOCH), tag, __ATOMIC_RELAXED, __HIP_MEMORY_SCOPE_AGENT);
    if (v == 0) STAMP(16);
  }
  if (is_ffn) {
    const int ua = 80 * v + lane, ub = 80 * v + 64 + (lane & 15);
    const gu64* xa = XG + (ua >> 3) * 16 + (ua & 7); const gu64* xb = XG + (ub >> 3) * 16 + (ub & 7);
    gu64* dq = DG + (size_t)(10 * v + g4) * 512 + head * 16 + c;
    PINP(xa); PINP(xb); PINP(dq);
    unsigned xo0, xo1;
    for (unsigned n = SPIN_G; n != 0u; --n) {
      const u64 g0 = ldg(xa), g1 = ldg(xb);
      xo0 = (unsigned)(g0 >> 32); xo1 = (unsigned)(g1 >> 32);
      if (none_bad(bad_pair(g0, tag) | bad_pair(g1, tag))) break;
      if (PSLEEP) __builtin_amdgcn_s_sleep(PSLEEP);
    }
    if (v == 0) STAMP(13);
    {
      const unsigned xm = lane < 16 ? xo1 : 0u;
      float s1 = dot2(xm, 0x3f803f80u, dot2(xo0, 0x3f803f80u, 0.f)), s2 = dot2(xm, xm, dot2(xo0, xo0, 0.f));
      s1 += dpp<XOR1>(s1); s2 += dpp<XOR1>(s2); s1 += dpp<XOR2>(s1); s2 += dpp<XOR2>(s2);
      s1 += dpp<XOR7>(s1); s2 += dpp<XOR7>(s2); s1 += dpp<XOR8>(s1); s2 += dpp<XOR8>(s2);
      s1 = xrow_sum(s1); s2 = xrow_sum(s2);
      if (lane == 0) *(LAS f32x2*)(LF + L_ST + 2 * v) = f32x2{s1, s2};
    }
    if (v == 0) STAMP(25);
    BARRIER();
    if (v == 0) STAMP(26);
    {
      const f32x4 sa = *(const LAS f32x4*)(LF + L_ST), sb = *(const LAS f32x4*)(LF + L_ST + 4);
      const float mean_f = ((sa[0] + sa[2]) + (sb[0] + sb[2])) * (1.f / D);
      const float ex2 = ((sa[1] + sa[3]) + (sb[1] + sb[3])) * (1.f / D);
      float var_f = fmaf(-mean_f, mean_f, ex2);
      if (__builtin_amdgcn_ballot_w64(var_f * 256.f < ex2) != 0ull) {
        float vs = 0.f;
#pragma unroll
        for (int i = 0; i < 5; ++i) { const int u_ = lane + 64 * i; const unsigned xp = (unsigned)(ldg(XG + (u_ >> 3) * 16 + (u_ & 7)) >> 32); const float d0 = bflo(xp) - mean_f, d1 = bfhi(xp) - mean_f; vs = fmaf(d0, d0, vs); vs = fmaf(d1, d1, vs); }
        var_f = wave_sum(vs) * (1.f / D);
      }
      const float var = rbf(var_f), mean = rbf(mean_f);
      const float rs = __builtin_amdgcn_rcpf(rbf(__builtin_amdgcn_sqrtf(rbf(var + 1e-5f))));
#define LNPAIR(xp, ma, mb_) ({ const unsigned d_ = pk2(bflo(xp) - mean, bfhi(xp) - mean); const unsigned n_ = pk2(bflo(d_) * rs, bfhi(d_) * rs); \
        const unsigned m_ = pk2(bflo(n_) * bfhi(ma), bfhi(n_) * bfhi(mb_)); pk2(bflo(m_) + bflo(ma), bfhi(m_) + bflo(mb_)); })
      L[L_H2 + 80 * v + lane] = LNPAIR(xo0, mbA[0], mbB[0]);
      const unsigned h1 = LNPAIR(xo1, mbA[1], mbB[1]);
      if (lane < 16) L[L_H2 + 80 * v + 64 + lane] = h1;
#undef LNPAIR
    }
    if (v == 0) STAMP(27);
    v4u wd[2][10];
    {
      v4u A[5];
#pragma unroll
      for (int kb = 0; kb < 5; ++kb) A[kb] = *(const LAS v4u*)(L + L_H2 + 80 * v + 16 * kb + 4 * g4);
      f32x4 acc[8];
#pragma unroll
      for (int cg = 0; cg < 8; ++cg) acc[cg] = f32x4{0.f, 0.f, 0.f, 0.f};
      const LAS unsigned char* src = LB + B_WD + v * 20480 + (8 * g4 + tq) * 320 + tp * 8;
#pragma unroll
      for (int kb = 0; kb < 5; ++kb) {
#pragma unroll
        for (int cg = 0; cg < 8; ++cg) acc[cg] = mfma(A[kb], wf[kb][cg], acc[cg]);
        __builtin_amdgcn_sched_barrier(0);
        if (kb < 4) {
#pragma unroll
          for (int j = 0; j < 5; ++j) { const int f = 5 * kb + j; wd[f / 10][f % 10] = tr_frag(src + (f / 10) * 32 * 320 + (f % 10) * 32, 4 * 320); }
        }
        __builtin_amdgcn_sched_barrier(0);
      }
      LF[L_RGU + v * 128 + lane] = sel4(g4, acc[0][0], acc[1][0], acc[2][0], acc[3][0]);
      LF[L_RGU + v * 128 + 64 + lane] = sel4(g4, acc[4][0], acc[5][0], acc[6][0], acc[7][0]);
    }
    if (v == 0) STAMP(28);
    BARRIER();
    if (v == 0) STAMP(29);
    {
      const float G = rbf((LF[L_RGU + lane] + LF[L_RGU + 128 + lane]) + (LF[L_RGU + 256 + lane] + LF[L_RGU + 384 + lane]));
      const float U = rbf((LF[L_RGU + 64 + lane] + LF[L_RGU + 192 + lane]) + (LF[L_RGU + 320 + lane] + LF[L_RGU + 448 + lane]));
      ((LAS bf16_t*)(L + L_ACT + 32 * v))[lane] = (bf16_t)f2bf(rbf(silu(G)) * U);
      if (v == 0) STAMP(14);
      const v4u A0 = *(const LAS v4u*)(L + L_ACT + 32 * v + 4 * g4), A1 = *(const LAS v4u*)(L + L_ACT + 32 * v + 16 + 4 * g4);
      f32x4 acc[10];
#pragma unroll
      for (int cg = 0; cg < 10; ++cg) { acc[cg] = mfma(A0, wd[0][cg], f32x4{0.f, 0.f, 0.f, 0.f}); acc[cg] = mfma(A1, wd[1][cg], acc[cg]); }
      stg(dq, gr_f24(tag, sel4(g4, acc[0][0], acc[1][0], acc[2][0], acc[3][0])));
      stg(dq + 4 * 512, gr_f24(tag, sel4(g4, acc[4][0], acc[5][0], acc[6][0], acc[7][0])));
      if (lane < 32) stg(dq + 8 * 512, gr_f24(tag, g4 == 0 ? acc[8][0] : acc[9][0]));
    }
    if (v == 0) STAMP(17);
  }
  if (v == 0) STAMP_DUMP();
}

extern "C" void kernel_launch(void* const* d_in, const int* in_sizes, int n_in, void* d_out, int out_size, void* d_ws, size_t ws_size, hipStream_t stream) {
  Params p;
  p.x = (const bf16_t*)d_in[0]; p.kc = (const bf16_t*)d_in[1]; p.vc = (const bf16_t*)d_in[2]; p.wq = (const bf16_t*)d_in[3]; p.wo = (const bf16_t*)d_in[4];
  p.wg = (const bf16_t*)d_in[6]; p.wu = (const bf16_t*)d_in[7]; p.wd = (const bf16_t*)d_in[8]; p.c = (const bf16_t*)d_in[9]; p.wmod = (const bf16_t*)d_in[10];
  p.out = (bf16_t*)d_out; p.ws = (unsigned char*)d_ws;
  (void)hipFuncSetAttribute((const void*)adaln_block_kernel, hipFuncAttributeMaxDynamicSharedMemorySize, LDS_BYTES);
  adaln_block_kernel<<<NCU, NTHR, LDS_BYTES, stream>>>(p);
}
```

```cpp
#include <hip/hip_runtime.h>
#include <hip/hip_bf16.h>
#include <stdint.h>

#define TD 6
#define PACE 0
#define PACE0 0
#define KVDELAY 0
#define PSLEEP 0
#define TLBWARM 2
#define WOLEAD 1
#define GATE 1
#define STAG 12
#define QDELAY 64
#define STAMPS 0
#define NT_Q 54
#define NT_M 64
#define NT_F 42
#define NT_P ((512 - 2 * NT_Q - 2 * NT_M - NT_F) / 3)
static_assert(2 * NT_Q + 2 * NT_M + NT_F + 3 * NT_P == 512, "tiles of a head");
#define NT_G0 54
#define NT_G1 60
#define NT_G2 ((512 - 2 * NT_G0 - 2 * NT_G1) / 4)
static_assert(2 * NT_G0 + 2 * NT_G1 + 4 * NT_G2 == 512, "tiles of a head");
#define NT_H0 52
#define NT_H1 ((512 - 2 * NT_H0) / 6)
static_assert(2 * NT_H0 + 6 * NT_H1 == 512, "tiles of a head");
static_assert(NT_F >= 36, "every stream wave takes at least 9 tiles (the catch-up schedule)");

typedef unsigned short bf16_t;
typedef unsigned long long u64;
typedef unsigned v4u __attribute__((ext_vector_type(4)));
typedef float f32x4 __attribute__((ext_vector_type(4)));
typedef float f32x2 __attribute__((ext_vector_type(2)));
typedef unsigned v2u __attribute__((ext_vector_type(2)));
typedef __bf16 bf16x2 __attribute__((ext_vector_type(2)));
typedef __bf16 bf16x8 __attribute__((ext_vector_type(8)));
typedef short v4s __attribute__((ext_vector_type(4)));
#define GAS __attribute__((address_space(1)))
#define LAS __attribute__((address_space(3)))
typedef GAS u64 gu64;
typedef GAS unsigned gu32;

constexpr int D = 640, HQ = 32, SK = 8192, DH = 128, F = 1728, NMOD = 6 * D;
constexpr int NCU = 256, NTHR = 512, NFFN = 27;
constexpr float LOG2E = 1.4426950408889634f;

constexpr size_t WS_EPOCH = 0;
constexpr size_t WS_XG = 393216 + 32768;
constexpr size_t WS_MG = 8192;
constexpr size_t WS_QG = 32768;
constexpr size_t WS_YG = 65536;
constexpr size_t WS_DG = 262144;
constexpr size_t WS_PG = 524288;
constexpr int PG_STRIDE = 160;
constexpr size_t WS_STAMP = 64u << 20;

constexpr int L_FLAG_Q = 0, L_CNT_S1 = 1, L_FLAG_S1 = 2, L_FLAG_E = 3, L_E = 4, L_CNT_M = 5, L_FLAG_H = 6, L_CNT_G = 7, L_CNT_W = 8, L_FLAG_L = 9, L_CNT_OV = 10, L_CNT_Q = 11;
constexpr int L_Q = 32;
constexpr int L_REC = 96;
constexpr int L_O = 624;
constexpr int L_MR = 704;
constexpr int L_SC = 960;
constexpr int L_H2 = 1600;
constexpr int L_ST = 1920;
constexpr int L_RGU = 1936;
constexpr int L_ACT = 2448;
constexpr int L_STAMP = 2816;
constexpr int B_WO = 12288;
constexpr int B_STG = 32768;
constexpr int B_WD = 49152;
constexpr int LDS_BYTES = 131072;

constexpr int CODE_BYTES = 29 * 1024;
constexpr unsigned SPIN_G = 1u << 15;
constexpr unsigned SPIN_L = 1u << 20;

struct Params {
  const bf16_t* x; const bf16_t* kc; const bf16_t* vc; const bf16_t* wq; const bf16_t* wo;
  const bf16_t* wg; const bf16_t* wu; const bf16_t* wd; const bf16_t* c; const bf16_t* wmod;
  bf16_t* out; unsigned char* ws;
};

__device__ __forceinline__ float bf2f(unsigned b) { return __uint_as_float(b << 16); }
__device__ __forceinline__ float bfhi(unsigned b) { return __uint_as_float(b & 0xffff0000u); }
__device__ __forceinline__ float bflo(unsigned b) { return __uint_as_float(b << 16); }
__device__ __forceinline__ unsigned f2bf(float f) { __bf16 b = (__bf16)f; return (unsigned)__builtin_bit_cast(unsigned short, b); }
__device__ __forceinline__ float rbf(float f) { return bf2f(f2bf(f)); }
__device__ __forceinline__ unsigned pk2(float lo, float hi) { return __builtin_bit_cast(unsigned, __builtin_convertvector(f32x2{lo, hi}, bf16x2)); }
__device__ __forceinline__ float silu(float x) { return x * __builtin_amdgcn_rcpf(1.f + __builtin_amdgcn_exp2f(-x * LOG2E)); }
__device__ __forceinline__ float dot2(unsigned a, unsigned b, float acc) {
  return __builtin_amdgcn_fdot2_f32_bf16(__builtin_bit_cast(bf16x2, a), __builtin_bit_cast(bf16x2, b), acc, false);
}
template <int CTRL> __device__ __forceinline__ float dpp(float x) {
  return __builtin_bit_cast(float, __builtin_amdgcn_mov_dpp(__builtin_bit_cast(int, x), CTRL, 0xf, 0xf, true));
}
template <int CTRL> __device__ __forceinline__ unsigned dppu(unsigned x) { return (unsigned)__builtin_amdgcn_mov_dpp((int)x, CTRL, 0xf, 0xf, true); }
#define MAX_DPP(x, ctrl) ({ float r_; asm("s_nop 1\n\tv_max_f32_dpp %0, %1, %1 " ctrl " row_mask:0xf bank_mask:0xf bound_ctrl:1" : "=v"(r_) : "v"(x)); r_; })
constexpr int XOR1 = 0xB1, XOR2 = 0x4E, XOR7 = 0x141, XOR8 = 0x128, ROR4 = 0x124, BCAST0 = 0x150;
__device__ __forceinline__ float swap32_sum(float a, float b) {
  auto t = __builtin_amdgcn_permlane32_swap(__float_as_uint(a), __float_as_uint(b), false, false);
  return __uint_as_float(t[0]) + __uint_as_float(t[1]);
}
__device__ __forceinline__ float swap16_sum(float a, float b) {
  auto t = __builtin_amdgcn_permlane16_swap(__float_as_uint(a), __float_as_uint(b), false, false);
  return __uint_as_float(t[0]) + __uint_as_float(t[1]);
}
__device__ __forceinline__ float xrow_max(float x) {
  auto s = __builtin_amdgcn_permlane16_swap(__float_as_uint(x), __float_as_uint(x), false, false);
  x = fmaxf(__uint_as_float(s[0]), __uint_as_float(s[1]));
  auto t = __builtin_amdgcn_permlane32_swap(__float_as_uint(x), __float_as_uint(x), false, false);
  return fmaxf(__uint_as_float(t[0]), __uint_as_float(t[1]));
}
__device__ __forceinline__ float xrow_sum(float x) { x = swap16_sum(x, x); return swap32_sum(x, x); }
__device__ __forceinline__ float wave_sum(float x) {
  x += dpp<XOR1>(x); x += dpp<XOR2>(x); x += dpp<XOR7>(x); x += dpp<XOR8>(x);
  return xrow_sum(x);
}
__device__ __forceinline__ void reduce_rows(const float* x, float& e0, float& e1) {
  const float y0 = swap32_sum(x[0], x[4]), y1 = swap32_sum(x[1], x[5]), y2 = swap32_sum(x[2], x[6]), y3 = swap32_sum(x[3], x[7]);
  e0 = swap16_sum(y0, y2); e1 = swap16_sum(y1, y3);
}
__device__ __forceinline__ float reduce_oct(const float* x, int lane) {
  float e0, e1; reduce_rows(x, e0, e1);
  const bool b3 = (lane & 8) != 0;
  return (b3 ? e1 : e0) + dpp<XOR8>(b3 ? e0 : e1);
}

__device__ __forceinline__ u64 ldg(const gu64* p) { return __hip_atomic_load(p, __ATOMIC_RELAXED, __HIP_MEMORY_SCOPE_AGENT); }
__device__ __forceinline__ void stg(gu64* p, u64 v) { __hip_atomic_store(p, v, __ATOMIC_RELAXED, __HIP_MEMORY_SCOPE_AGENT); }
__device__ __forceinline__ u64 gr_pair(unsigned tag, unsigned pay) { return ((u64)pay << 32) | tag; }
__device__ __forceinline__ u64 gr_f24(unsigned tag, float v) { return ((u64)((__float_as_uint(v) + 0x80u) & 0xffffff00u) << 32) | tag; }
__device__ __forceinline__ unsigned bad_pair(u64 g, unsigned tag) { return (unsigned)g ^ tag; }
__device__ __forceinline__ unsigned bad_f24(u64 g, unsigned tag) { return ((unsigned)g ^ tag) | ((unsigned)(g >> 32) & 0xffu); }
__device__ __forceinline__ float val_f24(u64 g) { return __uint_as_float((unsigned)(g >> 32)); }
__device__ __forceinline__ bool none_bad(unsigned bad) { return __builtin_amdgcn_ballot_w64(bad != 0u) == 0ull; }
__device__ __forceinline__ bool ok_pair(u64 g, unsigned tag) { return (unsigned)g == tag; }
__device__ __forceinline__ bool all_ok(bool ok) { return __builtin_amdgcn_ballot_w64(!ok) == 0ull; }
#define PINP(ptr) asm volatile("" : "+v"(ptr))

__device__ __forceinline__ void lds_wait(volatile LAS unsigned* w, unsigned want) {
#define LSLEEP 1
  for (unsigned n = 0; *w != want && n < SPIN_L; ++n) __builtin_amdgcn_s_sleep(LSLEEP);
  asm volatile("" ::: "memory");
}
#define WAIT_LGKM() asm volatile("s_waitcnt lgkmcnt(0)" ::: "memory")
#define BARRIER() do { asm volatile("s_waitcnt lgkmcnt(0)" ::: "memory"); __builtin_amdgcn_s_barrier(); asm volatile("" ::: "memory"); } while (0)

#define STAMP(i) do { } while (0)
#define STAMP_DUMP() do { } while (0)

struct AttnState { float m, l; float acc[8]; };

__device__ __forceinline__ void attn_tile(const v4u (&K)[4], const v4u (&V)[4], const v4u& q, AttnState& st, int c) {
  float x[4];
#pragma unroll
  for (int i = 0; i < 4; ++i) {
    float d = dot2(K[i].x, q.x, 0.f);
    d = dot2(K[i].y, q.y, d); d = dot2(K[i].z, q.z, d); d = dot2(K[i].w, q.w, d);
    x[i] = d;
  }
  const bool b3 = (c & 8) != 0, b2 = (c & 4) != 0;
  const float k0 = b3 ? x[2] : x[0], s0 = b3 ? x[0] : x[2];
  const float k1 = b3 ? x[3] : x[1], s1 = b3 ? x[1] : x[3];
  const float y0 = k0 + dpp<XOR8>(s0), y1 = k1 + dpp<XOR8>(s1);
  const float k2 = b2 ? y1 : y0, s2 = b2 ? y0 : y1;
  float z = k2 + dpp<XOR7>(s2);
  z += dpp<XOR2>(z); z += dpp<XOR1>(z);
  const float s = rbf(rbf(z) * 0.08838834764831845f);
  float pm = MAX_DPP(s, "row_ror:4"); pm = MAX_DPP(pm, "row_ror:8");
  const float mn = fmaxf(st.m, pm);
  if (__builtin_amdgcn_ballot_w64(mn > st.m) != 0ull) {
    const float al = __builtin_amdgcn_exp2f((st.m - mn) * LOG2E);
    st.l *= al;
#pragma unroll
    for (int j = 0; j < 8; ++j) st.acc[j] *= al;
    st.m = mn;
  }
  const float pr = __builtin_amdgcn_exp2f((s - st.m) * LOG2E);
  st.l += pr;
  const unsigned plo = f2bf(pr), phi = plo << 16;
#define PV(i) { const unsigned a = dppu<BCAST0 + 4 * i>(plo), b = dppu<BCAST0 + 4 * i>(phi); \
    st.acc[0] = dot2(V[i].x, a, st.acc[0]); st.acc[1] = dot2(V[i].x, b, st.acc[1]); st.acc[2] = dot2(V[i].y, a, st.acc[2]); st.acc[3] = dot2(V[i].y, b, st.acc[3]); \
    st.acc[4] = dot2(V[i].z, a, st.acc[4]); st.acc[5] = dot2(V[i].z, b, st.acc[5]); st.acc[6] = dot2(V[i].w, a, st.acc[6]); st.acc[7] = dot2(V[i].w, b, st.acc[7]); }
  PV(0) PV(1) PV(2) PV(3)
#undef PV
}

__device__ __forceinline__ void interleave(const v4u& r0, const v4u& r1, unsigned* o) {
#pragma unroll
  for (int j = 0; j < 4; ++j) { o[2 * j] = __builtin_amdgcn_perm(r1[j], r0[j], 0x05040100u); o[2 * j + 1] = __builtin_amdgcn_perm(r1[j], r0[j], 0x07060302u); }
}
__device__ __forceinline__ f32x4 mfma(const v4u& a, const v4u& b, const f32x4& c) {
  return __builtin_amdgcn_mfma_f32_16x16x32_bf16(__builtin_bit_cast(bf16x8, a), __builtin_bit_cast(bf16x8, b), c, 0, 0, 0);
}
__device__ __forceinline__ v4u tr_frag(const LAS unsigned char* p, int stride4) {
  const v2u lo = __builtin_bit_cast(v2u, __builtin_amdgcn_ds_read_tr16_b64_v4i16((LAS v4s*)p));
  const v2u hi = __builtin_bit_cast(v2u, __builtin_amdgcn_ds_read_tr16_b64_v4i16((LAS v4s*)(p + stride4)));
  return v4u{lo.x, lo.y, hi.x, hi.y};
}
#define GLDS(gsrc, ldst, aux) __builtin_amdgcn_global_load_lds((const GAS void*)(gsrc), (LAS void*)(ldst), 16, 0, aux)
__device__ __forceinline__ float sel4(int q4, float a, float b, float c, float d) { return q4 == 0 ? a : q4 == 1 ? b : q4 == 2 ? c : d; }
__device__ __forceinline__ v4u ldnt(const bf16_t* p) { return __builtin_nontemporal_load((const GAS v4u*)p); }
#define PIN8(a) asm volatile("" : "+v"((a)[0]), "+v"((a)[1]), "+v"((a)[2]), "+v"((a)[3]), "+v"((a)[4]), "+v"((a)[5]), "+v"((a)[6]), "+v"((a)[7]))

__global__ void __launch_bounds__(NTHR, 2) adaln_block_kernel(Params p) {
  extern __shared__ __attribute__((aligned(16))) unsigned lds_raw[];
  LAS unsigned* L = (LAS unsigned*)lds_raw;
  volatile LAS unsigned* LV = (volatile LAS unsigned*)lds_raw;
  LAS float* LF = (LAS float*)lds_raw;
  LAS unsigned char* LB = (LAS unsigned char*)lds_raw;
  u64 code_base;
  asm volatile("s_getpc_b64 %0" : "=s"(code_base));
  code_base &= ~255ull;
  const int tid = threadIdx.x, lane = tid & 63, wave = __builtin_amdgcn_readfirstlane(tid >> 6);
  const int r = lane >> 4, c = lane & 15;
  const int b = blockIdx.x, X = b & 7, J = b >> 3, sp = J & 7;
  const int head = 4 * X + (J >> 3);
  const bool fh = head < NFFN;
  const bool is_qc = sp < 2, is_ffn = fh && sp == 5;
  const int mu = (sp == 2 || sp == 3) ? 2 * head + sp - 2 : 999;
  const int own = head;
  const bool is_mod = mu < 60, is_own = sp == 4 && head < 10;
  const int cf = fh, cg = !fh && head < 30, ch_ = head >= 30;
  const int nq_ = cf * NT_Q + cg * NT_G0 + ch_ * NT_H0, nm_ = cf * NT_M + cg * NT_G1 + ch_ * NT_H1;
  const int np_ = cf * NT_P + cg * NT_G2 + ch_ * NT_H1, nf_ = cf * NT_F + cg * NT_G2 + ch_ * NT_H1;
  const int t0 = min(sp, 2) * nq_ + min(max(sp - 2, 0), 2) * nm_ + (int)(sp > 4) * np_ + (int)(sp > 5) * nf_ + (int)(sp > 6) * np_;
  const int nt = (int)(sp < 2) * nq_ + (int)(sp == 2 || sp == 3) * nm_ + (int)(sp == 5) * nf_ + (int)(sp == 4 || sp > 5) * np_;
  if (tid < 16) L[tid] = 0u;
  asm volatile("" :: "s"(p.kc), "s"(p.vc), "s"(p.ws), "s"(p.wq), "s"(p.wmod), "s"(p.c), "s"(p.x), "s"(t0), "s"(nt));
  BARRIER();
  gu64* const XG = (gu64*)(p.ws + WS_XG); gu64* const MG = (gu64*)(p.ws + WS_MG); gu64* const YG = (gu64*)(p.ws + WS_YG);
  gu64* const DG = (gu64*)(p.ws + WS_DG); gu64* const QG = (gu64*)(p.ws + WS_QG); gu64* const PG = (gu64*)(p.ws + WS_PG);

  if (__builtin_expect(wave < 4, 1)) {
    const size_t base = ((size_t)head * SK + (size_t)t0 * 16) * DH;
    const __amdgpu_buffer_rsrc_t rk = __builtin_amdgcn_make_buffer_rsrc((void*)(p.kc + base), 0, nt * 4096, 0x00020000);
    const __amdgpu_buffer_rsrc_t rv = __builtin_amdgcn_make_buffer_rsrc((void*)(p.vc + base), 0, nt * 4096, 0x00020000);
    const int voff = lane * 16;
    const int nw = (nt - wave + 3) >> 2;
    v4u K[TD][4], V[TD][4];
#define ISSUE(d, t) do { const int so_ = (wave + 4 * (t)) * 4096; \
    _Pragma("unroll") for (int i_ = 0; i_ < 4; ++i_) K[d][i_] = __builtin_amdgcn_raw_buffer_load_b128(rk, voff + 1024 * i_, so_, 2); \
    _Pragma("unroll") for (int i_ = 0; i_ < 4; ++i_) V[d][i_] = __builtin_amdgcn_raw_buffer_load_b128(rv, voff + 1024 * i_, so_, 2); \
    __builtin_amdgcn_sched_barrier(0); } while (0)
#define PACE_WAIT(n) do { asm volatile("s_waitcnt vmcnt(%0)" :: "n"(n) : "memory"); __builtin_amdgcn_sched_barrier(0); } while (0)
#define QGATE 1
    if (QGATE == 1 && is_qc) lds_wait(LV + L_FLAG_H, 1u);
    if (QGATE == 2 && is_qc) lds_wait(LV + L_CNT_Q, 4u);
    if (is_mod) lds_wait(LV + L_CNT_G, 1u);
    if (KVDELAY) __builtin_amdgcn_s_sleep(KVDELAY);
    if (wave == 0) STAMP(24);
    ISSUE(0, 0);
#pragma unroll
    for (int d = 1; d < TD; ++d) { PACE_WAIT(PACE0); ISSUE(d, d); }
    const unsigned ov = (unsigned)(size_t)(LB + B_WD + wave * 16384);
#define DMA4(m0v, rsrc, so) do { unsigned keep_; asm volatile("s_mov_b32 %0, m0\n\ts_mov_b32 m0, %1\n\ts_nop 0\n\t" \
      "buffer_load_dwordx4 %2, %3, %4 offen nt lds\n\tbuffer_load_dwordx4 %2, %3, %4 offen offset:1024 nt lds\n\t" \
      "buffer_load_dwordx4 %2, %3, %4 offen offset:2048 nt lds\n\tbuffer_load_dwordx4 %2, %3, %4 offen offset:3072 nt lds\n\t" \
      "s_mov_b32 m0, %0" : "=&s"(keep_) : "s"(m0v), "v"(voff), "s"(rsrc), "s"(so) : "memory"); } while (0)
#define DMA_TILE(e, t) do { const int so_ = (wave + 4 * (t)) * 4096; DMA4(ov + (e) * 8192, rk, so_); DMA4(ov + (e) * 8192 + 4096, rv, so_); __builtin_amdgcn_sched_barrier(0); } while (0)
#define LD_OV(d, e) do { const LAS v4u* src_ = (const LAS v4u*)(LB + B_WD + wave * 16384 + (e) * 8192) + lane; \
      _Pragma("unroll") for (int i_ = 0; i_ < 4; ++i_) { K[d][i_] = src_[64 * i_]; V[d][i_] = src_[256 + 64 * i_]; } __builtin_amdgcn_sched_barrier(0); } while (0)
    PACE_WAIT(PACE0); DMA_TILE(0, 6);
    PACE_WAIT(PACE0); DMA_TILE(1, 7);
    lds_wait(LV + L_FLAG_Q, 1u);
    if (wave == 0) STAMP(1);
    const v4u q = *(const LAS v4u*)(L + L_Q + 4 * c);
    const unsigned tag = L[L_E] + 1u;
    AttnState st; st.m = -INFINITY; st.l = 0.f;
#pragma unroll
    for (int j = 0; j < 8; ++j) st.acc[j] = 0.f;
#define TILE(d) do { attn_tile(K[d], V[d], q, st, c); __builtin_amdgcn_sched_barrier(0); } while (0)
    static_assert(TD == 6, "the catch-up schedule is written for six slots");
    TILE(0); PACE_WAIT(0); ISSUE(0, 8);
    TILE(1); TILE(2); TILE(3); TILE(4);
    LD_OV(4, 0);
    PACE_WAIT(PACE); ISSUE(1, 9);
    TILE(5);
    LD_OV(5, 1);
    WAIT_LGKM();
    if (lane == 0) __hip_atomic_fetch_add((LAS unsigned*)(L + L_CNT_OV), 1u, __ATOMIC_RELAXED, __HIP_MEMORY_SCOPE_WORKGROUP);
    TILE(4); TILE(5);
    TILE(0);
#pragma nounroll
    for (int u = 9; u < nw; u += TD) {
#pragma unroll
      for (int k = 0; k < TD; ++k) {
        if (u + k < nw) {
          PACE_WAIT(PACE);
          ISSUE((k + 2) % TD, u + k + 1);
          if (u + k + 1 + WOLEAD == nw && wave == 0 && lane == 0) LV[L_FLAG_L] = 1u;
          TILE((k + 1) % TD);
        }
      }
    }
#undef TILE
    if (wave == 0 && lane == 0) LV[L_FLAG_L] = 1u;
#undef ISSUE
    if (wave == 0) STAMP(2);
    const float M = xrow_max(st.m);
    const float sc = __builtin_amdgcn_exp2f((st.m - M) * LOG2E);
    float l = ((c & 3) == 0) ? st.l * sc : 0.f;
    l += dpp<ROR4>(l); l += dpp<XOR8>(l); l = xrow_sum(l);
#pragma unroll
    for (int j = 0; j < 8; ++j) st.acc[j] *= sc;
    float e0, e1; reduce_rows(st.acc, e0, e1);
    LAS float* rec = LF + L_REC + wave * 132;
    *(LAS f32x2*)(rec + 4 + 8 * c + 2 * r) = f32x2{e0, e1};
    if (lane == 0) { rec[0] = M; rec[1] = l; }
    WAIT_LGKM();
    unsigned old = 0;
    if (lane == 0) old = __hip_atomic_fetch_add((LAS unsigned*)(L + L_CNT_S1), 1u, __ATOMIC_RELAXED, __HIP_MEMORY_SCOPE_WORKGROUP);
    old = __builtin_amdgcn_readfirstlane(old);
    if (old == 3u) {
      asm volatile("" ::: "memory");
      const LAS float* R = LF + L_REC;
      const float m0 = R[0], m1 = R[132], m2 = R[264], m3 = R[396];
      const float MM = fmaxf(fmaxf(m0, m1), fmaxf(m2, m3));
      const float w0 = __builtin_amdgcn_exp2f((m0 - MM) * LOG2E), w1 = __builtin_amdgcn_exp2f((m1 - MM) * LOG2E);
      const float w2 = __builtin_amdgcn_exp2f((m2 - MM) * LOG2E), w3 = __builtin_amdgcn_exp2f((m3 - MM) * LOG2E);
      const float LL = (w0 * R[1] + w1 * R[133]) + (w2 * R[265] + w3 * R[397]);
      const float oa = (w0 * R[4 + lane] + w1 * R[136 + lane]) + (w2 * R[268 + lane] + w3 * R[400 + lane]);
      const float ob = (w0 * R[68 + lane] + w1 * R[200 + lane]) + (w2 * R[332 + lane] + w3 * R[464 + lane]);
      gu64* rp = PG + (size_t)(head * 8 + sp) * PG_STRIDE;
      stg(rp + lane, gr_f24(tag, oa));
      stg(rp + 64 + lane, gr_f24(tag, ob));
      if (lane < 2) stg(rp + 128 + lane, gr_f24(tag, lane == 0 ? MM : LL));
      if (lane == 0) LV[L_FLAG_S1] = 1u;
      STAMP(3);
    }
    return;
  }

  const int v = wave - 4, t = tid - 256;
  const int r8 = lane >> 3, c8 = lane & 7;
  unsigned tag = 0;
  if (is_qc) {
    const unsigned e_in = __hip_atomic_load((gu32*)(p.ws + WS_EPOCH), __ATOMIC_RELAXED, __HIP_MEMORY_SCOPE_AGENT);
    v4u w[20];
    {
      const bf16_t* src = p.wq + (size_t)(160 * v + 2 * r8) * (HQ * DH) + head * DH + 64 * sp + 8 * c8;
#pragma unroll
      for (int i = 0; i < 10; ++i) { w[2 * i] = ldnt(src + (size_t)(16 * i) * (HQ * DH)); w[2 * i + 1] = ldnt(src + (size_t)(16 * i + 1) * (HQ * DH)); }
    }
    unsigned xs[5];
#pragma unroll
    for (int i = 0; i < 5; ++i) xs[i] = ((const GAS unsigned*)p.x)[lane + 64 * i];
    const unsigned xo0 = ((const GAS unsigned*)p.x)[80 * v + lane], xo1 = ((const GAS unsigned*)p.x)[80 * v + 64 + (lane & 15)];
    __builtin_amdgcn_sched_barrier(0);
    if (v == 0 && lane == 0) { LV[L_E] = e_in; WAIT_LGKM(); LV[L_FLAG_E] = 1u; }
    float n0a, n0b, n1a, n1b;
    {
      float s = 0.f;
#pragma unroll
      for (int i = 0; i < 5; ++i) s += bflo(xs[i]) + bfhi(xs[i]);
      const float mean_f = wave_sum(s) * (1.f / D);
      float vs = 0.f;
#pragma unroll
      for (int i = 0; i < 5; ++i) { const float d0 = bflo(xs[i]) - mean_f, d1 = bfhi(xs[i]) - mean_f; vs = fmaf(d0, d0, vs); vs = fmaf(d1, d1, vs); }
      const float var = rbf(wave_sum(vs) * (1.f / D)), mean = rbf(mean_f);
      const float rs = __builtin_amdgcn_rcpf(rbf(__builtin_amdgcn_sqrtf(rbf(var + 1e-5f))));
      n0a = rbf(rbf(bflo(xo0) - mean) * rs); n0b = rbf(rbf(bfhi(xo0) - mean) * rs);
      n1a = rbf(rbf(bflo(xo1) - mean) * rs); n1b = rbf(rbf(bfhi(xo1) - mean) * rs);
      asm volatile("" : "+v"(n0a), "+v"(n0b), "+v"(n1a), "+v"(n1b));
    }
    unsigned wi[10][8];
#pragma unroll
    for (int i = 0; i < 10; ++i) { interleave(w[2 * i], w[2 * i + 1], wi[i]); PIN8(wi[i]); }
    if (lane == 0) __hip_atomic_fetch_add((LAS unsigned*)(L + L_CNT_Q), 1u, __ATOMIC_RELAXED, __HIP_MEMORY_SCOPE_WORKGROUP);
    lds_wait(LV + L_FLAG_E, 1u);
    tag = LV[L_E] + 1u;
    if (v == 0) STAMP(18);
    unsigned s0, c0, s1, c1;
    {
      const gu64* m0 = MG + 80 * v + lane; const gu64* m1 = MG + 80 * v + 64 + (lane & 15);
      for (unsigned n = 0; n < SPIN_G; ++n) {
        const u64 a0 = ldg(m0), b0 = ldg(m0 + 320), a1 = ldg(m1), b1 = ldg(m1 + 320);
        s0 = (unsigned)(a0 >> 32); c0 = (unsigned)(b0 >> 32); s1 = (unsigned)(a1 >> 32); c1 = (unsigned)(b1 >> 32);
        if (all_ok(ok_pair(a0, tag) & ok_pair(b0, tag) & ok_pair(a1, tag) & ok_pair(b1, tag))) break;
      }
    }
    if (v == 0) STAMP(5);
    L[L_H2 + 80 * v + lane] = f2bf(rbf(n0a * rbf(1.f + bflo(c0))) + bflo(s0)) | (f2bf(rbf(n0b * rbf(1.f + bfhi(c0))) + bfhi(s0)) << 16);
    if (lane < 16) L[L_H2 + 80 * v + 64 + lane] = f2bf(rbf(n1a * rbf(1.f + bflo(c1))) + bflo(s1)) | (f2bf(rbf(n1b * rbf(1.f + bfhi(c1))) + bfhi(s1)) << 16);
    WAIT_LGKM();
    float acc[8];
#pragma unroll
    for (int j = 0; j < 8; ++j) acc[j] = 0.f;
#pragma unroll
    for (int i = 0; i < 10; ++i) {
      const unsigned hp = L[L_H2 + 80 * v + 8 * i + r8];
#pragma unroll
      for (int j = 0; j < 8; ++j) acc[j] = dot2(wi[i][j], hp, acc[j]);
    }
    LF[L_MR + 64 * v + 8 * c8 + r8] = reduce_oct(acc, lane);
    WAIT_LGKM();
    unsigned old = 0;
    if (lane == 0) old = __hip_atomic_fetch_add((LAS unsigned*)(L + L_CNT_M), 1u, __ATOMIC_RELAXED, __HIP_MEMORY_SCOPE_WORKGROUP);
    if (__builtin_amdgcn_readfirstlane(old) == 3u) {
      asm volatile("" ::: "memory");
      const unsigned qb = f2bf((LF[L_MR + lane] + LF[L_MR + 64 + lane]) + (LF[L_MR + 128 + lane] + LF[L_MR + 192 + lane]));
      const unsigned up = dppu<XOR1>(qb);
      if ((lane & 1) == 0) stg(QG + head * 64 + 32 * sp + (lane >> 1), gr_pair(tag, qb | (up << 16)));
      if (lane == 0) LV[L_FLAG_H] = 1u;
      STAMP(6);
    }
  } else {
    const unsigned e_in = __hip_atomic_load((gu32*)(p.ws + WS_EPOCH), __ATOMIC_RELAXED, __HIP_MEMORY_SCOPE_AGENT);
    const __amdgpu_buffer_rsrc_t rm = __builtin_amdgcn_make_buffer_rsrc((void*)p.wmod, 0, is_mod ? D * NMOD * 2 : 0, 0x00020000);
    const __amdgpu_buffer_rsrc_t rc = __builtin_amdgcn_make_buffer_rsrc((void*)p.c, 0, is_mod ? D * 2 : 0, 0x00020000);
    const unsigned cv0 = __builtin_amdgcn_raw_buffer_load_b16(rc, (160 * v + lane) * 2, 0, 0), cv1 = __builtin_amdgcn_raw_buffer_load_b16(rc, (160 * v + 64 + lane) * 2, 0, 0),
                   cv2 = __builtin_amdgcn_raw_buffer_load_b16(rc, (160 * v + 128 + (lane & 31)) * 2, 0, 0);
    v4u wm[20];
#pragma unroll
    for (int i = 0; i < 20; ++i) wm[i] = __builtin_amdgcn_raw_buffer_load_b128(rm, ((160 * v + r8) * NMOD + 64 * mu + 8 * c8) * 2, i * (8 * NMOD * 2), 2);
    __builtin_amdgcn_sched_barrier(0);
    if (v == 0 && lane == 0) { LV[L_E] = e_in; WAIT_LGKM(); LV[L_FLAG_E] = 1u; }
    if (is_mod) {
      L[L_SC + 160 * v + lane] = f2bf(silu(bf2f(cv0))); L[L_SC + 160 * v + 64 + lane] = f2bf(silu(bf2f(cv1)));
      if (lane < 32) L[L_SC + 160 * v + 128 + lane] = f2bf(silu(bf2f(cv2)));
      WAIT_LGKM();
      float acc[8];
#pragma unroll
      for (int j = 0; j < 8; ++j) acc[j] = 0.f;
#pragma unroll
      for (int i = 0; i < 20; ++i) {
        const unsigned a = L[L_SC + 160 * v + 8 * i + r8], bb = a << 16;
#pragma unroll
        for (int j = 0; j < 4; ++j) { acc[2 * j] = dot2(wm[i][j], a, acc[2 * j]); acc[2 * j + 1] = dot2(wm[i][j], bb, acc[2 * j + 1]); }
      }
      LF[L_MR + 64 * v + 8 * c8 + r8] = reduce_oct(acc, lane);
      WAIT_LGKM();
      unsigned old = 0;
      if (lane == 0) old = __hip_atomic_fetch_add((LAS unsigned*)(L + L_CNT_M), 1u, __ATOMIC_RELAXED, __HIP_MEMORY_SCOPE_WORKGROUP);
      if (__builtin_amdgcn_readfirstlane(old) == 3u) {
        asm volatile("" ::: "memory");
        const unsigned mb_ = f2bf((LF[L_MR + lane] + LF[L_MR + 64 + lane]) + (LF[L_MR + 128 + lane] + LF[L_MR + 192 + lane]));
        const unsigned up = dppu<XOR1>(mb_);
        lds_wait(LV + L_FLAG_E, 1u);
        if ((lane & 1) == 0) stg(MG + 32 * mu + (lane >> 1), gr_pair(LV[L_E] + 1u, mb_ | (up << 16)));
        if (lane == 0) LV[L_CNT_G] = 1u;
        STAMP(4);
      }
    }
    if (v == 3 && J * 1024 < CODE_BYTES) {
      const v4u cw = ((const GAS v4u*)(code_base + (u64)(J * 1024)))[lane]; asm volatile("" :: "v"(cw));
    }
  }
  if (v == 2) {
      const int k = lane & 15;
      const GAS unsigned* a = k == 0 ? (const GAS unsigned*)(p.wo + (size_t)(head * DH) * D + 80 * sp)
                            : k == 1 ? (const GAS unsigned*)(p.wo + (size_t)(head * DH + DH - 1) * D + 80 * sp)
                            : k == 2 ? (const GAS unsigned*)p.out
                            : k == 3 ? (const GAS unsigned*)(p.ws + WS_PG + (size_t)(head * 8) * PG_STRIDE * 8)
                            : k == 4 ? (const GAS unsigned*)(p.ws + WS_YG + (size_t)(5 * sp) * 4096)
                            : k == 5 ? (const GAS unsigned*)(p.ws + WS_DG)
                            : k == 6 ? (const GAS unsigned*)(p.ws + WS_DG + 131072)
                            : k == 7 ? (const GAS unsigned*)(p.ws + WS_XG)
                            : k == 8 ? (const GAS unsigned*)(p.kc + ((size_t)head * SK + (size_t)(t0 + nt) * 16 - 1) * DH)
                            : k == 9 ? (const GAS unsigned*)(p.vc + ((size_t)head * SK + (size_t)(t0 + nt) * 16 - 1) * DH)
                            : (const GAS unsigned*)(p.ws + WS_QG);
      const unsigned w_ = *a; asm volatile("" :: "v"(w_));
      const int st_ = min((lane & 31) * 16, nt - 1);
      const bf16_t* kv = lane < 32 ? p.kc : p.vc;
      const unsigned w2_ = *(const GAS unsigned*)(kv + ((size_t)head * SK + (size_t)(t0 + st_) * 16) * DH); asm volatile("" :: "v"(w2_));
  }
  if (v == 1) {
    lds_wait(LV + L_FLAG_E, 1u);
    tag = LV[L_E] + 1u;
    const gu64* qp = QG + head * 64 + lane;
    if (!is_qc) __builtin_amdgcn_s_sleep(QDELAY);
    u64 g0 = ldg(qp); __builtin_amdgcn_s_sleep(STAG);
    u64 g1 = ldg(qp); __builtin_amdgcn_s_sleep(STAG);
    u64 g2 = ldg(qp), g;
    for (unsigned n = 0;; ++n) {
      if (all_ok(ok_pair(g0, tag)) || n >= SPIN_G) { g = g0; break; }
      g0 = ldg(qp);
      if (all_ok(ok_pair(g1, tag))) { g = g1; break; }
      g1 = ldg(qp);
      if (all_ok(ok_pair(g2, tag))) { g = g2; break; }
      g2 = ldg(qp);
    }
    L[L_Q + lane] = (unsigned)(g >> 32);
    WAIT_LGKM();
    if (lane == 0) LV[L_FLAG_Q] = 1u;
    STAMP(7);
  }
  lds_wait(LV + L_FLAG_E, 1u);
  tag = LV[L_E] + 1u;

  const int g4 = lane >> 4, tq = (lane & 15) >> 2, tp = lane & 3;
  v4u wf[5][8];
  unsigned mbA[2], mbB[2];
  if (is_ffn) {
#define FPACE 63
    v4u raw[5][8];
    {
      const size_t ro = (size_t)(160 * v + r8) * F + 64 * head + 8 * c8;
#pragma unroll
      for (int kb = 0; kb < 5; ++kb) {
        if (FPACE < 63) asm volatile("s_waitcnt vmcnt(%0)" :: "n"(FPACE) : "memory");
#pragma unroll
        for (int i = 0; i < 4; ++i) { raw[kb][i] = ldnt(p.wg + ro + (size_t)(32 * kb + 8 * i) * F); raw[kb][4 + i] = ldnt(p.wu + ro + (size_t)(32 * kb + 8 * i) * F); }
        __builtin_amdgcn_sched_barrier(0);
      }
    }
    __builtin_amdgcn_sched_barrier(0);
    LAS unsigned char* stg_w = LB + B_STG + v * 4096 + r8 * 128 + c8 * 16;
    const LAS unsigned char* stg_r = LB + B_STG + v * 4096 + (8 * g4 + tq) * 128 + tp * 8;
#pragma unroll
    for (int kb = 0; kb < 5; ++kb)
#pragma unroll
      for (int m = 0; m < 2; ++m) {
#pragma unroll
        for (int i = 0; i < 4; ++i) *(LAS v4u*)(stg_w + i * 1024) = raw[kb][4 * m + i];
        asm volatile("" ::: "memory");
#pragma unroll
        for (int cg = 0; cg < 4; ++cg) wf[kb][4 * m + cg] = tr_frag(stg_r + cg * 32, 512);
        asm volatile("s_waitcnt lgkmcnt(0)" ::: "memory");
      }
    if (v == 0) STAMP(21);
    if (v == 1) STAMP(18);
    lds_wait(LV + L_CNT_OV, 4u);
    if (v == 0) STAMP(22);
#pragma unroll
    for (int i = 0; i < 20; ++i) {
      const int idx = 64 * i + lane, row = idx / 20, ch = idx - 20 * row;
      GLDS(p.wd + (size_t)(64 * head + row) * D + 160 * v + 8 * ch, LB + B_WD + v * 20480 + 1024 * i, 2);
    }
    {
      const gu64* m0 = MG + 80 * v + lane; const gu64* m1 = MG + 80 * v + 64 + (lane & 15);
      for (unsigned n = 0; n < SPIN_G; ++n) {
        const u64 s0 = ldg(m0 + 960), c0 = ldg(m0 + 1280), s1 = ldg(m1 + 960), c1 = ldg(m1 + 1280);
        const unsigned sp0 = (unsigned)(s0 >> 32), cp0 = (unsigned)(c0 >> 32), sp1 = (unsigned)(s1 >> 32), cp1 = (unsigned)(c1 >> 32);
        mbA[0] = (sp0 & 0xffffu) | (f2bf(1.f + bflo(cp0)) << 16); mbB[0] = (sp0 >> 16) | (f2bf(1.f + bfhi(cp0)) << 16);
        mbA[1] = (sp1 & 0xffffu) | (f2bf(1.f + bflo(cp1)) << 16); mbB[1] = (sp1 >> 16) | (f2bf(1.f + bfhi(cp1)) << 16);
        if (all_ok(ok_pair(s0, tag) & ok_pair(c0, tag) & ok_pair(s1, tag) & ok_pair(c1, tag))) break;
        __builtin_amdgcn_s_sleep(8);
      }
    }
  }
  float own_x = 0.f, own_ga = 0.f, own_gb = 0.f;
  if (is_own) {
    const int col = 64 * own + 16 * v + c;
    const gu64* m = MG + (lane < 32 ? 640 : 1600) + (col >> 1);
    own_x = bf2f(((const GAS bf16_t*)p.x)[col]);
    unsigned g = 0;
    for (unsigned n = SPIN_G; n != 0u; --n) { const u64 a = ldg(m); g = (unsigned)(a >> 32); if (none_bad(bad_pair(a, tag))) break; __builtin_amdgcn_s_sleep(8); }
    const float gv = (c & 1) ? bfhi(g) : bflo(g);
    const float ga_h = lane < 32 ? gv : 0.f, gb_h = lane < 32 ? 0.f : gv;
    own_ga = swap32_sum(ga_h, ga_h); own_gb = swap32_sum(gb_h, gb_h);
  }
  lds_wait(LV + L_FLAG_L, 1u);
#pragma unroll
  for (int i = 0; i < 5; ++i) {
    const int idx = 256 * i + t, row = idx / 10, ch = idx - 10 * row;
    GLDS(p.wo + (size_t)(head * DH + row) * D + 80 * sp + 8 * ch, LB + B_WO + (256 * i + 64 * v) * 16, 0);
  }
  asm volatile("s_waitcnt vmcnt(0)" ::: "memory");
  if (lane == 0) __hip_atomic_fetch_add((LAS unsigned*)(L + L_CNT_W), 1u, __ATOMIC_RELAXED, __HIP_MEMORY_SCOPE_WORKGROUP);
  lds_wait(LV + L_CNT_W, 4u);
  v4u wof[4], wof4[4];
  {
    const LAS unsigned char* src = LB + B_WO + (8 * g4 + tq) * 160 + tp * 8;
#pragma unroll
    for (int kb = 0; kb < 4; ++kb) { wof[kb] = tr_frag(src + kb * 32 * 160 + v * 32, 4 * 160); wof4[kb] = tr_frag(src + kb * 32 * 160 + 4 * 32, 4 * 160); }
  }
  if (v == 0) STAMP(8);

  const int hf = lane >> 5;
  const gu64* hop = PG + (size_t)(head * 8 + hf) * PG_STRIDE + 32 * v + (lane & 31);
  const gu64* hmp = PG + (size_t)(head * 8 + (((lane & 15) >> 1) ^ hf)) * PG_STRIDE + 128 + (lane & 1);
  gu64* ysp = YG + (size_t)(5 * sp + (lane < 16 ? v : 4)) * 512 + head * 16 + c;
  LAS bf16_t* odst = (LAS bf16_t*)(L + L_O) + 32 * v + (lane & 31);
  PINP(hop); PINP(hmp); PINP(ysp);
  lds_wait(LV + L_FLAG_S1, 1u);
  if (v == 0) STAMP(9);
  {
    u64 g0, g1, g2, g3, gm;
    for (unsigned n = SPIN_G; n != 0u; --n) {
      g0 = ldg(hop); g1 = ldg(hop + 2 * PG_STRIDE); g2 = ldg(hop + 4 * PG_STRIDE); g3 = ldg(hop + 6 * PG_STRIDE); gm = ldg(hmp);
      if (none_bad((bad_f24(g0, tag) | bad_f24(g1, tag) | bad_f24(g2, tag)) | (bad_f24(g3, tag) | bad_f24(gm, tag)))) break;
      if (PSLEEP) __builtin_amdgcn_s_sleep(PSLEEP);
    }
    const float ml = val_f24(gm);
    const float mm = (lane & 1) ? -INFINITY : ml;
    float M = MAX_DPP(mm, "quad_perm:[2,3,0,1]"); M = MAX_DPP(M, "row_half_mirror"); M = MAX_DPP(M, "row_ror:8"); M = MAX_DPP(M, "quad_perm:[1,0,3,2]");
    const float wgt = __builtin_amdgcn_exp2f((mm - M) * LOG2E);
    const float wn = dpp<XOR1>(wgt);
    const float wl = (lane & 1) ? wn * ml : 0.f;
    float Ls = wl + dpp<XOR1>(wl); Ls += dpp<XOR2>(Ls); Ls += dpp<XOR7>(Ls); Ls += dpp<XOR8>(Ls);
    float acc = dpp<BCAST0>(wgt) * val_f24(g0);
    acc = fmaf(dpp<BCAST0 + 4>(wgt), val_f24(g1), acc); acc = fmaf(dpp<BCAST0 + 8>(wgt), val_f24(g2), acc); acc = fmaf(dpp<BCAST0 + 12>(wgt), val_f24(g3), acc);
    acc = swap32_sum(acc, acc);
    *odst = (bf16_t)f2bf(acc * __builtin_amdgcn_rcpf(Ls));
  }
  BARRIER();
  if (v == 0) STAMP(10);
  {
    v4u A[4];
#pragma unroll
    for (int kb = 0; kb < 4; ++kb) A[kb] = *(const LAS v4u*)(L + L_O + 16 * kb + 4 * g4);
    f32x4 y = {0.f, 0.f, 0.f, 0.f}, y4 = {0.f, 0.f, 0.f, 0.f};
#pragma unroll
    for (int kb = 0; kb < 4; ++kb) y = mfma(A[kb], wof[kb], y);
    if (v == 0) {
#pragma unroll
      for (int kb = 0; kb < 4; ++kb) y4 = mfma(A[kb], wof4[kb], y4);
    }
    if (lane < 16 || (v == 0 && lane < 32)) stg(ysp, gr_f24(tag, lane < 16 ? y[0] : y4[0]));
  }
  if (v == 0) STAMP(11);
  if (is_own) {
    const int og = 4 * own + v;
    const gu64* yp = YG + (size_t)og * 512 + lane;
    const gu64* dp = DG + (size_t)og * 512 + lane;
    const gu64* dp6 = dp + (lane < 48 ? 384 : 0);
    gu64* xp = XG + 16 * og + (c >> 1);
    GAS bf16_t* op = (GAS bf16_t*)p.out + 16 * og + c;
    PINP(yp); PINP(dp); PINP(dp6); PINP(xp); PINP(op);
    u64 g[8];
    for (unsigned n = SPIN_G; n != 0u; --n) {
      unsigned bad = 0;
#pragma unroll
      for (int i = 0; i < 8; ++i) { g[i] = ldg(yp + 64 * i); bad |= bad_f24(g[i], tag); }
      if (none_bad(bad)) break;
      if (PSLEEP) __builtin_amdgcn_s_sleep(PSLEEP);
    }
    if (v == 0) STAMP(30);
    float s = ((val_f24(g[0]) + val_f24(g[1])) + (val_f24(g[2]) + val_f24(g[3]))) + ((val_f24(g[4]) + val_f24(g[5])) + (val_f24(g[6]) + val_f24(g[7])));
    s = xrow_sum(s);
    const float x1 = rbf(own_x + rbf(own_ga * rbf(s)));
    const unsigned xb = f2bf(x1), pair = xb | (dppu<XOR1>(xb) << 16);
    if (lane < 16 && (lane & 1) == 0) stg(xp, gr_pair(tag, pair));
    if (v == 0) STAMP(12);
    for (unsigned n = SPIN_G; n != 0u; --n) {
      unsigned bad = 0;
#pragma unroll
      for (int i = 0; i < 6; ++i) { g[i] = ldg(dp + 64 * i); bad |= bad_f24(g[i], tag); }
      g[6] = ldg(dp6); bad |= bad_f24(g[6], tag);
      if (none_bad(bad)) break;
      if (PSLEEP) __builtin_amdgcn_s_sleep(PSLEEP);
    }
    if (v == 0) STAMP(15);
    s = ((val_f24(g[0]) + val_f24(g[1])) + (val_f24(g[2]) + val_f24(g[3]))) + ((val_f24(g[4]) + val_f24(g[5])) + (lane < 48 ? val_f24(g[6]) : 0.f));
    s = xrow_sum(s);
    if (lane < 16) *op = (bf16_t)f2bf(x1 + rbf(own_gb * rbf(s)));
    if (og == 0 && lane == 0) __hip_atomic_store((gu32*)(p.ws + WS_EPOCH), tag, __ATOMIC_RELAXED, __HIP_MEMORY_SCOPE_AGENT);
    if (v == 0) STAMP(16);
  }
  if (is_ffn) {
    const int ua = 80 * v + lane, ub = 80 * v + 64 + (lane & 15);
    const gu64* xa = XG + (ua >> 3) * 16 + (ua & 7); const gu64* xb = XG + (ub >> 3) * 16 + (ub & 7);
    gu64* dq = DG + (size_t)(10 * v + g4) * 512 + head * 16 + c;
    PINP(xa); PINP(xb); PINP(dq);
    unsigned xo0, xo1;
    for (unsigned n = SPIN_G; n != 0u; --n) {
      const u64 g0 = ldg(xa), g1 = ldg(xb);
      xo0 = (unsigned)(g0 >> 32); xo1 = (unsigned)(g1 >> 32);
      if (none_bad(bad_pair(g0, tag) | bad_pair(g1, tag))) break;
      if (PSLEEP) __builtin_amdgcn_s_sleep(PSLEEP);
    }
    if (v == 0) STAMP(13);
    {
      const unsigned xm = lane < 16 ? xo1 : 0u;
      float s1 = dot2(xm, 0x3f803f80u, dot2(xo0, 0x3f803f80u, 0.f)), s2 = dot2(xm, xm, dot2(xo0, xo0, 0.f));
      s1 += dpp<XOR1>(s1); s2 += dpp<XOR1>(s2); s1 += dpp<XOR2>(s1); s2 += dpp<XOR2>(s2);
      s1 += dpp<XOR7>(s1); s2 += dpp<XOR7>(s2); s1 += dpp<XOR8>(s1); s2 += dpp<XOR8>(s2);
      s1 = xrow_sum(s1); s2 = xrow_sum(s2);
      if (lane == 0) *(LAS f32x2*)(LF + L_ST + 2 * v) = f32x2{s1, s2};
    }
    if (v == 0) STAMP(25);
    BARRIER();
    if (v == 0) STAMP(26);
    {
      const f32x4 sa = *(const LAS f32x4*)(LF + L_ST), sb = *(const LAS f32x4*)(LF + L_ST + 4);
      const float mean_f = ((sa[0] + sa[2]) + (sb[0] + sb[2])) * (1.f / D);
      const float ex2 = ((sa[1] + sa[3]) + (sb[1] + sb[3])) * (1.f / D);
      float var_f = fmaf(-mean_f, mean_f, ex2);
      if (__builtin_amdgcn_ballot_w64(var_f * 256.f < ex2) != 0ull) {
        float vs = 0.f;
#pragma unroll
        for (int i = 0; i < 5; ++i) { const int u_ = lane + 64 * i; const unsigned xp = (unsigned)(ldg(XG + (u_ >> 3) * 16 + (u_ & 7)) >> 32); const float d0 = bflo(xp) - mean_f, d1 = bfhi(xp) - mean_f; vs = fmaf(d0, d0, vs); vs = fmaf(d1, d1, vs); }
        var_f = wave_sum(vs) * (1.f / D);
      }
      const float var = rbf(var_f), mean = rbf(mean_f);
      const float rs = __builtin_amdgcn_rcpf(rbf(__builtin_amdgcn_sqrtf(rbf(var + 1e-5f))));
#define LNPAIR(xp, ma, mb_) ({ const unsigned d_ = pk2(bflo(xp) - mean, bfhi(xp) - mean); const unsigned n_ = pk2(bflo(d_) * rs, bfhi(d_) * rs); \
        const unsigned m_ = pk2(bflo(n_) * bfhi(ma), bfhi(n_) * bfhi(mb_)); pk2(bflo(m_) + bflo(ma), bfhi(m_) + bflo(mb_)); })
      L[L_H2 + 80 * v + lane] = LNPAIR(xo0, mbA[0], mbB[0]);
      const unsigned h1 = LNPAIR(xo1, mbA[1], mbB[1]);
      if (lane < 16) L[L_H2 + 80 * v + 64 + lane] = h1;
#undef LNPAIR
    }
    if (v == 0) STAMP(27);
    v4u wd[2][10];
    {
      v4u A[5];
#pragma unroll
      for (int kb = 0; kb < 5; ++kb) A[kb] = *(const LAS v4u*)(L + L_H2 + 80 * v + 16 * kb + 4 * g4);
      f32x4 acc[8];
#pragma unroll
      for (int cg = 0; cg < 8; ++cg) acc[cg] = f32x4{0.f, 0.f, 0.f, 0.f};
      const LAS unsigned char* src = LB + B_WD + v * 20480 + (8 * g4 + tq) * 320 + tp * 8;
#pragma unroll
      for (int kb = 0; kb < 5; ++kb) {
#pragma unroll
        for (int cg = 0; cg < 8; ++cg) acc[cg] = mfma(A[kb], wf[kb][cg], acc[cg]);
        __builtin_amdgcn_sched_barrier(0);
        if (kb < 4) {
#pragma unroll
          for (int j = 0; j < 5; ++j) { const int f = 5 * kb + j; wd[f / 10][f % 10] = tr_frag(src + (f / 10) * 32 * 320 + (f % 10) * 32, 4 * 320); }
        }
        __builtin_amdgcn_sched_barrier(0);
      }
      LF[L_RGU + v * 128 + lane] = sel4(g4, acc[0][0], acc[1][0], acc[2][0], acc[3][0]);
      LF[L_RGU + v * 128 + 64 + lane] = sel4(g4, acc[4][0], acc[5][0], acc[6][0], acc[7][0]);
    }
    if (v == 0) STAMP(28);
    BARRIER();
    if (v == 0) STAMP(29);
    {
      const float G = rbf((LF[L_RGU + lane] + LF[L_RGU + 128 + lane]) + (LF[L_RGU + 256 + lane] + LF[L_RGU + 384 + lane]));
      const float U = rbf((LF[L_RGU + 64 + lane] + LF[L_RGU + 192 + lane]) + (LF[L_RGU + 320 + lane] + LF[L_RGU + 448 + lane]));
      ((LAS bf16_t*)(L + L_ACT + 32 * v))[lane] = (bf16_t)f2bf(rbf(silu(G)) * U);
      if (v == 0) STAMP(14);
      const v4u A0 = *(const LAS v4u*)(L + L_ACT + 32 * v + 4 * g4), A1 = *(const LAS v4u*)(L + L_ACT + 32 * v + 16 + 4 * g4);
      f32x4 acc[10];
#pragma unroll
      for (int cg = 0; cg < 10; ++cg) { acc[cg] = mfma(A0, wd[0][cg], f32x4{0.f, 0.f, 0.f, 0.f}); acc[cg] = mfma(A1, wd[1][cg], acc[cg]); }
      stg(dq, gr_f24(tag, sel4(g4, acc[0][0], acc[1][0], acc[2][0], acc[3][0])));
      stg(dq + 4 * 512, gr_f24(tag, sel4(g4, acc[4][0], acc[5][0], acc[6][0], acc[7][0])));
      if (lane < 32) stg(dq + 8 * 512, gr_f24(tag, g4 == 0 ? acc[8][0] : acc[9][0]));
    }
    if (v == 0) STAMP(17);
  }
  if (v == 0) STAMP_DUMP();
}

extern "C" void kernel_launch(void* const* d_in, const int* in_sizes, int n_in, void* d_out, int out_size, void* d_ws, size_t ws_size, hipStream_t stream) {
  Params p;
  p.x = (const bf16_t*)d_in[0]; p.kc = (const bf16_t*)d_in[1]; p.vc = (const bf16_t*)d_in[2]; p.wq = (const bf16_t*)d_in[3]; p.wo = (const bf16_t*)d_in[4];
  p.wg = (const bf16_t*)d_in[6]; p.wu = (const bf16_t*)d_in[7]; p.wd = (const bf16_t*)d_in[8]; p.c = (const bf16_t*)d_in[9]; p.wmod = (const bf16_t*)d_in[10];
  p.out = (bf16_t*)d_out; p.ws = (unsigned char*)d_ws;
  (void)hipFuncSetAttribute((const void*)adaln_block_kernel, hipFuncAttributeMaxDynamicSharedMemorySize, LDS_BYTES);
  adaln_block_kernel<<<NCU, NTHR, LDS_BYTES, stream>>>(p);
}
```

```cpp
#include <hip/hip_runtime.h>
#include <hip/hip_bf16.h>
#include <stdint.h>

#define TD 6
#define PACE 0
#define PACE0 0
#define KVDELAY 0
#define PSLEEP 0
#define TLBWARM 2
#define TLBSTEP 4
#define WOLEAD 1
#define GATE 1
#define STAG 12
#define QDELAY 64
#define STAMPS 0
#define NT_Q 54
#define NT_M 64
#define NT_F 42
#define NT_P ((512 - 2 * NT_Q - 2 * NT_M - NT_F) / 3)
static_assert(2 * NT_Q + 2 * NT_M + NT_F + 3 * NT_P == 512, "tiles of a head");
#define NT_G0 54
#define NT_G1 60
#define NT_G2 ((512 - 2 * NT_G0 - 2 * NT_G1) / 4)
static_assert(2 * NT_G0 + 2 * NT_G1 + 4 * NT_G2 == 512, "tiles of a head");
#define NT_H0 52
#define NT_H1 ((512 - 2 * NT_H0) / 6)
static_assert(2 * NT_H0 + 6 * NT_H1 == 512, "tiles of a head");
static_assert(NT_F >= 36, "every stream wave takes at least 9 tiles (the catch-up schedule)");

typedef unsigned short bf16_t;
typedef unsigned long long u64;
typedef unsigned v4u __attribute__((ext_vector_type(4)));
typedef float f32x4 __attribute__((ext_vector_type(4)));
typedef float f32x2 __attribute__((ext_vector_type(2)));
typedef unsigned v2u __attribute__((ext_vector_type(2)));
typedef __bf16 bf16x2 __attribute__((ext_vector_type(2)));
typedef __bf16 bf16x8 __attribute__((ext_vector_type(8)));
typedef short v4s __attribute__((ext_vector_type(4)));
#define GAS __attribute__((address_space(1)))
#define LAS __attribute__((address_space(3)))
typedef GAS u64 gu64;
typedef GAS unsigned gu32;

constexpr int D = 640, HQ = 32, SK = 8192, DH = 128, F = 1728, NMOD = 6 * D;
constexpr int NCU = 256, NTHR = 512, NFFN = 27;
constexpr float LOG2E = 1.4426950408889634f;

constexpr size_t WS_EPOCH = 0;
constexpr size_t WS_XG = 393216 + 32768;
constexpr size_t WS_MG = 8192;
constexpr size_t WS_QG = 32768;
constexpr size_t WS_YG = 65536;
constexpr size_t WS_DG = 262144;
constexpr size_t WS_PG = 524288;
constexpr int PG_STRIDE = 160;
constexpr size_t WS_STAMP = 64u << 20;

constexpr int L_FLAG_Q = 0, L_CNT_S1 = 1, L_FLAG_S1 = 2, L_FLAG_E = 3, L_E = 4, L_CNT_M = 5, L_FLAG_H = 6, L_CNT_G = 7, L_CNT_W = 8, L_FLAG_L = 9, L_CNT_OV = 10, L_CNT_Q = 11;
constexpr int L_Q = 32;
constexpr int L_REC = 96;
constexpr int L_O = 624;
constexpr int L_MR = 704;
constexpr int L_SC = 960;
constexpr int L_H2 = 1600;
constexpr int L_ST = 1920;
constexpr int L_RGU = 1936;
constexpr int L_ACT = 2448;
constexpr int L_STAMP = 2816;
constexpr int B_WO = 12288;
constexpr int B_STG = 32768;
constexpr int B_WD = 49152;
constexpr int LDS_BYTES = 131072;

constexpr int CODE_BYTES = 29 * 1024;
constexpr unsigned SPIN_G = 1u << 15;
constexpr unsigned SPIN_L = 1u << 20;

struct Params {
  const bf16_t* x; const bf16_t* kc; const bf16_t* vc; const bf16_t* wq; const bf16_t* wo;
  const bf16_t* wg; const bf16_t* wu; const bf16_t* wd; const bf16_t* c; const bf16_t* wmod;
  bf16_t* out; unsigned char* ws;
};

__device__ __forceinline__ float bf2f(unsigned b) { return __uint_as_float(b << 16); }
__device__ __forceinline__ float bfhi(unsigned b) { return __uint_as_float(b & 0xffff0000u); }
__device__ __forceinline__ float bflo(unsigned b) { return __uint_as_float(b << 16); }
__device__ __forceinline__ unsigned f2bf(float f) { __bf16 b = (__bf16)f; return (unsigned)__builtin_bit_cast(unsigned short, b); }
__device__ __forceinline__ float rbf(float f) { return bf2f(f2bf(f)); }
__device__ __forceinline__ unsigned pk2(float lo, float hi) { return __builtin_bit_cast(unsigned, __builtin_convertvector(f32x2{lo, hi}, bf16x2)); }
__device__ __forceinline__ float silu(float x) { return x * __builtin_amdgcn_rcpf(1.f + __builtin_amdgcn_exp2f(-x * LOG2E)); }
__device__ __forceinline__ float dot2(unsigned a, unsigned b, float acc) {
  return __builtin_amdgcn_fdot2_f32_bf16(__builtin_bit_cast(bf16x2, a), __builtin_bit_cast(bf16x2, b), acc, false);
}
template <int CTRL> __device__ __forceinline__ float dpp(float x) {
  return __builtin_bit_cast(float, __builtin_amdgcn_mov_dpp(__builtin_bit_cast(int, x), CTRL, 0xf, 0xf, true));
}
template <int CTRL> __device__ __forceinline__ unsigned dppu(unsigned x) { return (unsigned)__builtin_amdgcn_mov_dpp((int)x, CTRL, 0xf, 0xf, true); }
#define MAX_DPP(x, ctrl) ({ float r_; asm("s_nop 1\n\tv_max_f32_dpp %0, %1, %1 " ctrl " row_mask:0xf bank_mask:0xf bound_ctrl:1" : "=v"(r_) : "v"(x)); r_; })
constexpr int XOR1 = 0xB1, XOR2 = 0x4E, XOR7 = 0x141, XOR8 = 0x128, ROR4 = 0x124, BCAST0 = 0x150;
__device__ __forceinline__ float swap32_sum(float a, float b) {
  auto t = __builtin_amdgcn_permlane32_swap(__float_as_uint(a), __float_as_uint(b), false, false);
  return __uint_as_float(t[0]) + __uint_as_float(t[1]);
}
__device__ __forceinline__ float swap16_sum(float a, float b) {
  auto t = __builtin_amdgcn_permlane16_swap(__float_as_uint(a), __float_as_uint(b), false, false);
  return __uint_as_float(t[0]) + __uint_as_float(t[1]);
}
__device__ __forceinline__ float xrow_max(float x) {
  auto s = __builtin_amdgcn_permlane16_swap(__float_as_uint(x), __float_as_uint(x), false, false);
  x = fmaxf(__uint_as_float(s[0]), __uint_as_float(s[1]));
  auto t = __builtin_amdgcn_permlane32_swap(__float_as_uint(x), __float_as_uint(x), false, false);
  return fmaxf(__uint_as_float(t[0]), __uint_as_float(t[1]));
}
__device__ __forceinline__ float xrow_sum(float x) { x = swap16_sum(x, x); return swap32_sum(x, x); }
__device__ __forceinline__ float wave_sum(float x) {
  x += dpp<XOR1>(x); x += dpp<XOR2>(x); x += dpp<XOR7>(x); x += dpp<XOR8>(x);
  return xrow_sum(x);
}
__device__ __forceinline__ void reduce_rows(const float* x, float& e0, float& e1) {
  const float y0 = swap32_sum(x[0], x[4]), y1 = swap32_sum(x[1], x[5]), y2 = swap32_sum(x[2], x[6]), y3 = swap32_sum(x[3], x[7]);
  e0 = swap16_sum(y0, y2); e1 = swap16_sum(y1, y3);
}
__device__ __forceinline__ float reduce_oct(const float* x, int lane) {
  float e0, e1; reduce_rows(x, e0, e1);
  const bool b3 = (lane & 8) != 0;
  return (b3 ? e1 : e0) + dpp<XOR8>(b3 ? e0 : e1);
}

__device__ __forceinline__ u64 ldg(const gu64* p) { return __hip_atomic_load(p, __ATOMIC_RELAXED, __HIP_MEMORY_SCOPE_AGENT); }
__device__ __forceinline__ void stg(gu64* p, u64 v) { __hip_atomic_store(p, v, __ATOMIC_RELAXED, __HIP_MEMORY_SCOPE_AGENT); }
__device__ __forceinline__ u64 gr_pair(unsigned tag, unsigned pay) { return ((u64)pay << 32) | tag; }
__device__ __forceinline__ u64 gr_f24(unsigned tag, float v) { return ((u64)((__float_as_uint(v) + 0x80u) & 0xffffff00u) << 32) | tag; }
__device__ __forceinline__ unsigned bad_pair(u64 g, unsigned tag) { return (unsigned)g ^ tag; }
__device__ __forceinline__ unsigned bad_f24(u64 g, unsigned tag) { return ((unsigned)g ^ tag) | ((unsigned)(g >> 32) & 0xffu); }
__device__ __forceinline__ float val_f24(u64 g) { return __uint_as_float((unsigned)(g >> 32)); }
__device__ __forceinline__ bool none_bad(unsigned bad) { return __builtin_amdgcn_ballot_w64(bad != 0u) == 0ull; }
__device__ __forceinline__ bool ok_pair(u64 g, unsigned tag) { return (unsigned)g == tag; }
__device__ __forceinline__ bool all_ok(bool ok) { return __builtin_amdgcn_ballot_w64(!ok) == 0ull; }
#define PINP(ptr) asm volatile("" : "+v"(ptr))

__device__ __forceinline__ void lds_wait(volatile LAS unsigned* w, unsigned want) {
#define LSLEEP 1
  for (unsigned n = 0; *w != want && n < SPIN_L; ++n) __builtin_amdgcn_s_sleep(LSLEEP);
  asm volatile("" ::: "memory");
}
#define WAIT_LGKM() asm volatile("s_waitcnt lgkmcnt(0)" ::: "memory")
#define BARRIER() do { asm volatile("s_waitcnt lgkmcnt(0)" ::: "memory"); __builtin_amdgcn_s_barrier(); asm volatile("" ::: "memory"); } while (0)

#define STAMP(i) do { } while (0)
#define STAMP_DUMP() do { } while (0)

struct AttnState { float m, l; float acc[8]; };

__device__ __forceinline__ void attn_tile(const v4u (&K)[4], const v4u (&V)[4], const v4u& q, AttnState& st, int c) {
  float x[4];
#pragma unroll
  for (int i = 0; i < 4; ++i) {
    float d = dot2(K[i].x, q.x, 0.f);
    d = dot2(K[i].y, q.y, d); d = dot2(K[i].z, q.z, d); d = dot2(K[i].w, q.w, d);
    x[i] = d;
  }
  const bool b3 = (c & 8) != 0, b2 = (c & 4) != 0;
  const float k0 = b3 ? x[2] : x[0], s0 = b3 ? x[0] : x[2];
  const float k1 = b3 ? x[3] : x[1], s1 = b3 ? x[1] : x[3];
  const float y0 = k0 + dpp<XOR8>(s0), y1 = k1 + dpp<XOR8>(s1);
  const float k2 = b2 ? y1 : y0, s2 = b2 ? y0 : y1;
  float z = k2 + dpp<XOR7>(s2);
  z += dpp<XOR2>(z); z += dpp<XOR1>(z);
  const float s = rbf(rbf(z) * 0.08838834764831845f);
  float pm = MAX_DPP(s, "row_ror:4"); pm = MAX_DPP(pm, "row_ror:8");
  const float mn = fmaxf(st.m, pm);
  if (__builtin_amdgcn_ballot_w64(mn > st.m) != 0ull) {
    const float al = __builtin_amdgcn_exp2f((st.m - mn) * LOG2E);
    st.l *= al;
#pragma unroll
    for (int j = 0; j < 8; ++j) st.acc[j] *= al;
    st.m = mn;
  }
  const float pr = __builtin_amdgcn_exp2f((s - st.m) * LOG2E);
  st.l += pr;
  const unsigned plo = f2bf(pr), phi = plo << 16;
#define PV(i) { const unsigned a = dppu<BCAST0 + 4 * i>(plo), b = dppu<BCAST0 + 4 * i>(phi); \
    st.acc[0] = dot2(V[i].x, a, st.acc[0]); st.acc[1] = dot2(V[i].x, b, st.acc[1]); st.acc[2] = dot2(V[i].y, a, st.acc[2]); st.acc[3] = dot2(V[i].y, b, st.acc[3]); \
    st.acc[4] = dot2(V[i].z, a, st.acc[4]); st.acc[5] = dot2(V[i].z, b, st.acc[5]); st.acc[6] = dot2(V[i].w, a, st.acc[6]); st.acc[7] = dot2(V[i].w, b, st.acc[7]); }
  PV(0) PV(1) PV(2) PV(3)
#undef PV
}

__device__ __forceinline__ void interleave(const v4u& r0, const v4u& r1, unsigned* o) {
#pragma unroll
  for (int j = 0; j < 4; ++j) { o[2 * j] = __builtin_amdgcn_perm(r1[j], r0[j], 0x05040100u); o[2 * j + 1] = __builtin_amdgcn_perm(r1[j], r0[j], 0x07060302u); }
}
__device__ __forceinline__ f32x4 mfma(const v4u& a, const v4u& b, const f32x4& c) {
  return __builtin_amdgcn_mfma_f32_16x16x32_bf16(__builtin_bit_cast(bf16x8, a), __builtin_bit_cast(bf16x8, b), c, 0, 0, 0);
}
__device__ __forceinline__ v4u tr_frag(const LAS unsigned char* p, int stride4) {
  const v2u lo = __builtin_bit_cast(v2u, __builtin_amdgcn_ds_read_tr16_b64_v4i16((LAS v4s*)p));
  const v2u hi = __builtin_bit_cast(v2u, __builtin_amdgcn_ds_read_tr16_b64_v4i16((LAS v4s*)(p + stride4)));
  return v4u{lo.x, lo.y, hi.x, hi.y};
}
#define GLDS(gsrc, ldst, aux) __builtin_amdgcn_global_load_lds((const GAS void*)(gsrc), (LAS void*)(ldst), 16, 0, aux)
__device__ __forceinline__ float sel4(int q4, float a, float b, float c, float d) { return q4 == 0 ? a : q4 == 1 ? b : q4 == 2 ? c : d; }
__device__ __forceinline__ v4u ldnt(const bf16_t* p) { return __builtin_nontemporal_load((const GAS v4u*)p); }
#define PIN8(a) asm volatile("" : "+v"((a)[0]), "+v"((a)[1]), "+v"((a)[2]), "+v"((a)[3]), "+v"((a)[4]), "+v"((a)[5]), "+v"((a)[6]), "+v"((a)[7]))

__global__ void __launch_bounds__(NTHR, 2) adaln_block_kernel(Params p) {
  extern __shared__ __attribute__((aligned(16))) unsigned lds_raw[];
  LAS unsigned* L = (LAS unsigned*)lds_raw;
  volatile LAS unsigned* LV = (volatile LAS unsigned*)lds_raw;
  LAS float* LF = (LAS float*)lds_raw;
  LAS unsigned char* LB = (LAS unsigned char*)lds_raw;
  u64 code_base;
  asm volatile("s_getpc_b64 %0" : "=s"(code_base));
  code_base &= ~255ull;
  const int tid = threadIdx.x, lane = tid & 63, wave = __builtin_amdgcn_readfirstlane(tid >> 6);
  const int r = lane >> 4, c = lane & 15;
  const int b = blockIdx.x, X = b & 7, J = b >> 3, sp = J & 7;
  const int head = 4 * X + (J >> 3);
  const bool fh = head < NFFN;
  const bool is_qc = sp < 2, is_ffn = fh && sp == 5;
  const int mu = (sp == 2 || sp == 3) ? 2 * head + sp - 2 : 999;
  const int own = head;
  const bool is_mod = mu < 60, is_own = sp == 4 && head < 10;
  const int cf = fh, cg = !fh && head < 30, ch_ = head >= 30;
  const int nq_ = cf * NT_Q + cg * NT_G0 + ch_ * NT_H0, nm_ = cf * NT_M + cg * NT_G1 + ch_ * NT_H1;
  const int np_ = cf * NT_P + cg * NT_G2 + ch_ * NT_H1, nf_ = cf * NT_F + cg * NT_G2 + ch_ * NT_H1;
  const int t0 = min(sp, 2) * nq_ + min(max(sp - 2, 0), 2) * nm_ + (int)(sp > 4) * np_ + (int)(sp > 5) * nf_ + (int)(sp > 6) * np_;
  const int nt = (int)(sp < 2) * nq_ + (int)(sp == 2 || sp == 3) * nm_ + (int)(sp == 5) * nf_ + (int)(sp == 4 || sp > 5) * np_;
  if (tid < 16) L[tid] = 0u;
  asm volatile("" :: "s"(p.kc), "s"(p.vc), "s"(p.ws), "s"(p.wq), "s"(p.wmod), "s"(p.c), "s"(p.x), "s"(t0), "s"(nt));
  BARRIER();
  gu64* const XG = (gu64*)(p.ws + WS_XG); gu64* const MG = (gu64*)(p.ws + WS_MG); gu64* const YG = (gu64*)(p.ws + WS_YG);
  gu64* const DG = (gu64*)(p.ws + WS_DG); gu64* const QG = (gu64*)(p.ws + WS_QG); gu64* const PG = (gu64*)(p.ws + WS_PG);

  if (__builtin_expect(wave < 4, 1)) {
    const size_t base = ((size_t)head * SK + (size_t)t0 * 16) * DH;
    const __amdgpu_buffer_rsrc_t rk = __builtin_amdgcn_make_buffer_rsrc((void*)(p.kc + base), 0, nt * 4096, 0x00020000);
    const __amdgpu_buffer_rsrc_t rv = __builtin_amdgcn_make_buffer_rsrc((void*)(p.vc + base), 0, nt * 4096, 0x00020000);
    const int voff = lane * 16;
    const int nw = (nt - wave + 3) >> 2;
    v4u K[TD][4], V[TD][4];
#define ISSUE(d, t) do { const int so_ = (wave + 4 * (t)) * 4096; \
    _Pragma("unroll") for (int i_ = 0; i_ < 4; ++i_) K[d][i_] = __builtin_amdgcn_raw_buffer_load_b128(rk, voff + 1024 * i_, so_, 2); \
    _Pragma("unroll") for (int i_ = 0; i_ < 4; ++i_) V[d][i_] = __builtin_amdgcn_raw_buffer_load_b128(rv, voff + 1024 * i_, so_, 2); \
    __builtin_amdgcn_sched_barrier(0); } while (0)
#define PACE_WAIT(n) do { asm volatile("s_waitcnt vmcnt(%0)" :: "n"(n) : "memory"); __builtin_amdgcn_sched_barrier(0); } while (0)
#define QGATE 1
    if (QGATE == 1 && is_qc) lds_wait(LV + L_FLAG_H, 1u);
    if (QGATE == 2 && is_qc) lds_wait(LV + L_CNT_Q, 4u);
    if (is_mod) lds_wait(LV + L_CNT_G, 1u);
    if (KVDELAY) __builtin_amdgcn_s_sleep(KVDELAY);
    if (wave == 0) STAMP(24);
    ISSUE(0, 0);
#pragma unroll
    for (int d = 1; d < TD; ++d) { PACE_WAIT(PACE0); ISSUE(d, d); }
    const unsigned ov = (unsigned)(size_t)(LB + B_WD + wave * 16384);
#define DMA4(m0v, rsrc, so) do { unsigned keep_; asm volatile("s_mov_b32 %0, m0\n\ts_mov_b32 m0, %1\n\ts_nop 0\n\t" \
      "buffer_load_dwordx4 %2, %3, %4 offen nt lds\n\tbuffer_load_dwordx4 %2, %3, %4 offen offset:1024 nt lds\n\t" \
      "buffer_load_dwordx4 %2, %3, %4 offen offset:2048 nt lds\n\tbuffer_load_dwordx4 %2, %3, %4 offen offset:3072 nt lds\n\t" \
      "s_mov_b32 m0, %0" : "=&s"(keep_) : "s"(m0v), "v"(voff), "s"(rsrc), "s"(so) : "memory"); } while (0)
#define DMA_TILE(e, t) do { const int so_ = (wave + 4 * (t)) * 4096; DMA4(ov + (e) * 8192, rk, so_); DMA4(ov + (e) * 8192 + 4096, rv, so_); __builtin_amdgcn_sched_barrier(0); } while (0)
#define LD_OV(d, e) do { const LAS v4u* src_ = (const LAS v4u*)(LB + B_WD + wave * 16384 + (e) * 8192) + lane; \
      _Pragma("unroll") for (int i_ = 0; i_ < 4; ++i_) { K[d][i_] = src_[64 * i_]; V[d][i_] = src_[256 + 64 * i_]; } __builtin_amdgcn_sched_barrier(0); } while (0)
    PACE_WAIT(PACE0); DMA_TILE(0, 6);
    PACE_WAIT(PACE0); DMA_TILE(1, 7);
    lds_wait(LV + L_FLAG_Q, 1u);
    if (wave == 0) STAMP(1);
    const v4u q = *(const LAS v4u*)(L + L_Q + 4 * c);
    const unsigned tag = L[L_E] + 1u;
    AttnState st; st.m = -INFINITY; st.l = 0.f;
#pragma unroll
    for (int j = 0; j < 8; ++j) st.acc[j] = 0.f;
#define TILE(d) do { attn_tile(K[d], V[d], q, st, c); __builtin_amdgcn_sched_barrier(0); } while (0)
    static_assert(TD == 6, "the catch-up schedule is written for six slots");
    TILE(0); PACE_WAIT(0); ISSUE(0, 8);
    TILE(1); TILE(2); TILE(3); TILE(4);
    LD_OV(4, 0);
    PACE_WAIT(PACE); ISSUE(1, 9);
    TILE(5);
    LD_OV(5, 1);
    WAIT_LGKM();
    if (lane == 0) __hip_atomic_fetch_add((LAS unsigned*)(L + L_CNT_OV), 1u, __ATOMIC_RELAXED, __HIP_MEMORY_SCOPE_WORKGROUP);
    TILE(4); TILE(5);
    TILE(0);
#pragma nounroll
    for (int u = 9; u < nw; u += TD) {
#pragma unroll
      for (int k = 0; k < TD; ++k) {
        if (u + k < nw) {
          PACE_WAIT(PACE);
          ISSUE((k + 2) % TD, u + k + 1);
          if (u + k + 1 + WOLEAD == nw && wave == 0 && lane == 0) LV[L_FLAG_L] = 1u;
          TILE((k + 1) % TD);
        }
      }
    }
#undef TILE
    if (wave == 0 && lane == 0) LV[L_FLAG_L] = 1u;
#undef ISSUE
    if (wave == 0) STAMP(2);
    const float M = xrow_max(st.m);
    const float sc = __builtin_amdgcn_exp2f((st.m - M) * LOG2E);
    float l = ((c & 3) == 0) ? st.l * sc : 0.f;
    l += dpp<ROR4>(l); l += dpp<XOR8>(l); l = xrow_sum(l);
#pragma unroll
    for (int j = 0; j < 8; ++j) st.acc[j] *= sc;
    float e0, e1; reduce_rows(st.acc, e0, e1);
    LAS float* rec = LF + L_REC + wave * 132;
    *(LAS f32x2*)(rec + 4 + 8 * c + 2 * r) = f32x2{e0, e1};
    if (lane == 0) { rec[0] = M; rec[1] = l; }
    WAIT_LGKM();
    unsigned old = 0;
    if (lane == 0) old = __hip_atomic_fetch_add((LAS unsigned*)(L + L_CNT_S1), 1u, __ATOMIC_RELAXED, __HIP_MEMORY_SCOPE_WORKGROUP);
    old = __builtin_amdgcn_readfirstlane(old);
    if (old == 3u) {
      asm volatile("" ::: "memory");
      const LAS float* R = LF + L_REC;
      const float m0 = R[0], m1 = R[132], m2 = R[264], m3 = R[396];
      const float MM = fmaxf(fmaxf(m0, m1), fmaxf(m2, m3));
      const float w0 = __builtin_amdgcn_exp2f((m0 - MM) * LOG2E), w1 = __builtin_amdgcn_exp2f((m1 - MM) * LOG2E);
      const float w2 = __builtin_amdgcn_exp2f((m2 - MM) * LOG2E), w3 = __builtin_amdgcn_exp2f((m3 - MM) * LOG2E);
      const float LL = (w0 * R[1] + w1 * R[133]) + (w2 * R[265] + w3 * R[397]);
      const float oa = (w0 * R[4 + lane] + w1 * R[136 + lane]) + (w2 * R[268 + lane] + w3 * R[400 + lane]);
      const float ob = (w0 * R[68 + lane] + w1 * R[200 + lane]) + (w2 * R[332 + lane] + w3 * R[464 + lane]);
      gu64* rp = PG + (size_t)(head * 8 + sp) * PG_STRIDE;
      stg(rp + lane, gr_f24(tag, oa));
      stg(rp + 64 + lane, gr_f24(tag, ob));
      if (lane < 2) stg(rp + 128 + lane, gr_f24(tag, lane == 0 ? MM : LL));
      if (lane == 0) LV[L_FLAG_S1] = 1u;
      STAMP(3);
    }
    return;
  }

  const int v = wave - 4, t = tid - 256;
  const int r8 = lane >> 3, c8 = lane & 7;
  unsigned tag = 0;
  if (is_qc) {
    const unsigned e_in = __hip_atomic_load((gu32*)(p.ws + WS_EPOCH), __ATOMIC_RELAXED, __HIP_MEMORY_SCOPE_AGENT);
    v4u w[20];
    {
      const bf16_t* src = p.wq + (size_t)(160 * v + 2 * r8) * (HQ * DH) + head * DH + 64 * sp + 8 * c8;
#pragma unroll
      for (int i = 0; i < 10; ++i) { w[2 * i] = ldnt(src + (size_t)(16 * i) * (HQ * DH)); w[2 * i + 1] = ldnt(src + (size_t)(16 * i + 1) * (HQ * DH)); }
    }
    unsigned xs[5];
#pragma unroll
    for (int i = 0; i < 5; ++i) xs[i] = ((const GAS unsigned*)p.x)[lane + 64 * i];
    const unsigned xo0 = ((const GAS unsigned*)p.x)[80 * v + lane], xo1 = ((const GAS unsigned*)p.x)[80 * v + 64 + (lane & 15)];
    __builtin_amdgcn_sched_barrier(0);
    if (v == 0 && lane == 0) { LV[L_E] = e_in; WAIT_LGKM(); LV[L_FLAG_E] = 1u; }
    float n0a, n0b, n1a, n1b;
    {
      float s = 0.f;
#pragma unroll
      for (int i = 0; i < 5; ++i) s += bflo(xs[i]) + bfhi(xs[i]);
      const float mean_f = wave_sum(s) * (1.f / D);
      float vs = 0.f;
#pragma unroll
      for (int i = 0; i < 5; ++i) { const float d0 = bflo(xs[i]) - mean_f, d1 = bfhi(xs[i]) - mean_f; vs = fmaf(d0, d0, vs); vs = fmaf(d1, d1, vs); }
      const float var = rbf(wave_sum(vs) * (1.f / D)), mean = rbf(mean_f);
      const float rs = __builtin_amdgcn_rcpf(rbf(__builtin_amdgcn_sqrtf(rbf(var + 1e-5f))));
      n0a = rbf(rbf(bflo(xo0) - mean) * rs); n0b = rbf(rbf(bfhi(xo0) - mean) * rs);
      n1a = rbf(rbf(bflo(xo1) - mean) * rs); n1b = rbf(rbf(bfhi(xo1) - mean) * rs);
      asm volatile("" : "+v"(n0a), "+v"(n0b), "+v"(n1a), "+v"(n1b));
    }
    unsigned wi[10][8];
#pragma unroll
    for (int i = 0; i < 10; ++i) { interleave(w[2 * i], w[2 * i + 1], wi[i]); PIN8(wi[i]); }
    if (lane == 0) __hip_atomic_fetch_add((LAS unsigned*)(L + L_CNT_Q), 1u, __ATOMIC_RELAXED, __HIP_MEMORY_SCOPE_WORKGROUP);
    lds_wait(LV + L_FLAG_E, 1u);
    tag = LV[L_E] + 1u;
    if (v == 0) STAMP(18);
    unsigned s0, c0, s1, c1;
    {
      const gu64* m0 = MG + 80 * v + lane; const gu64* m1 = MG + 80 * v + 64 + (lane & 15);
      for (unsigned n = 0; n < SPIN_G; ++n) {
        const u64 a0 = ldg(m0), b0 = ldg(m0 + 320), a1 = ldg(m1), b1 = ldg(m1 + 320);
        s0 = (unsigned)(a0 >> 32); c0 = (unsigned)(b0 >> 32); s1 = (unsigned)(a1 >> 32); c1 = (unsigned)(b1 >> 32);
        if (all_ok(ok_pair(a0, tag) & ok_pair(b0, tag) & ok_pair(a1, tag) & ok_pair(b1, tag))) break;
      }
    }
    if (v == 0) STAMP(5);
    L[L_H2 + 80 * v + lane] = f2bf(rbf(n0a * rbf(1.f + bflo(c0))) + bflo(s0)) | (f2bf(rbf(n0b * rbf(1.f + bfhi(c0))) + bfhi(s0)) << 16);
    if (lane < 16) L[L_H2 + 80 * v + 64 + lane] = f2bf(rbf(n1a * rbf(1.f + bflo(c1))) + bflo(s1)) | (f2bf(rbf(n1b * rbf(1.f + bfhi(c1))) + bfhi(s1)) << 16);
    WAIT_LGKM();
    float acc[8];
#pragma unroll
    for (int j = 0; j < 8; ++j) acc[j] = 0.f;
#pragma unroll
    for (int i = 0; i < 10; ++i) {
      const unsigned hp = L[L_H2 + 80 * v + 8 * i + r8];
#pragma unroll
      for (int j = 0; j < 8; ++j) acc[j] = dot2(wi[i][j], hp, acc[j]);
    }
    LF[L_MR + 64 * v + 8 * c8 + r8] = reduce_oct(acc, lane);
    WAIT_LGKM();
    unsigned old = 0;
    if (lane == 0) old = __hip_atomic_fetch_add((LAS unsigned*)(L + L_CNT_M), 1u, __ATOMIC_RELAXED, __HIP_MEMORY_SCOPE_WORKGROUP);
    if (__builtin_amdgcn_readfirstlane(old) == 3u) {
      asm volatile("" ::: "memory");
      const unsigned qb = f2bf((LF[L_MR + lane] + LF[L_MR + 64 + lane]) + (LF[L_MR + 128 + lane] + LF[L_MR + 192 + lane]));
      const unsigned up = dppu<XOR1>(qb);
      if ((lane & 1) == 0) stg(QG + head * 64 + 32 * sp + (lane >> 1), gr_pair(tag, qb | (up << 16)));
      if (lane == 0) LV[L_FLAG_H] = 1u;
      STAMP(6);
    }
  } else {
    const unsigned e_in = __hip_atomic_load((gu32*)(p.ws + WS_EPOCH), __ATOMIC_RELAXED, __HIP_MEMORY_SCOPE_AGENT);
    const __amdgpu_buffer_rsrc_t rm = __builtin_amdgcn_make_buffer_rsrc((void*)p.wmod, 0, is_mod ? D * NMOD * 2 : 0, 0x00020000);
    const __amdgpu_buffer_rsrc_t rc = __builtin_amdgcn_make_buffer_rsrc((void*)p.c, 0, is_mod ? D * 2 : 0, 0x00020000);
    const unsigned cv0 = __builtin_amdgcn_raw_buffer_load_b16(rc, (160 * v + lane) * 2, 0, 0), cv1 = __builtin_amdgcn_raw_buffer_load_b16(rc, (160 * v + 64 + lane) * 2, 0, 0),
                   cv2 = __builtin_amdgcn_raw_buffer_load_b16(rc, (160 * v + 128 + (lane & 31)) * 2, 0, 0);
    v4u wm[20];
#pragma unroll
    for (int i = 0; i < 20; ++i) wm[i] = __builtin_amdgcn_raw_buffer_load_b128(rm, ((160 * v + r8) * NMOD + 64 * mu + 8 * c8) * 2, i * (8 * NMOD * 2), 2);
    __builtin_amdgcn_sched_barrier(0);
    if (v == 0 && lane == 0) { LV[L_E] = e_in; WAIT_LGKM(); LV[L_FLAG_E] = 1u; }
    if (is_mod) {
      L[L_SC + 160 * v + lane] = f2bf(silu(bf2f(cv0))); L[L_SC + 160 * v + 64 + lane] = f2bf(silu(bf2f(cv1)));
      if (lane < 32) L[L_SC + 160 * v + 128 + lane] = f2bf(silu(bf2f(cv2)));
      WAIT_LGKM();
      float acc[8];
#pragma unroll
      for (int j = 0; j < 8; ++j) acc[j] = 0.f;
#pragma unroll
      for (int i = 0; i < 20; ++i) {
        const unsigned a = L[L_SC + 160 * v + 8 * i + r8], bb = a << 16;
#pragma unroll
        for (int j = 0; j < 4; ++j) { acc[2 * j] = dot2(wm[i][j], a, acc[2 * j]); acc[2 * j + 1] = dot2(wm[i][j], bb, acc[2 * j + 1]); }
      }
      LF[L_MR + 64 * v + 8 * c8 + r8] = reduce_oct(acc, lane);
      WAIT_LGKM();
      unsigned old = 0;
      if (lane == 0) old = __hip_atomic_fetch_add((LAS unsigned*)(L + L_CNT_M), 1u, __ATOMIC_RELAXED, __HIP_MEMORY_SCOPE_WORKGROUP);
      if (__builtin_amdgcn_readfirstlane(old) == 3u) {
        asm volatile("" ::: "memory");
        const unsigned mb_ = f2bf((LF[L_MR + lane] + LF[L_MR + 64 + lane]) + (LF[L_MR + 128 + lane] + LF[L_MR + 192 + lane]));
        const unsigned up = dppu<XOR1>(mb_);
        lds_wait(LV + L_FLAG_E, 1u);
        if ((lane & 1) == 0) stg(MG + 32 * mu + (lane >> 1), gr_pair(LV[L_E] + 1u, mb_ | (up << 16)));
        if (lane == 0) LV[L_CNT_G] = 1u;
        STAMP(4);
      }
    }
    if (v == 3 && J * 1024 < CODE_BYTES) {
      const v4u cw = ((const GAS v4u*)(code_base + (u64)(J * 1024)))[lane]; asm volatile("" :: "v"(cw));
    }
  }
  if (v == 2) {
      const int k = lane & 15;
      const GAS unsigned* a = k == 0 ? (const GAS unsigned*)(p.wo + (size_t)(head * DH) * D + 80 * sp)
                            : k == 1 ? (const GAS unsigned*)(p.wo + (size_t)(head * DH + DH - 1) * D + 80 * sp)
                            : k == 2 ? (const GAS unsigned*)p.out
                            : k == 3 ? (const GAS unsigned*)(p.ws + WS_PG + (size_t)(head * 8) * PG_STRIDE * 8)
                            : k == 4 ? (const GAS unsigned*)(p.ws + WS_YG + (size_t)(5 * sp) * 4096)
                            : k == 5 ? (const GAS unsigned*)(p.ws + WS_DG)
                            : k == 6 ? (const GAS unsigned*)(p.ws + WS_DG + 131072)
                            : k == 7 ? (const GAS unsigned*)(p.ws + WS_XG)
                            : k == 8 ? (const GAS unsigned*)(p.kc + ((size_t)head * SK + (size_t)(t0 + nt) * 16 - 1) * DH)
                            : k == 9 ? (const GAS unsigned*)(p.vc + ((size_t)head * SK + (size_t)(t0 + nt) * 16 - 1) * DH)
                            : k == 10 ? (const GAS unsigned*)(p.wo + (size_t)(head * DH + 32) * D + 80 * sp)
                            : k == 11 ? (const GAS unsigned*)(p.wo + (size_t)(head * DH + 64) * D + 80 * sp)
                            : k == 12 ? (const GAS unsigned*)(p.wo + (size_t)(head * DH + 96) * D + 80 * sp)
                            : (const GAS unsigned*)(p.ws + WS_QG);
      const unsigned w_ = *a; asm volatile("" :: "v"(w_));
      const int st_ = min((lane & 31) * TLBSTEP, nt - 1);
      const bf16_t* kv = lane < 32 ? p.kc : p.vc;
      const unsigned w2_ = *(const GAS unsigned*)(kv + ((size_t)head * SK + (size_t)(t0 + st_) * 16) * DH); asm volatile("" :: "v"(w2_));
  }
  if (v == 1) {
    lds_wait(LV + L_FLAG_E, 1u);
    tag = LV[L_E] + 1u;
    const gu64* qp = QG + head * 64 + lane;
    if (!is_qc) __builtin_amdgcn_s_sleep(QDELAY);
    u64 g0 = ldg(qp); __builtin_amdgcn_s_sleep(STAG);
    u64 g1 = ldg(qp); __builtin_amdgcn_s_sleep(STAG);
    u64 g2 = ldg(qp), g;
    for (unsigned n = 0;; ++n) {
      if (all_ok(ok_pair(g0, tag)) || n >= SPIN_G) { g = g0; break; }
      g0 = ldg(qp);
      if (all_ok(ok_pair(g1, tag))) { g = g1; break; }
      g1 = ldg(qp);
      if (all_ok(ok_pair(g2, tag))) { g = g2; break; }
      g2 = ldg(qp);
    }
    L[L_Q + lane] = (unsigned)(g >> 32);
    WAIT_LGKM();
    if (lane == 0) LV[L_FLAG_Q] = 1u;
    STAMP(7);
  }
  lds_wait(LV + L_FLAG_E, 1u);
  tag = LV[L_E] + 1u;

  const int g4 = lane >> 4, tq = (lane & 15) >> 2, tp = lane & 3;
  v4u wf[5][8];
  unsigned mbA[2], mbB[2];
  if (is_ffn) {
#define FPACE 63
    v4u raw[5][8];
    {
      const size_t ro = (size_t)(160 * v + r8) * F + 64 * head + 8 * c8;
#pragma unroll
      for (int kb = 0; kb < 5; ++kb) {
        if (FPACE < 63) asm volatile("s_waitcnt vmcnt(%0)" :: "n"(FPACE) : "memory");
#pragma unroll
        for (int i = 0; i < 4; ++i) { raw[kb][i] = ldnt(p.wg + ro + (size_t)(32 * kb + 8 * i) * F); raw[kb][4 + i] = ldnt(p.wu + ro + (size_t)(32 * kb + 8 * i) * F); }
        __builtin_amdgcn_sched_barrier(0);
      }
    }
    __builtin_amdgcn_sched_barrier(0);
    LAS unsigned char* stg_w = LB + B_STG + v * 4096 + r8 * 128 + c8 * 16;
    const LAS unsigned char* stg_r = LB + B_STG + v * 4096 + (8 * g4 + tq) * 128 + tp * 8;
#pragma unroll
    for (int kb = 0; kb < 5; ++kb)
#pragma unroll
      for (int m = 0; m < 2; ++m) {
#pragma unroll
        for (int i = 0; i < 4; ++i) *(LAS v4u*)(stg_w + i * 1024) = raw[kb][4 * m + i];
        asm volatile("" ::: "memory");
#pragma unroll
        for (int cg = 0; cg < 4; ++cg) wf[kb][4 * m + cg] = tr_frag(stg_r + cg * 32, 512);
        asm volatile("s_waitcnt lgkmcnt(0)" ::: "memory");
      }
    if (v == 0) STAMP(21);
    if (v == 1) STAMP(18);
    lds_wait(LV + L_CNT_OV, 4u);
    if (v == 0) STAMP(22);
#pragma unroll
    for (int i = 0; i < 20; ++i) {
      const int idx = 64 * i + lane, row = idx / 20, ch = idx - 20 * row;
      GLDS(p.wd + (size_t)(64 * head + row) * D + 160 * v + 8 * ch, LB + B_WD + v * 20480 + 1024 * i, 2);
    }
    {
      const gu64* m0 = MG + 80 * v + lane; const gu64* m1 = MG + 80 * v + 64 + (lane & 15);
      for (unsigned n = 0; n < SPIN_G; ++n) {
        const u64 s0 = ldg(m0 + 960), c0 = ldg(m0 + 1280), s1 = ldg(m1 + 960), c1 = ldg(m1 + 1280);
        const unsigned sp0 = (unsigned)(s0 >> 32), cp0 = (unsigned)(c0 >> 32), sp1 = (unsigned)(s1 >> 32), cp1 = (unsigned)(c1 >> 32);
        mbA[0] = (sp0 & 0xffffu) | (f2bf(1.f + bflo(cp0)) << 16); mbB[0] = (sp0 >> 16) | (f2bf(1.f + bfhi(cp0)) << 16);
        mbA[1] = (sp1 & 0xffffu) | (f2bf(1.f + bflo(cp1)) << 16); mbB[1] = (sp1 >> 16) | (f2bf(1.f + bfhi(cp1)) << 16);
        if (all_ok(ok_pair(s0, tag) & ok_pair(c0, tag) & ok_pair(s1, tag) & ok_pair(c1, tag))) break;
        __builtin_amdgcn_s_sleep(8);
      }
    }
  }
  float own_x = 0.f, own_ga = 0.f, own_gb = 0.f;
  if (is_own) {
    const int col = 64 * own + 16 * v + c;
    const gu64* m = MG + (lane < 32 ? 640 : 1600) + (col >> 1);
    own_x = bf2f(((const GAS bf16_t*)p.x)[col]);
    unsigned g = 0;
    for (unsigned n = SPIN_G; n != 0u; --n) { const u64 a = ldg(m); g = (unsigned)(a >> 32); if (none_bad(bad_pair(a, tag))) break; __builtin_amdgcn_s_sleep(8); }
    const float gv = (c & 1) ? bfhi(g) : bflo(g);
    const float ga_h = lane < 32 ? gv : 0.f, gb_h = lane < 32 ? 0.f : gv;
    own_ga = swap32_sum(ga_h, ga_h); own_gb = swap32_sum(gb_h, gb_h);
  }
  lds_wait(LV + L_FLAG_L, 1u);
#pragma unroll
  for (int i = 0; i < 5; ++i) {
    const int idx = 256 * i + t, row = idx / 10, ch = idx - 10 * row;
    GLDS(p.wo + (size_t)(head * DH + row) * D + 80 * sp + 8 * ch, LB + B_WO + (256 * i + 64 * v) * 16, 0);
  }
  asm volatile("s_waitcnt vmcnt(0)" ::: "memory");
  if (lane == 0) __hip_atomic_fetch_add((LAS unsigned*)(L + L_CNT_W), 1u, __ATOMIC_RELAXED, __HIP_MEMORY_SCOPE_WORKGROUP);
  lds_wait(LV + L_CNT_W, 4u);
  v4u wof[4], wof4[4];
  {
    const LAS unsigned char* src = LB + B_WO + (8 * g4 + tq) * 160 + tp * 8;
#pragma unroll
    for (int kb = 0; kb < 4; ++kb) { wof[kb] = tr_frag(src + kb * 32 * 160 + v * 32, 4 * 160); wof4[kb] = tr_frag(src + kb * 32 * 160 + 4 * 32, 4 * 160); }
  }
  if (v == 0) STAMP(8);

  const int hf = lane >> 5;
  const gu64* hop = PG + (size_t)(head * 8 + hf) * PG_STRIDE + 32 * v + (lane & 31);
  const gu64* hmp = PG + (size_t)(head * 8 + (((lane & 15) >> 1) ^ hf)) * PG_STRIDE + 128 + (lane & 1);
  gu64* ysp = YG + (size_t)(5 * sp + (lane < 16 ? v : 4)) * 512 + head * 16 + c;
  LAS bf16_t* odst = (LAS bf16_t*)(L + L_O) + 32 * v + (lane & 31);
  PINP(hop); PINP(hmp); PINP(ysp);
  lds_wait(LV + L_FLAG_S1, 1u);
  if (v == 0) STAMP(9);
  {
    u64 g0, g1, g2, g3, gm;
    for (unsigned n = SPIN_G; n != 0u; --n) {
      g0 = ldg(hop); g1 = ldg(hop + 2 * PG_STRIDE); g2 = ldg(hop + 4 * PG_STRIDE); g3 = ldg(hop + 6 * PG_STRIDE); gm = ldg(hmp);
      if (none_bad((bad_f24(g0, tag) | bad_f24(g1, tag) | bad_f24(g2, tag)) | (bad_f24(g3, tag) | bad_f24(gm, tag)))) break;
      if (PSLEEP) __builtin_amdgcn_s_sleep(PSLEEP);
    }
    const float ml = val_f24(gm);
    const float mm = (lane & 1) ? -INFINITY : ml;
    float M = MAX_DPP(mm, "quad_perm:[2,3,0,1]"); M = MAX_DPP(M, "row_half_mirror"); M = MAX_DPP(M, "row_ror:8"); M = MAX_DPP(M, "quad_perm:[1,0,3,2]");
    const float wgt = __builtin_amdgcn_exp2f((mm - M) * LOG2E);
    const float wn = dpp<XOR1>(wgt);
    const float wl = (lane & 1) ? wn * ml : 0.f;
    float Ls = wl + dpp<XOR1>(wl); Ls += dpp<XOR2>(Ls); Ls += dpp<XOR7>(Ls); Ls += dpp<XOR8>(Ls);
    float acc = dpp<BCAST0>(wgt) * val_f24(g0);
    acc = fmaf(dpp<BCAST0 + 4>(wgt), val_f24(g1), acc); acc = fmaf(dpp<BCAST0 + 8>(wgt), val_f24(g2), acc); acc = fmaf(dpp<BCAST0 + 12>(wgt), val_f24(g3), acc);
    acc = swap32_sum(acc, acc);
    *odst = (bf16_t)f2bf(acc * __builtin_amdgcn_rcpf(Ls));
  }
  BARRIER();
  if (v == 0) STAMP(10);
  {
    v4u A[4];
#pragma unroll
    for (int kb = 0; kb < 4; ++kb) A[kb] = *(const LAS v4u*)(L + L_O + 16 * kb + 4 * g4);
    f32x4 y = {0.f, 0.f, 0.f, 0.f}, y4 = {0.f, 0.f, 0.f, 0.f};
#pragma unroll
    for (int kb = 0; kb < 4; ++kb) y = mfma(A[kb], wof[kb], y);
    if (v == 0) {
#pragma unroll
      for (int kb = 0; kb < 4; ++kb) y4 = mfma(A[kb], wof4[kb], y4);
    }
    if (lane < 16 || (v == 0 && lane < 32)) stg(ysp, gr_f24(tag, lane < 16 ? y[0] : y4[0]));
  }
  if (v == 0) STAMP(11);
  if (is_own) {
    const int og = 4 * own + v;
    const gu64* yp = YG + (size_t)og * 512 + lane;
    const gu64* dp = DG + (size_t)og * 512 + lane;
    const gu64* dp6 = dp + (lane < 48 ? 384 : 0);
    gu64* xp = XG + 16 * og + (c >> 1);
    GAS bf16_t* op = (GAS bf16_t*)p.out + 16 * og + c;
    PINP(yp); PINP(dp); PINP(dp6); PINP(xp); PINP(op);
    u64 g[8];
    for (unsigned n = SPIN_G; n != 0u; --n) {
      unsigned bad = 0;
#pragma unroll
      for (int i = 0; i < 8; ++i) { g[i] = ldg(yp + 64 * i); bad |= bad_f24(g[i], tag); }
      if (none_bad(bad)) break;
      if (PSLEEP) __builtin_amdgcn_s_sleep(PSLEEP);
    }
    if (v == 0) STAMP(30);
    float s = ((val_f24(g[0]) + val_f24(g[1])) + (val_f24(g[2]) + val_f24(g[3]))) + ((val_f24(g[4]) + val_f24(g[5])) + (val_f24(g[6]) + val_f24(g[7])));
    s = xrow_sum(s);
    const float x1 = rbf(own_x + rbf(own_ga * rbf(s)));
    const unsigned xb = f2bf(x1), pair = xb | (dppu<XOR1>(xb) << 16);
    if (lane < 16 && (lane & 1) == 0) stg(xp, gr_pair(tag, pair));
    if (v == 0) STAMP(12);
    for (unsigned n = SPIN_G; n != 0u; --n) {
      unsigned bad = 0;
#pragma unroll
      for (int i = 0; i < 6; ++i) { g[i] = ldg(dp + 64 * i); bad |= bad_f24(g[i], tag); }
      g[6] = ldg(dp6); bad |= bad_f24(g[6], tag);
      if (none_bad(bad)) break;
      if (PSLEEP) __builtin_amdgcn_s_sleep(PSLEEP);
    }
    if (v == 0) STAMP(15);
    s = ((val_f24(g[0]) + val_f24(g[1])) + (val_f24(g[2]) + val_f24(g[3]))) + ((val_f24(g[4]) + val_f24(g[5])) + (lane < 48 ? val_f24(g[6]) : 0.f));
    s = xrow_sum(s);
    if (lane < 16) *op = (bf16_t)f2bf(x1 + rbf(own_gb * rbf(s)));
    if (og == 0 && lane == 0) __hip_atomic_store((gu32*)(p.ws + WS_EPOCH), tag, __ATOMIC_RELAXED, __HIP_MEMORY_SCOPE_AGENT);
    if (v == 0) STAMP(16);
  }
  if (is_ffn) {
    const int ua = 80 * v + lane, ub = 80 * v + 64 + (lane & 15);
    const gu64* xa = XG + (ua >> 3) * 16 + (ua & 7); const gu64* xb = XG + (ub >> 3) * 16 + (ub & 7);
    gu64* dq = DG + (size_t)(10 * v + g4) * 512 + head * 16 + c;
    PINP(xa); PINP(xb); PINP(dq);
    unsigned xo0, xo1;
    for (unsigned n = SPIN_G; n != 0u; --n) {
      const u64 g0 = ldg(xa), g1 = ldg(xb);
      xo0 = (unsigned)(g0 >> 32); xo1 = (unsigned)(g1 >> 32);
      if (none_bad(bad_pair(g0, tag) | bad_pair(g1, tag))) break;
      if (PSLEEP) __builtin_amdgcn_s_sleep(PSLEEP);
    }
    if (v == 0) STAMP(13);
    {
      const unsigned xm = lane < 16 ? xo1 : 0u;
      float s1 = dot2(xm, 0x3f803f80u, dot2(xo0, 0x3f803f80u, 0.f)), s2 = dot2(xm, xm, dot2(xo0, xo0, 0.f));
      s1 += dpp<XOR1>(s1); s2 += dpp<XOR1>(s2); s1 += dpp<XOR2>(s1); s2 += dpp<XOR2>(s2);
      s1 += dpp<XOR7>(s1); s2 += dpp<XOR7>(s2); s1 += dpp<XOR8>(s1); s2 += dpp<XOR8>(s2);
      s1 = xrow_sum(s1); s2 = xrow_sum(s2);
      if (lane == 0) *(LAS f32x2*)(LF + L_ST + 2 * v) = f32x2{s1, s2};
    }
    if (v == 0) STAMP(25);
    BARRIER();
    if (v == 0) STAMP(26);
    {
      const f32x4 sa = *(const LAS f32x4*)(LF + L_ST), sb = *(const LAS f32x4*)(LF + L_ST + 4);
      const float mean_f = ((sa[0] + sa[2]) + (sb[0] + sb[2])) * (1.f / D);
      const float ex2 = ((sa[1] + sa[3]) + (sb[1] + sb[3])) * (1.f / D);
      float var_f = fmaf(-mean_f, mean_f, ex2);
      if (__builtin_amdgcn_ballot_w64(var_f * 256.f < ex2) != 0ull) {
        float vs = 0.f;
#pragma unroll
        for (int i = 0; i < 5; ++i) { const int u_ = lane + 64 * i; const unsigned xp = (unsigned)(ldg(XG + (u_ >> 3) * 16 + (u_ & 7)) >> 32); const float d0 = bflo(xp) - mean_f, d1 = bfhi(xp) - mean_f; vs = fmaf(d0, d0, vs); vs = fmaf(d1, d1, vs); }
        var_f = wave_sum(vs) * (1.f / D);
      }
      const float var = rbf(var_f), mean = rbf(mean_f);
      const float rs = __builtin_amdgcn_rcpf(rbf(__builtin_amdgcn_sqrtf(rbf(var + 1e-5f))));
#define LNPAIR(xp, ma, mb_) ({ const unsigned d_ = pk2(bflo(xp) - mean, bfhi(xp) - mean); const unsigned n_ = pk2(bflo(d_) * rs, bfhi(d_) * rs); \
        const unsigned m_ = pk2(bflo(n_) * bfhi(ma), bfhi(n_) * bfhi(mb_)); pk2(bflo(m_) + bflo(ma), bfhi(m_) + bflo(mb_)); })
      L[L_H2 + 80 * v + lane] = LNPAIR(xo0, mbA[0], mbB[0]);
      const unsigned h1 = LNPAIR(xo1, mbA[1], mbB[1]);
      if (lane < 16) L[L_H2 + 80 * v + 64 + lane] = h1;
#undef LNPAIR
    }
    if (v == 0) STAMP(27);
    v4u wd[2][10];
    {
      v4u A[5];
#pragma unroll
      for (int kb = 0; kb < 5; ++kb) A[kb] = *(const LAS v4u*)(L + L_H2 + 80 * v + 16 * kb + 4 * g4);
      f32x4 acc[8];
#pragma unroll
      for (int cg = 0; cg < 8; ++cg) acc[cg] = f32x4{0.f, 0.f, 0.f, 0.f};
      const LAS unsigned char* src = LB + B_WD + v * 20480 + (8 * g4 + tq) * 320 + tp * 8;
#pragma unroll
      for (int kb = 0; kb < 5; ++kb) {
#pragma unroll
        for (int cg = 0; cg < 8; ++cg) acc[cg] = mfma(A[kb], wf[kb][cg], acc[cg]);
        __builtin_amdgcn_sched_barrier(0);
        if (kb < 4) {
#pragma unroll
          for (int j = 0; j < 5; ++j) { const int f = 5 * kb + j; wd[f / 10][f % 10] = tr_frag(src + (f / 10) * 32 * 320 + (f % 10) * 32, 4 * 320); }
        }
        __builtin_amdgcn_sched_barrier(0);
      }
      LF[L_RGU + v * 128 + lane] = sel4(g4, acc[0][0], acc[1][0], acc[2][0], acc[3][0]);
      LF[L_RGU + v * 128 + 64 + lane] = sel4(g4, acc[4][0], acc[5][0], acc[6][0], acc[7][0]);
    }
    if (v == 0) STAMP(28);
    BARRIER();
    if (v == 0) STAMP(29);
    {
      const float G = rbf((LF[L_RGU + lane] + LF[L_RGU + 128 + lane]) + (LF[L_RGU + 256 + lane] + LF[L_RGU + 384 + lane]));
      const float U = rbf((LF[L_RGU + 64 + lane] + LF[L_RGU + 192 + lane]) + (LF[L_RGU + 320 + lane] + LF[L_RGU + 448 + lane]));
      ((LAS bf16_t*)(L + L_ACT + 32 * v))[lane] = (bf16_t)f2bf(rbf(silu(G)) * U);
      if (v == 0) STAMP(14);
      const v4u A0 = *(const LAS v4u*)(L + L_ACT + 32 * v + 4 * g4), A1 = *(const LAS v4u*)(L + L_ACT + 32 * v + 16 + 4 * g4);
      f32x4 acc[10];
#pragma unroll
      for (int cg = 0; cg < 10; ++cg) { acc[cg] = mfma(A0, wd[0][cg], f32x4{0.f, 0.f, 0.f, 0.f}); acc[cg] = mfma(A1, wd[1][cg], acc[cg]); }
      stg(dq, gr_f24(tag, sel4(g4, acc[0][0], acc[1][0], acc[2][0], acc[3][0])));
      stg(dq + 4 * 512, gr_f24(tag, sel4(g4, acc[4][0], acc[5][0], acc[6][0], acc[7][0])));
      if (lane < 32) stg(dq + 8 * 512, gr_f24(tag, g4 == 0 ? acc[8][0] : acc[9][0]));
    }
    if (v == 0) STAMP(17);
  }
  if (v == 0) STAMP_DUMP();
}

extern "C" void kernel_launch(void* const* d_in, const int* in_sizes, int n_in, void* d_out, int out_size, void* d_ws, size_t ws_size, hipStream_t stream) {
  Params p;
  p.x = (const bf16_t*)d_in[0]; p.kc = (const bf16_t*)d_in[1]; p.vc = (const bf16_t*)d_in[2]; p.wq = (const bf16_t*)d_in[3]; p.wo = (const bf16_t*)d_in[4];
  p.wg = (const bf16_t*)d_in[6]; p.wu = (const bf16_t*)d_in[7]; p.wd = (const bf16_t*)d_in[8]; p.c = (const bf16_t*)d_in[9]; p.wmod = (const bf16_t*)d_in[10];
  p.out = (bf16_t*)d_out; p.ws = (unsigned char*)d_ws;
  (void)hipFuncSetAttribute((const void*)adaln_block_kernel, hipFuncAttributeMaxDynamicSharedMemorySize, LDS_BYTES);
  adaln_block_kernel<<<NCU, NTHR, LDS_BYTES, stream>>>(p);
}
```

```cpp
#include <hip/hip_runtime.h>
#include <hip/hip_bf16.h>
#include <stdint.h>

#define TD 6
#define PACE 0
#define PACE0 0
#define KVDELAY 0
#define PSLEEP 0
#define TLBWARM 2
#define TLBSTEP 4
#define WOLEAD 1
#define GATE 1
#define STAG 12
#define QDELAY 64
#define STAMPS 0
#define NT_Q 54
#define NT_M 64
#define NT_F 42
#define NT_P ((512 - 2 * NT_Q - 2 * NT_M - NT_F) / 3)
static_assert(2 * NT_Q + 2 * NT_M + NT_F + 3 * NT_P == 512, "tiles of a head");
#define NT_G0 54
#define NT_G1 60
#define NT_G2 ((512 - 2 * NT_G0 - 2 * NT_G1) / 4)
static_assert(2 * NT_G0 + 2 * NT_G1 + 4 * NT_G2 == 512, "tiles of a head");
#define NT_H0 52
#define NT_H1 ((512 - 2 * NT_H0) / 6)
static_assert(2 * NT_H0 + 6 * NT_H1 == 512, "tiles of a head");
static_assert(NT_F >= 40, "every stream wave takes at least 10 tiles (the catch-up schedule)");

typedef unsigned short bf16_t;
typedef unsigned long long u64;
typedef unsigned v4u __attribute__((ext_vector_type(4)));
typedef float f32x4 __attribute__((ext_vector_type(4)));
typedef float f32x2 __attribute__((ext_vector_type(2)));
typedef unsigned v2u __attribute__((ext_vector_type(2)));
typedef __bf16 bf16x2 __attribute__((ext_vector_type(2)));
typedef __bf16 bf16x8 __attribute__((ext_vector_type(8)));
typedef short v4s __attribute__((ext_vector_type(4)));
#define GAS __attribute__((address_space(1)))
#define LAS __attribute__((address_space(3)))
typedef GAS u64 gu64;
typedef GAS unsigned gu32;

constexpr int D = 640, HQ = 32, SK = 8192, DH = 128, F = 1728, NMOD = 6 * D;
constexpr int NCU = 256, NTHR = 512, NFFN = 27;
constexpr float LOG2E = 1.4426950408889634f;

constexpr size_t WS_EPOCH = 0;
constexpr size_t WS_XG = 393216 + 32768;
constexpr size_t WS_MG = 8192;
constexpr size_t WS_QG = 32768;
constexpr size_t WS_YG = 65536;
constexpr size_t WS_DG = 262144;
constexpr size_t WS_PG = 524288;
constexpr int PG_STRIDE = 160;
constexpr size_t WS_STAMP = 64u << 20;

constexpr int L_FLAG_Q = 0, L_CNT_S1 = 1, L_FLAG_S1 = 2, L_FLAG_E = 3, L_E = 4, L_CNT_M = 5, L_FLAG_H = 6, L_CNT_G = 7, L_CNT_W = 8, L_FLAG_L = 9, L_CNT_OV = 10, L_CNT_Q = 11;
constexpr int L_Q = 32;
constexpr int L_REC = 96;
constexpr int L_O = 624;
constexpr int L_MR = 704;
constexpr int L_SC = 960;
constexpr int L_H2 = 1600;
constexpr int L_ST = 1920;
constexpr int L_RGU = 1936;
constexpr int L_ACT = 2448;
constexpr int L_STAMP = 2816;
constexpr int B_STG = 12288;
constexpr int B_WO = 28672;
constexpr int B_WD = 49152;
constexpr int B_OV = 28672;
constexpr int LDS_BYTES = 163840;
static_assert(B_OV + 4 * 32768 <= LDS_BYTES && B_WD + 81920 <= LDS_BYTES, "lds");

constexpr int CODE_BYTES = 32 * 1024;
constexpr unsigned SPIN_G = 1u << 15;
constexpr unsigned SPIN_L = 1u << 20;

struct Params {
  const bf16_t* x; const bf16_t* kc; const bf16_t* vc; const bf16_t* wq; const bf16_t* wo;
  const bf16_t* wg; const bf16_t* wu; const bf16_t* wd; const bf16_t* c; const bf16_t* wmod;
  bf16_t* out; unsigned char* ws;
};

__device__ __forceinline__ float bf2f(unsigned b) { return __uint_as_float(b << 16); }
__device__ __forceinline__ float bfhi(unsigned b) { return __uint_as_float(b & 0xffff0000u); }
__device__ __forceinline__ float bflo(unsigned b) { return __uint_as_float(b << 16); }
__device__ __forceinline__ unsigned f2bf(float f) { __bf16 b = (__bf16)f; return (unsigned)__builtin_bit_cast(unsigned short, b); }
__device__ __forceinline__ float rbf(float f) { return bf2f(f2bf(f)); }
__device__ __forceinline__ unsigned pk2(float lo, float hi) { return __builtin_bit_cast(unsigned, __builtin_convertvector(f32x2{lo, hi}, bf16x2)); }
__device__ __forceinline__ float silu(float x) { return x * __builtin_amdgcn_rcpf(1.f + __builtin_amdgcn_exp2f(-x * LOG2E)); }
__device__ __forceinline__ float dot2(unsigned a, unsigned b, float acc) {
  return __builtin_amdgcn_fdot2_f32_bf16(__builtin_bit_cast(bf16x2, a), __builtin_bit_cast(bf16x2, b), acc, false);
}
template <int CTRL> __device__ __forceinline__ float dpp(float x) {
  return __builtin_bit_cast(float, __builtin_amdgcn_mov_dpp(__builtin_bit_cast(int, x), CTRL, 0xf, 0xf, true));
}
template <int CTRL> __device__ __forceinline__ unsigned dppu(unsigned x) { return (unsigned)__builtin_amdgcn_mov_dpp((int)x, CTRL, 0xf, 0xf, true); }
#define MAX_DPP(x, ctrl) ({ float r_; asm("s_nop 1\n\tv_max_f32_dpp %0, %1, %1 " ctrl " row_mask:0xf bank_mask:0xf bound_ctrl:1" : "=v"(r_) : "v"(x)); r_; })
constexpr int XOR1 = 0xB1, XOR2 = 0x4E, XOR7 = 0x141, XOR8 = 0x128, ROR4 = 0x124, BCAST0 = 0x150;
__device__ __forceinline__ float swap32_sum(float a, float b) {
  auto t = __builtin_amdgcn_permlane32_swap(__float_as_uint(a), __float_as_uint(b), false, false);
  return __uint_as_float(t[0]) + __uint_as_float(t[1]);
}
__device__ __forceinline__ float swap16_sum(float a, float b) {
  auto t = __builtin_amdgcn_permlane16_swap(__float_as_uint(a), __float_as_uint(b), false, false);
  return __uint_as_float(t[0]) + __uint_as_float(t[1]);
}
__device__ __forceinline__ float xrow_max(float x) {
  auto s = __builtin_amdgcn_permlane16_swap(__float_as_uint(x), __float_as_uint(x), false, false);
  x = fmaxf(__uint_as_float(s[0]), __uint_as_float(s[1]));
  auto t = __builtin_amdgcn_permlane32_swap(__float_as_uint(x), __float_as_uint(x), false, false);
  return fmaxf(__uint_as_float(t[0]), __uint_as_float(t[1]));
}
__device__ __forceinline__ float xrow_sum(float x) { x = swap16_sum(x, x); return swap32_sum(x, x); }
__device__ __forceinline__ float wave_sum(float x) {
  x += dpp<XOR1>(x); x += dpp<XOR2>(x); x += dpp<XOR7>(x); x += dpp<XOR8>(x);
  return xrow_sum(x);
}
__device__ __forceinline__ void reduce_rows(const float* x, float& e0, float& e1) {
  const float y0 = swap32_sum(x[0], x[4]), y1 = swap32_sum(x[1], x[5]), y2 = swap32_sum(x[2], x[6]), y3 = swap32_sum(x[3], x[7]);
  e0 = swap16_sum(y0, y2); e1 = swap16_sum(y1, y3);
}
__device__ __forceinline__ float reduce_oct(const float* x, int lane) {
  float e0, e1; reduce_rows(x, e0, e1);
  const bool b3 = (lane & 8) != 0;
  return (b3 ? e1 : e0) + dpp<XOR8>(b3 ? e0 : e1);
}

__device__ __forceinline__ u64 ldg(const gu64* p) { return __hip_atomic_load(p, __ATOMIC_RELAXED, __HIP_MEMORY_SCOPE_AGENT); }
__device__ __forceinline__ void stg(gu64* p, u64 v) { __hip_atomic_store(p, v, __ATOMIC_RELAXED, __HIP_MEMORY_SCOPE_AGENT); }
__device__ __forceinline__ u64 gr_pair(unsigned tag, unsigned pay) { return ((u64)pay << 32) | tag; }
__device__ __forceinline__ u64 gr_f24(unsigned tag, float v) { return ((u64)((__float_as_uint(v) + 0x80u) & 0xffffff00u) << 32) | tag; }
__device__ __forceinline__ unsigned bad_pair(u64 g, unsigned tag) { return (unsigned)g ^ tag; }
__device__ __forceinline__ unsigned bad_f24(u64 g, unsigned tag) { return ((unsigned)g ^ tag) | ((unsigned)(g >> 32) & 0xffu); }
__device__ __forceinline__ float val_f24(u64 g) { return __uint_as_float((unsigned)(g >> 32)); }
__device__ __forceinline__ bool none_bad(unsigned bad) { return __builtin_amdgcn_ballot_w64(bad != 0u) == 0ull; }
__device__ __forceinline__ bool ok_pair(u64 g, unsigned tag) { return (unsigned)g == tag; }
__device__ __forceinline__ bool all_ok(bool ok) { return __builtin_amdgcn_ballot_w64(!ok) == 0ull; }
#define PINP(ptr) asm volatile("" : "+v"(ptr))

__device__ __forceinline__ void lds_wait(volatile LAS unsigned* w, unsigned want) {
#define LSLEEP 1
  for (unsigned n = 0; *w != want && n < SPIN_L; ++n) __builtin_amdgcn_s_sleep(LSLEEP);
  asm volatile("" ::: "memory");
}
#define WAIT_LGKM() asm volatile("s_waitcnt lgkmcnt(0)" ::: "memory")
#define BARRIER() do { asm volatile("s_waitcnt lgkmcnt(0)" ::: "memory"); __builtin_amdgcn_s_barrier(); asm volatile("" ::: "memory"); } while (0)

#define STAMP(i) do { } while (0)
#define STAMP_DUMP() do { } while (0)

struct AttnState { float m, l; float acc[8]; };

__device__ __forceinline__ void attn_tile(const v4u (&K)[4], const v4u (&V)[4], const v4u& q, AttnState& st, int c) {
  float x[4];
#pragma unroll
  for (int i = 0; i < 4; ++i) {
    float d = dot2(K[i].x, q.x, 0.f);
    d = dot2(K[i].y, q.y, d); d = dot2(K[i].z, q.z, d); d = dot2(K[i].w, q.w, d);
    x[i] = d;
  }
  const bool b3 = (c & 8) != 0, b2 = (c & 4) != 0;
  const float k0 = b3 ? x[2] : x[0], s0 = b3 ? x[0] : x[2];
  const float k1 = b3 ? x[3] : x[1], s1 = b3 ? x[1] : x[3];
  const float y0 = k0 + dpp<XOR8>(s0), y1 = k1 + dpp<XOR8>(s1);
  const float k2 = b2 ? y1 : y0, s2 = b2 ? y0 : y1;
  float z = k2 + dpp<XOR7>(s2);
  z += dpp<XOR2>(z); z += dpp<XOR1>(z);
  const float s = rbf(rbf(z) * 0.08838834764831845f);
  float pm = MAX_DPP(s, "row_ror:4"); pm = MAX_DPP(pm, "row_ror:8");
  const float mn = fmaxf(st.m, pm);
  if (__builtin_amdgcn_ballot_w64(mn > st.m) != 0ull) {
    const float al = __builtin_amdgcn_exp2f((st.m - mn) * LOG2E);
    st.l *= al;
#pragma unroll
    for (int j = 0; j < 8; ++j) st.acc[j] *= al;
    st.m = mn;
  }
  const float pr = __builtin_amdgcn_exp2f((s - st.m) * LOG2E);
  st.l += pr;
  const unsigned plo = f2bf(pr), phi = plo << 16;
#define PV(i) { const unsigned a = dppu<BCAST0 + 4 * i>(plo), b = dppu<BCAST0 + 4 * i>(phi); \
    st.acc[0] = dot2(V[i].x, a, st.acc[0]); st.acc[1] = dot2(V[i].x, b, st.acc[1]); st.acc[2] = dot2(V[i].y, a, st.acc[2]); st.acc[3] = dot2(V[i].y, b, st.acc[3]); \
    st.acc[4] = dot2(V[i].z, a, st.acc[4]); st.acc[5] = dot2(V[i].z, b, st.acc[5]); st.acc[6] = dot2(V[i].w, a, st.acc[6]); st.acc[7] = dot2(V[i].w, b, st.acc[7]); }
  PV(0) PV(1) PV(2) PV(3)
#undef PV
}

__device__ __forceinline__ void interleave(const v4u& r0, const v4u& r1, unsigned* o) {
#pragma unroll
  for (int j = 0; j < 4; ++j) { o[2 * j] = __builtin_amdgcn_perm(r1[j], r0[j], 0x05040100u); o[2 * j + 1] = __builtin_amdgcn_perm(r1[j], r0[j], 0x07060302u); }
}
__device__ __forceinline__ f32x4 mfma(const v4u& a, const v4u& b, const f32x4& c) {
  return __builtin_amdgcn_mfma_f32_16x16x32_bf16(__builtin_bit_cast(bf16x8, a), __builtin_bit_cast(bf16x8, b), c, 0, 0, 0);
}
__device__ __forceinline__ v4u tr_frag(const LAS unsigned char* p, int stride4) {
  const v2u lo = __builtin_bit_cast(v2u, __builtin_amdgcn_ds_read_tr16_b64_v4i16((LAS v4s*)p));
  const v2u hi = __builtin_bit_cast(v2u, __builtin_amdgcn_ds_read_tr16_b64_v4i16((LAS v4s*)(p + stride4)));
  return v4u{lo.x, lo.y, hi.x, hi.y};
}
#define GLDS(gsrc, ldst, aux) __builtin_amdgcn_global_load_lds((const GAS void*)(gsrc), (LAS void*)(ldst), 16, 0, aux)
__device__ __forceinline__ float sel4(int q4, float a, float b, float c, float d) { return q4 == 0 ? a : q4 == 1 ? b : q4 == 2 ? c : d; }
__device__ __forceinline__ v4u ldnt(const bf16_t* p) { return __builtin_nontemporal_load((const GAS v4u*)p); }
#define PIN8(a) asm volatile("" : "+v"((a)[0]), "+v"((a)[1]), "+v"((a)[2]), "+v"((a)[3]), "+v"((a)[4]), "+v"((a)[5]), "+v"((a)[6]), "+v"((a)[7]))

__global__ void __launch_bounds__(NTHR, 2) adaln_block_kernel(Params p) {
  extern __shared__ __attribute__((aligned(16))) unsigned lds_raw[];
  LAS unsigned* L = (LAS unsigned*)lds_raw;
  volatile LAS unsigned* LV = (volatile LAS unsigned*)lds_raw;
  LAS float* LF = (LAS float*)lds_raw;
  LAS unsigned char* LB = (LAS unsigned char*)lds_raw;
  u64 code_base;
  asm volatile("s_getpc_b64 %0" : "=s"(code_base));
  code_base &= ~255ull;
  const int tid = threadIdx.x, lane = tid & 63, wave = __builtin_amdgcn_readfirstlane(tid >> 6);
  const int r = lane >> 4, c = lane & 15;
  const int b = blockIdx.x, X = b & 7, J = b >> 3, sp = J & 7;
  const int head = 4 * X + (J >> 3);
  const bool fh = head < NFFN;
  const bool is_qc = sp < 2, is_ffn = fh && sp == 5;
  const int mu = (sp == 2 || sp == 3) ? 2 * head + sp - 2 : 999;
  const int own = head;
  const bool is_mod = mu < 60, is_own = sp == 4 && head < 10;
  const int cf = fh, cg = !fh && head < 30, ch_ = head >= 30;
  const int nq_ = cf * NT_Q + cg * NT_G0 + ch_ * NT_H0, nm_ = cf * NT_M + cg * NT_G1 + ch_ * NT_H1;
  const int np_ = cf * NT_P + cg * NT_G2 + ch_ * NT_H1, nf_ = cf * NT_F + cg * NT_G2 + ch_ * NT_H1;
  const int t0 = min(sp, 2) * nq_ + min(max(sp - 2, 0), 2) * nm_ + (int)(sp > 4) * np_ + (int)(sp > 5) * nf_ + (int)(sp > 6) * np_;
  const int nt = (int)(sp < 2) * nq_ + (int)(sp == 2 || sp == 3) * nm_ + (int)(sp == 5) * nf_ + (int)(sp == 4 || sp > 5) * np_;
  if (tid < 16) L[tid] = 0u;
  asm volatile("" :: "s"(p.kc), "s"(p.vc), "s"(p.ws), "s"(p.wq), "s"(p.wmod), "s"(p.c), "s"(p.x), "s"(t0), "s"(nt));
  BARRIER();
  gu64* const XG = (gu64*)(p.ws + WS_XG); gu64* const MG = (gu64*)(p.ws + WS_MG); gu64* const YG = (gu64*)(p.ws + WS_YG);
  gu64* const DG = (gu64*)(p.ws + WS_DG); gu64* const QG = (gu64*)(p.ws + WS_QG); gu64* const PG = (gu64*)(p.ws + WS_PG);

  if (__builtin_expect(wave < 4, 1)) {
    const size_t base = ((size_t)head * SK + (size_t)t0 * 16) * DH;
    const __amdgpu_buffer_rsrc_t rk = __builtin_amdgcn_make_buffer_rsrc((void*)(p.kc + base), 0, nt * 4096, 0x00020000);
    const __amdgpu_buffer_rsrc_t rv = __builtin_amdgcn_make_buffer_rsrc((void*)(p.vc + base), 0, nt * 4096, 0x00020000);
    const int voff = lane * 16;
    const int nw = (nt - wave + 3) >> 2;
    v4u K[TD][4], V[TD][4];
#define ISSUE(d, t) do { const int so_ = (wave + 4 * (t)) * 4096; \
    _Pragma("unroll") for (int i_ = 0; i_ < 4; ++i_) K[d][i_] = __builtin_amdgcn_raw_buffer_load_b128(rk, voff + 1024 * i_, so_, 2); \
    _Pragma("unroll") for (int i_ = 0; i_ < 4; ++i_) V[d][i_] = __builtin_amdgcn_raw_buffer_load_b128(rv, voff + 1024 * i_, so_, 2); \
    __builtin_amdgcn_sched_barrier(0); } while (0)
#define PACE_WAIT(n) do { asm volatile("s_waitcnt vmcnt(%0)" :: "n"(n) : "memory"); __builtin_amdgcn_sched_barrier(0); } while (0)
#define QGATE 1
    if (QGATE == 1 && is_qc) lds_wait(LV + L_FLAG_H, 1u);
    if (QGATE == 2 && is_qc) lds_wait(LV + L_CNT_Q, 4u);
    if (is_mod) lds_wait(LV + L_CNT_G, 1u);
    if (KVDELAY) __builtin_amdgcn_s_sleep(KVDELAY);
    if (wave == 0) STAMP(24);
    ISSUE(0, 0);
#pragma unroll
    for (int d = 1; d < TD; ++d) { PACE_WAIT(PACE0); ISSUE(d, d); }
    const unsigned ov = (unsigned)(size_t)(LB + B_OV + wave * 32768);
#define DMA4(m0v, rsrc, so) do { unsigned keep_; asm volatile("s_mov_b32 %0, m0\n\ts_mov_b32 m0, %1\n\ts_nop 0\n\t" \
      "buffer_load_dwordx4 %2, %3, %4 offen nt lds\n\tbuffer_load_dwordx4 %2, %3, %4 offen offset:1024 nt lds\n\t" \
      "buffer_load_dwordx4 %2, %3, %4 offen offset:2048 nt lds\n\tbuffer_load_dwordx4 %2, %3, %4 offen offset:3072 nt lds\n\t" \
      "s_mov_b32 m0, %0" : "=&s"(keep_) : "s"(m0v), "v"(voff), "s"(rsrc), "s"(so) : "memory"); } while (0)
#define DMA_TILE(e, t) do { const int so_ = (wave + 4 * (t)) * 4096; DMA4(ov + (e) * 8192, rk, so_); DMA4(ov + (e) * 8192 + 4096, rv, so_); __builtin_amdgcn_sched_barrier(0); } while (0)
#define LD_OV(d, e) do { const LAS v4u* src_ = (const LAS v4u*)(LB + B_OV + wave * 32768 + (e) * 8192) + lane; \
      _Pragma("unroll") for (int i_ = 0; i_ < 4; ++i_) { K[d][i_] = src_[64 * i_]; V[d][i_] = src_[256 + 64 * i_]; } __builtin_amdgcn_sched_barrier(0); } while (0)
#pragma unroll
    for (int e = 0; e < 4; ++e) { PACE_WAIT(PACE0); DMA_TILE(e, 6 + e); }
    lds_wait(LV + L_FLAG_Q, 1u);
    if (wave == 0) STAMP(1);
    const v4u q = *(const LAS v4u*)(L + L_Q + 4 * c);
    const unsigned tag = L[L_E] + 1u;
    AttnState st; st.m = -INFINITY; st.l = 0.f;
#pragma unroll
    for (int j = 0; j < 8; ++j) st.acc[j] = 0.f;
#define TILE(d) do { attn_tile(K[d], V[d], q, st, c); __builtin_amdgcn_sched_barrier(0); } while (0)
    static_assert(TD == 6, "the catch-up schedule is written for six slots");
    TILE(0); PACE_WAIT(0); ISSUE(0, 10);
    TILE(1); TILE(2); TILE(3); TILE(4);
    LD_OV(4, 0);
    PACE_WAIT(PACE); ISSUE(1, 11);
    TILE(5);
    LD_OV(5, 1);
    TILE(4);
    LD_OV(4, 2);
    TILE(5);
    LD_OV(5, 3);
    WAIT_LGKM();
    if (lane == 0) __hip_atomic_fetch_add((LAS unsigned*)(L + L_CNT_OV), 1u, __ATOMIC_RELAXED, __HIP_MEMORY_SCOPE_WORKGROUP);
    PACE_WAIT(PACE); ISSUE(2, 12);
    TILE(4); TILE(5);
    if (nw > 10) TILE(0);
    if (nw > 11) TILE(1);
#pragma nounroll
    for (int u = 12; u < nw; u += TD) {
#pragma unroll
      for (int k = 0; k < TD; ++k) {
        if (u + k < nw) {
          PACE_WAIT(PACE);
          ISSUE((k + 3) % TD, u + k + 1);
          if (u + k + 1 + WOLEAD == nw && wave == 0 && lane == 0) LV[L_FLAG_L] = 1u;
          TILE((k + 2) % TD);
        }
      }
    }
#undef TILE
    if (wave == 0 && lane == 0) LV[L_FLAG_L] = 1u;
#undef ISSUE
    if (wave == 0) STAMP(2);
    const float M = xrow_max(st.m);
    const float sc = __builtin_amdgcn_exp2f((st.m - M) * LOG2E);
    float l = ((c & 3) == 0) ? st.l * sc : 0.f;
    l += dpp<ROR4>(l); l += dpp<XOR8>(l); l = xrow_sum(l);
#pragma unroll
    for (int j = 0; j < 8; ++j) st.acc[j] *= sc;
    float e0, e1; reduce_rows(st.acc, e0, e1);
    LAS float* rec = LF + L_REC + wave * 132;
    *(LAS f32x2*)(rec + 4 + 8 * c + 2 * r) = f32x2{e0, e1};
    if (lane == 0) { rec[0] = M; rec[1] = l; }
    WAIT_LGKM();
    unsigned old = 0;
    if (lane == 0) old = __hip_atomic_fetch_add((LAS unsigned*)(L + L_CNT_S1), 1u, __ATOMIC_RELAXED, __HIP_MEMORY_SCOPE_WORKGROUP);
    old = __builtin_amdgcn_readfirstlane(old);
    if (old == 3u) {
      asm volatile("" ::: "memory");
      const LAS float* R = LF + L_REC;
      const float m0 = R[0], m1 = R[132], m2 = R[264], m3 = R[396];
      const float MM = fmaxf(fmaxf(m0, m1), fmaxf(m2, m3));
      const float w0 = __builtin_amdgcn_exp2f((m0 - MM) * LOG2E), w1 = __builtin_amdgcn_exp2f((m1 - MM) * LOG2E);
      const float w2 = __builtin_amdgcn_exp2f((m2 - MM) * LOG2E), w3 = __builtin_amdgcn_exp2f((m3 - MM) * LOG2E);
      const float LL = (w0 * R[1] + w1 * R[133]) + (w2 * R[265] + w3 * R[397]);
      const float oa = (w0 * R[4 + lane] + w1 * R[136 + lane]) + (w2 * R[268 + lane] + w3 * R[400 + lane]);
      const float ob = (w0 * R[68 + lane] + w1 * R[200 + lane]) + (w2 * R[332 + lane] + w3 * R[464 + lane]);
      gu64* rp = PG + (size_t)(head * 8 + sp) * PG_STRIDE;
      stg(rp + lane, gr_f24(tag, oa));
      stg(rp + 64 + lane, gr_f24(tag, ob));
      if (lane < 2) stg(rp + 128 + lane, gr_f24(tag, lane == 0 ? MM : LL));
      if (lane == 0) LV[L_FLAG_S1] = 1u;
      STAMP(3);
    }
    return;
  }

  const int v = wave - 4, t = tid - 256;
  const int r8 = lane >> 3, c8 = lane & 7;
  unsigned tag = 0;
  if (is_qc) {
    const unsigned e_in = __hip_atomic_load((gu32*)(p.ws + WS_EPOCH), __ATOMIC_RELAXED, __HIP_MEMORY_SCOPE_AGENT);
    v4u w[20];
    {
      const bf16_t* src = p.wq + (size_t)(160 * v + 2 * r8) * (HQ * DH) + head * DH + 64 * sp + 8 * c8;
#pragma unroll
      for (int i = 0; i < 10; ++i) { w[2 * i] = ldnt(src + (size_t)(16 * i) * (HQ * DH)); w[2 * i + 1] = ldnt(src + (size_t)(16 * i + 1) * (HQ * DH)); }
    }
    unsigned xs[5];
#pragma unroll
    for (int i = 0; i < 5; ++i) xs[i] = ((const GAS unsigned*)p.x)[lane + 64 * i];
    const unsigned xo0 = ((const GAS unsigned*)p.x)[80 * v + lane], xo1 = ((const GAS unsigned*)p.x)[80 * v + 64 + (lane & 15)];
    __builtin_amdgcn_sched_barrier(0);
    if (v == 0 && lane == 0) { LV[L_E] = e_in; WAIT_LGKM(); LV[L_FLAG_E] = 1u; }
    float n0a, n0b, n1a, n1b;
    {
      float s = 0.f;
#pragma unroll
      for (int i = 0; i < 5; ++i) s += bflo(xs[i]) + bfhi(xs[i]);
      const float mean_f = wave_sum(s) * (1.f / D);
      float vs = 0.f;
#pragma unroll
      for (int i = 0; i < 5; ++i) { const float d0 = bflo(xs[i]) - mean_f, d1 = bfhi(xs[i]) - mean_f; vs = fmaf(d0, d0, vs); vs = fmaf(d1, d1, vs); }
      const float var = rbf(wave_sum(vs) * (1.f / D)), mean = rbf(mean_f);
      const float rs = __builtin_amdgcn_rcpf(rbf(__builtin_amdgcn_sqrtf(rbf(var + 1e-5f))));
      n0a = rbf(rbf(bflo(xo0) - mean) * rs); n0b = rbf(rbf(bfhi(xo0) - mean) * rs);
      n1a = rbf(rbf(bflo(xo1) - mean) * rs); n1b = rbf(rbf(bfhi(xo1) - mean) * rs);
      asm volatile("" : "+v"(n0a), "+v"(n0b), "+v"(n1a), "+v"(n1b));
    }
    unsigned wi[10][8];
#pragma unroll
    for (int i = 0; i < 10; ++i) { interleave(w[2 * i], w[2 * i + 1], wi[i]); PIN8(wi[i]); }
    if (lane == 0) __hip_atomic_fetch_add((LAS unsigned*)(L + L_CNT_Q), 1u, __ATOMIC_RELAXED, __HIP_MEMORY_SCOPE_WORKGROUP);
    lds_wait(LV + L_FLAG_E, 1u);
    tag = LV[L_E] + 1u;
    if (v == 0) STAMP(18);
    unsigned s0, c0, s1, c1;
    {
      const gu64* m0 = MG + 80 * v + lane; const gu64* m1 = MG + 80 * v + 64 + (lane & 15);
      for (unsigned n = 0; n < SPIN_G; ++n) {
        const u64 a0 = ldg(m0), b0 = ldg(m0 + 320), a1 = ldg(m1), b1 = ldg(m1 + 320);
        s0 = (unsigned)(a0 >> 32); c0 = (unsigned)(b0 >> 32); s1 = (unsigned)(a1 >> 32); c1 = (unsigned)(b1 >> 32);
        if (all_ok(ok_pair(a0, tag) & ok_pair(b0, tag) & ok_pair(a1, tag) & ok_pair(b1, tag))) break;
      }
    }
    if (v == 0) STAMP(5);
    L[L_H2 + 80 * v + lane] = f2bf(rbf(n0a * rbf(1.f + bflo(c0))) + bflo(s0)) | (f2bf(rbf(n0b * rbf(1.f + bfhi(c0))) + bfhi(s0)) << 16);
    if (lane < 16) L[L_H2 + 80 * v + 64 + lane] = f2bf(rbf(n1a * rbf(1.f + bflo(c1))) + bflo(s1)) | (f2bf(rbf(n1b * rbf(1.f + bfhi(c1))) + bfhi(s1)) << 16);
    WAIT_LGKM();
    float acc[8];
#pragma unroll
    for (int j = 0; j < 8; ++j) acc[j] = 0.f;
#pragma unroll
    for (int i = 0; i < 10; ++i) {
      const unsigned hp = L[L_H2 + 80 * v + 8 * i + r8];
#pragma unroll
      for (int j = 0; j < 8; ++j) acc[j] = dot2(wi[i][j], hp, acc[j]);
    }
    LF[L_MR + 64 * v + 8 * c8 + r8] = reduce_oct(acc, lane);
    WAIT_LGKM();
    unsigned old = 0;
    if (lane == 0) old = __hip_atomic_fetch_add((LAS unsigned*)(L + L_CNT_M), 1u, __ATOMIC_RELAXED, __HIP_MEMORY_SCOPE_WORKGROUP);
    if (__builtin_amdgcn_readfirstlane(old) == 3u) {
      asm volatile("" ::: "memory");
      const unsigned qb = f2bf((LF[L_MR + lane] + LF[L_MR + 64 + lane]) + (LF[L_MR + 128 + lane] + LF[L_MR + 192 + lane]));
      const unsigned up = dppu<XOR1>(qb);
      if ((lane & 1) == 0) stg(QG + head * 64 + 32 * sp + (lane >> 1), gr_pair(tag, qb | (up << 16)));
      if (lane == 0) LV[L_FLAG_H] = 1u;
      STAMP(6);
    }
  } else {
    const unsigned e_in = __hip_atomic_load((gu32*)(p.ws + WS_EPOCH), __ATOMIC_RELAXED, __HIP_MEMORY_SCOPE_AGENT);
    const __amdgpu_buffer_rsrc_t rm = __builtin_amdgcn_make_buffer_rsrc((void*)p.wmod, 0, is_mod ? D * NMOD * 2 : 0, 0x00020000);
    const __amdgpu_buffer_rsrc_t rc = __builtin_amdgcn_make_buffer_rsrc((void*)p.c, 0, is_mod ? D * 2 : 0, 0x00020000);
    const unsigned cv0 = __builtin_amdgcn_raw_buffer_load_b16(rc, (160 * v + lane) * 2, 0, 0), cv1 = __builtin_amdgcn_raw_buffer_load_b16(rc, (160 * v + 64 + lane) * 2, 0, 0),
                   cv2 = __builtin_amdgcn_raw_buffer_load_b16(rc, (160 * v + 128 + (lane & 31)) * 2, 0, 0);
    v4u wm[20];
#pragma unroll
    for (int i = 0; i < 20; ++i) wm[i] = __builtin_amdgcn_raw_buffer_load_b128(rm, ((160 * v + r8) * NMOD + 64 * mu + 8 * c8) * 2, i * (8 * NMOD * 2), 2);
    __builtin_amdgcn_sched_barrier(0);
    if (v == 0 && lane == 0) { LV[L_E] = e_in; WAIT_LGKM(); LV[L_FLAG_E] = 1u; }
    if (is_mod) {
      L[L_SC + 160 * v + lane] = f2bf(silu(bf2f(cv0))); L[L_SC + 160 * v + 64 + lane] = f2bf(silu(bf2f(cv1)));
      if (lane < 32) L[L_SC + 160 * v + 128 + lane] = f2bf(silu(bf2f(cv2)));
      WAIT_LGKM();
      float acc[8];
#pragma unroll
      for (int j = 0; j < 8; ++j) acc[j] = 0.f;
#pragma unroll
      for (int i = 0; i < 20; ++i) {
        const unsigned a = L[L_SC + 160 * v + 8 * i + r8], bb = a << 16;
#pragma unroll
        for (int j = 0; j < 4; ++j) { acc[2 * j] = dot2(wm[i][j], a, acc[2 * j]); acc[2 * j + 1] = dot2(wm[i][j], bb, acc[2 * j + 1]); }
      }
      LF[L_MR + 64 * v + 8 * c8 + r8] = reduce_oct(acc, lane);
      WAIT_LGKM();
      unsigned old = 0;
      if (lane == 0) old = __hip_atomic_fetch_add((LAS unsigned*)(L + L_CNT_M), 1u, __ATOMIC_RELAXED, __HIP_MEMORY_SCOPE_WORKGROUP);
      if (__builtin_amdgcn_readfirstlane(old) == 3u) {
        asm volatile("" ::: "memory");
        const unsigned mb_ = f2bf((LF[L_MR + lane] + LF[L_MR + 64 + lane]) + (LF[L_MR + 128 + lane] + LF[L_MR + 192 + lane]));
        const unsigned up = dppu<XOR1>(mb_);
        lds_wait(LV + L_FLAG_E, 1u);
        if ((lane & 1) == 0) stg(MG + 32 * mu + (lane >> 1), gr_pair(LV[L_E] + 1u, mb_ | (up << 16)));
        if (lane == 0) LV[L_CNT_G] = 1u;
        STAMP(4);
      }
    }
    if (v == 3 && J * 1024 < CODE_BYTES) {
      const v4u cw = ((const GAS v4u*)(code_base + (u64)(J * 1024)))[lane]; asm volatile("" :: "v"(cw));
    }
  }
  if (v == 2) {
      const int k = lane & 15;
      const GAS unsigned* a = k == 0 ? (const GAS unsigned*)(p.wo + (size_t)(head * DH) * D + 80 * sp)
                            : k == 1 ? (const GAS unsigned*)(p.wo + (size_t)(head * DH + DH - 1) * D + 80 * sp)
                            : k == 2 ? (const GAS unsigned*)p.out
                            : k == 3 ? (const GAS unsigned*)(p.ws + WS_PG + (size_t)(head * 8) * PG_STRIDE * 8)
                            : k == 4 ? (const GAS unsigned*)(p.ws + WS_YG + (size_t)(5 * sp) * 4096)
                            : k == 5 ? (const GAS unsigned*)(p.ws + WS_DG)
                            : k == 6 ? (const GAS unsigned*)(p.ws + WS_DG + 131072)
                            : k == 7 ? (const GAS unsigned*)(p.ws + WS_XG)
                            : k == 8 ? (const GAS unsigned*)(p.kc + ((size_t)head * SK + (size_t)(t0 + nt) * 16 - 1) * DH)
                            : k == 9 ? (const GAS unsigned*)(p.vc + ((size_t)head * SK + (size_t)(t0 + nt) * 16 - 1) * DH)
                            : k == 10 ? (const GAS unsigned*)(p.wo + (size_t)(head * DH + 32) * D + 80 * sp)
                            : k == 11 ? (const GAS unsigned*)(p.wo + (size_t)(head * DH + 64) * D + 80 * sp)
                            : k == 12 ? (const GAS unsigned*)(p.wo + (size_t)(head * DH + 96) * D + 80 * sp)
                            : k == 13 ? (const GAS unsigned*)(p.ws + WS_DG + 65536)
                            : k == 14 ? (const GAS unsigned*)(p.ws + WS_YG + (size_t)(5 * sp + 4) * 4096 + 4088)
                            : (const GAS unsigned*)(p.ws + WS_PG + (size_t)(head * 8 + 8) * PG_STRIDE * 8 - 8);
      const unsigned w_ = *a; asm volatile("" :: "v"(w_));
      const int st_ = min((lane & 31) * TLBSTEP, nt - 1);
      const bf16_t* kv = lane < 32 ? p.kc : p.vc;
      const unsigned w2_ = *(const GAS unsigned*)(kv + ((size_t)head * SK + (size_t)(t0 + st_) * 16) * DH); asm volatile("" :: "v"(w2_));
  }
  if (v == 1) {
    lds_wait(LV + L_FLAG_E, 1u);
    tag = LV[L_E] + 1u;
    const gu64* qp = QG + head * 64 + lane;
    if (!is_qc) __builtin_amdgcn_s_sleep(QDELAY);
    u64 g0 = ldg(qp); __builtin_amdgcn_s_sleep(STAG);
    u64 g1 = ldg(qp); __builtin_amdgcn_s_sleep(STAG);
    u64 g2 = ldg(qp), g;
    for (unsigned n = 0;; ++n) {
      if (all_ok(ok_pair(g0, tag)) || n >= SPIN_G) { g = g0; break; }
      g0 = ldg(qp);
      if (all_ok(ok_pair(g1, tag))) { g = g1; break; }
      g1 = ldg(qp);
      if (all_ok(ok_pair(g2, tag))) { g = g2; break; }
      g2 = ldg(qp);
    }
    L[L_Q + lane] = (unsigned)(g >> 32);
    WAIT_LGKM();
    if (lane == 0) LV[L_FLAG_Q] = 1u;
    STAMP(7);
  }
  lds_wait(LV + L_FLAG_E, 1u);
  tag = LV[L_E] + 1u;

  const int g4 = lane >> 4, tq = (lane & 15) >> 2, tp = lane & 3;
  v4u wf[5][8];
  unsigned mbA[2], mbB[2];
  if (is_ffn) {
#define FPACE 63
    v4u raw[5][8];
    {
      const size_t ro = (size_t)(160 * v + r8) * F + 64 * head + 8 * c8;
#pragma unroll
      for (int kb = 0; kb < 5; ++kb) {
        if (FPACE < 63) asm volatile("s_waitcnt vmcnt(%0)" :: "n"(FPACE) : "memory");
#pragma unroll
        for (int i = 0; i < 4; ++i) { raw[kb][i] = ldnt(p.wg + ro + (size_t)(32 * kb + 8 * i) * F); raw[kb][4 + i] = ldnt(p.wu + ro + (size_t)(32 * kb + 8 * i) * F); }
        __builtin_amdgcn_sched_barrier(0);
      }
    }
    __builtin_amdgcn_sched_barrier(0);
    LAS unsigned char* stg_w = LB + B_STG + v * 4096 + r8 * 128 + c8 * 16;
    const LAS unsigned char* stg_r = LB + B_STG + v * 4096 + (8 * g4 + tq) * 128 + tp * 8;
#pragma unroll
    for (int kb = 0; kb < 5; ++kb)
#pragma unroll
      for (int m = 0; m < 2; ++m) {
#pragma unroll
        for (int i = 0; i < 4; ++i) *(LAS v4u*)(stg_w + i * 1024) = raw[kb][4 * m + i];
        asm volatile("" ::: "memory");
#pragma unroll
        for (int cg = 0; cg < 4; ++cg) wf[kb][4 * m + cg] = tr_frag(stg_r + cg * 32, 512);
        asm volatile("s_waitcnt lgkmcnt(0)" ::: "memory");
      }
    if (v == 0) STAMP(21);
    if (v == 1) STAMP(18);
    lds_wait(LV + L_CNT_OV, 4u);
    if (v == 0) STAMP(22);
#pragma unroll
    for (int i = 0; i < 20; ++i) {
      const int idx = 64 * i + lane, row = idx / 20, ch = idx - 20 * row;
      GLDS(p.wd + (size_t)(64 * head + row) * D + 160 * v + 8 * ch, LB + B_WD + v * 20480 + 1024 * i, 2);
    }
    {
      const gu64* m0 = MG + 80 * v + lane; const gu64* m1 = MG + 80 * v + 64 + (lane & 15);
      for (unsigned n = 0; n < SPIN_G; ++n) {
        const u64 s0 = ldg(m0 + 960), c0 = ldg(m0 + 1280), s1 = ldg(m1 + 960), c1 = ldg(m1 + 1280);
        const unsigned sp0 = (unsigned)(s0 >> 32), cp0 = (unsigned)(c0 >> 32), sp1 = (unsigned)(s1 >> 32), cp1 = (unsigned)(c1 >> 32);
        mbA[0] = (sp0 & 0xffffu) | (f2bf(1.f + bflo(cp0)) << 16); mbB[0] = (sp0 >> 16) | (f2bf(1.f + bfhi(cp0)) << 16);
        mbA[1] = (sp1 & 0xffffu) | (f2bf(1.f + bflo(cp1)) << 16); mbB[1] = (sp1 >> 16) | (f2bf(1.f + bfhi(cp1)) << 16);
        if (all_ok(ok_pair(s0, tag) & ok_pair(c0, tag) & ok_pair(s1, tag) & ok_pair(c1, tag))) break;
        __builtin_amdgcn_s_sleep(8);
      }
    }
  }
  float own_x = 0.f, own_ga = 0.f, own_gb = 0.f;
  if (is_own) {
    const int col = 64 * own + 16 * v + c;
    const gu64* m = MG + (lane < 32 ? 640 : 1600) + (col >> 1);
    own_x = bf2f(((const GAS bf16_t*)p.x)[col]);
    unsigned g = 0;
    for (unsigned n = SPIN_G; n != 0u; --n) { const u64 a = ldg(m); g = (unsigned)(a >> 32); if (none_bad(bad_pair(a, tag))) break; __builtin_amdgcn_s_sleep(8); }
    const float gv = (c & 1) ? bfhi(g) : bflo(g);
    const float ga_h = lane < 32 ? gv : 0.f, gb_h = lane < 32 ? 0.f : gv;
    own_ga = swap32_sum(ga_h, ga_h); own_gb = swap32_sum(gb_h, gb_h);
  }
  lds_wait(LV + L_FLAG_L, 1u);
#pragma unroll
  for (int i = 0; i < 5; ++i) {
    const int idx = 256 * i + t, row = idx / 10, ch = idx - 10 * row;
    GLDS(p.wo + (size_t)(head * DH + row) * D + 80 * sp + 8 * ch, LB + B_WO + (256 * i + 64 * v) * 16, 0);
  }
  asm volatile("s_waitcnt vmcnt(0)" ::: "memory");
  if (lane == 0) __hip_atomic_fetch_add((LAS unsigned*)(L + L_CNT_W), 1u, __ATOMIC_RELAXED, __HIP_MEMORY_SCOPE_WORKGROUP);
  lds_wait(LV + L_CNT_W, 4u);
  v4u wof[4], wof4[4];
  {
    const LAS unsigned char* src = LB + B_WO + (8 * g4 + tq) * 160 + tp * 8;
#pragma unroll
    for (int kb = 0; kb < 4; ++kb) { wof[kb] = tr_frag(src + kb * 32 * 160 + v * 32, 4 * 160); wof4[kb] = tr_frag(src + kb * 32 * 160 + 4 * 32, 4 * 160); }
  }
  if (v == 0) STAMP(8);

  const int hf = lane >> 5;
  const gu64* hop = PG + (size_t)(head * 8 + hf) * PG_STRIDE + 32 * v + (lane & 31);
  const gu64* hmp = PG + (size_t)(head * 8 + (((lane & 15) >> 1) ^ hf)) * PG_STRIDE + 128 + (lane & 1);
  gu64* ysp = YG + (size_t)(5 * sp + (lane < 16 ? v : 4)) * 512 + head * 16 + c;
  LAS bf16_t* odst = (LAS bf16_t*)(L + L_O) + 32 * v + (lane & 31);
  PINP(hop); PINP(hmp); PINP(ysp);
  lds_wait(LV + L_FLAG_S1, 1u);
  if (v == 0) STAMP(9);
  {
    u64 g0, g1, g2, g3, gm;
    for (unsigned n = SPIN_G; n != 0u; --n) {
      g0 = ldg(hop); g1 = ldg(hop + 2 * PG_STRIDE); g2 = ldg(hop + 4 * PG_STRIDE); g3 = ldg(hop + 6 * PG_STRIDE); gm = ldg(hmp);
      if (none_bad((bad_f24(g0, tag) | bad_f24(g1, tag) | bad_f24(g2, tag)) | (bad_f24(g3, tag) | bad_f24(gm, tag)))) break;
      if (PSLEEP) __builtin_amdgcn_s_sleep(PSLEEP);
    }
    const float ml = val_f24(gm);
    const float mm = (lane & 1) ? -INFINITY : ml;
    float M = MAX_DPP(mm, "quad_perm:[2,3,0,1]"); M = MAX_DPP(M, "row_half_mirror"); M = MAX_DPP(M, "row_ror:8"); M = MAX_DPP(M, "quad_perm:[1,0,3,2]");
    const float wgt = __builtin_amdgcn_exp2f((mm - M) * LOG2E);
    const float wn = dpp<XOR1>(wgt);
    const float wl = (lane & 1) ? wn * ml : 0.f;
    float Ls = wl + dpp<XOR1>(wl); Ls += dpp<XOR2>(Ls); Ls += dpp<XOR7>(Ls); Ls += dpp<XOR8>(Ls);
    float acc = dpp<BCAST0>(wgt) * val_f24(g0);
    acc = fmaf(dpp<BCAST0 + 4>(wgt), val_f24(g1), acc); acc = fmaf(dpp<BCAST0 + 8>(wgt), val_f24(g2), acc); acc = fmaf(dpp<BCAST0 + 12>(wgt), val_f24(g3), acc);
    acc = swap32_sum(acc, acc);
    *odst = (bf16_t)f2bf(acc * __builtin_amdgcn_rcpf(Ls));
  }
  BARRIER();
  if (v == 0) STAMP(10);
  {
    v4u A[4];
#pragma unroll
    for (int kb = 0; kb < 4; ++kb) A[kb] = *(const LAS v4u*)(L + L_O + 16 * kb + 4 * g4);
    f32x4 y = {0.f, 0.f, 0.f, 0.f}, y4 = {0.f, 0.f, 0.f, 0.f};
#pragma unroll
    for (int kb = 0; kb < 4; ++kb) y = mfma(A[kb], wof[kb], y);
    if (v == 0) {
#pragma unroll
      for (int kb = 0; kb < 4; ++kb) y4 = mfma(A[kb], wof4[kb], y4);
    }
    if (lane < 16 || (v == 0 && lane < 32)) stg(ysp, gr_f24(tag, lane < 16 ? y[0] : y4[0]));
  }
  if (v == 0) STAMP(11);
  if (is_own) {
    const int og = 4 * own + v;
    const gu64* yp = YG + (size_t)og * 512 + lane;
    const gu64* dp = DG + (size_t)og * 512 + lane;
    const gu64* dp6 = dp + (lane < 48 ? 384 : 0);
    gu64* xp = XG + 16 * og + (c >> 1);
    GAS bf16_t* op = (GAS bf16_t*)p.out + 16 * og + c;
    PINP(yp); PINP(dp); PINP(dp6); PINP(xp); PINP(op);
    u64 g[8];
    for (unsigned n = SPIN_G; n != 0u; --n) {
      unsigned bad = 0;
#pragma unroll
      for (int i = 0; i < 8; ++i) { g[i] = ldg(yp + 64 * i); bad |= bad_f24(g[i], tag); }
      if (none_bad(bad)) break;
      if (PSLEEP) __builtin_amdgcn_s_sleep(PSLEEP);
    }
    if (v == 0) STAMP(30);
    float s = ((val_f24(g[0]) + val_f24(g[1])) + (val_f24(g[2]) + val_f24(g[3]))) + ((val_f24(g[4]) + val_f24(g[5])) + (val_f24(g[6]) + val_f24(g[7])));
    s = xrow_sum(s);
    const float x1 = rbf(own_x + rbf(own_ga * rbf(s)));
    const unsigned xb = f2bf(x1), pair = xb | (dppu<XOR1>(xb) << 16);
    if (lane < 16 && (lane & 1) == 0) stg(xp, gr_pair(tag, pair));
    if (v == 0) STAMP(12);
    for (unsigned n = SPIN_G; n != 0u; --n) {
      unsigned bad = 0;
#pragma unroll
      for (int i = 0; i < 6; ++i) { g[i] = ldg(dp + 64 * i); bad |= bad_f24(g[i], tag); }
      g[6] = ldg(dp6); bad |= bad_f24(g[6], tag);
      if (none_bad(bad)) break;
      if (PSLEEP) __builtin_amdgcn_s_sleep(PSLEEP);
    }
    if (v == 0) STAMP(15);
    s = ((val_f24(g[0]) + val_f24(g[1])) + (val_f24(g[2]) + val_f24(g[3]))) + ((val_f24(g[4]) + val_f24(g[5])) + (lane < 48 ? val_f24(g[6]) : 0.f));
    s = xrow_sum(s);
    if (lane < 16) __hip_atomic_store(op, (bf16_t)f2bf(x1 + rbf(own_gb * rbf(s))), __ATOMIC_RELAXED, __HIP_MEMORY_SCOPE_AGENT);
    if (og == 0 && lane == 0) __hip_atomic_store((gu32*)(p.ws + WS_EPOCH), tag, __ATOMIC_RELAXED, __HIP_MEMORY_SCOPE_AGENT);
    if (v == 0) STAMP(16);
  }
  if (is_ffn) {
    const int ua = 80 * v + lane, ub = 80 * v + 64 + (lane & 15);
    const gu64* xa = XG + (ua >> 3) * 16 + (ua & 7); const gu64* xb = XG + (ub >> 3) * 16 + (ub & 7);
    gu64* dq = DG + (size_t)(10 * v + g4) * 512 + head * 16 + c;
    PINP(xa); PINP(xb); PINP(dq);
    unsigned xo0, xo1;
    for (unsigned n = SPIN_G; n != 0u; --n) {
      const u64 g0 = ldg(xa), g1 = ldg(xb);
      xo0 = (unsigned)(g0 >> 32); xo1 = (unsigned)(g1 >> 32);
      if (none_bad(bad_pair(g0, tag) | bad_pair(g1, tag))) break;
      if (PSLEEP) __builtin_amdgcn_s_sleep(PSLEEP);
    }
    if (v == 0) STAMP(13);
    {
      const unsigned xm = lane < 16 ? xo1 : 0u;
      float s1 = dot2(xm, 0x3f803f80u, dot2(xo0, 0x3f803f80u, 0.f)), s2 = dot2(xm, xm, dot2(xo0, xo0, 0.f));
      s1 += dpp<XOR1>(s1); s2 += dpp<XOR1>(s2); s1 += dpp<XOR2>(s1); s2 += dpp<XOR2>(s2);
      s1 += dpp<XOR7>(s1); s2 += dpp<XOR7>(s2); s1 += dpp<XOR8>(s1); s2 += dpp<XOR8>(s2);
      s1 = xrow_sum(s1); s2 = xrow_sum(s2);
      if (lane == 0) *(LAS f32x2*)(LF + L_ST + 2 * v) = f32x2{s1, s2};
    }
    if (v == 0) STAMP(25);
    BARRIER();
    if (v == 0) STAMP(26);
    {
      const f32x4 sa = *(const LAS f32x4*)(LF + L_ST), sb = *(const LAS f32x4*)(LF + L_ST + 4);
      const float mean_f = ((sa[0] + sa[2]) + (sb[0] + sb[2])) * (1.f / D);
      const float ex2 = ((sa[1] + sa[3]) + (sb[1] + sb[3])) * (1.f / D);
      float var_f = fmaf(-mean_f, mean_f, ex2);
      if (__builtin_amdgcn_ballot_w64(var_f * 256.f < ex2) != 0ull) {
        float vs = 0.f;
#pragma unroll
        for (int i = 0; i < 5; ++i) { const int u_ = lane + 64 * i; const unsigned xp = (unsigned)(ldg(XG + (u_ >> 3) * 16 + (u_ & 7)) >> 32); const float d0 = bflo(xp) - mean_f, d1 = bfhi(xp) - mean_f; vs = fmaf(d0, d0, vs); vs = fmaf(d1, d1, vs); }
        var_f = wave_sum(vs) * (1.f / D);
      }
      const float var = rbf(var_f), mean = rbf(mean_f);
      const float rs = __builtin_amdgcn_rcpf(rbf(__builtin_amdgcn_sqrtf(rbf(var + 1e-5f))));
#define LNPAIR(xp, ma, mb_) ({ const unsigned d_ = pk2(bflo(xp) - mean, bfhi(xp) - mean); const unsigned n_ = pk2(bflo(d_) * rs, bfhi(d_) * rs); \
        const unsigned m_ = pk2(bflo(n_) * bfhi(ma), bfhi(n_) * bfhi(mb_)); pk2(bflo(m_) + bflo(ma), bfhi(m_) + bflo(mb_)); })
      L[L_H2 + 80 * v + lane] = LNPAIR(xo0, mbA[0], mbB[0]);
      const unsigned h1 = LNPAIR(xo1, mbA[1], mbB[1]);
      if (lane < 16) L[L_H2 + 80 * v + 64 + lane] = h1;
#undef LNPAIR
    }
    if (v == 0) STAMP(27);
    v4u wd[2][10];
    {
      v4u A[5];
#pragma unroll
      for (int kb = 0; kb < 5; ++kb) A[kb] = *(const LAS v4u*)(L + L_H2 + 80 * v + 16 * kb + 4 * g4);
      f32x4 acc[8];
#pragma unroll
      for (int cg = 0; cg < 8; ++cg) acc[cg] = f32x4{0.f, 0.f, 0.f, 0.f};
      const LAS unsigned char* src = LB + B_WD + v * 20480 + (8 * g4 + tq) * 320 + tp * 8;
#pragma unroll
      for (int kb = 0; kb < 5; ++kb) {
#pragma unroll
        for (int cg = 0; cg < 8; ++cg) acc[cg] = mfma(A[kb], wf[kb][cg], acc[cg]);
        __builtin_amdgcn_sched_barrier(0);
        if (kb < 4) {
#pragma unroll
          for (int j = 0; j < 5; ++j) { const int f = 5 * kb + j; wd[f / 10][f % 10] = tr_frag(src + (f / 10) * 32 * 320 + (f % 10) * 32, 4 * 320); }
        }
        __builtin_amdgcn_sched_barrier(0);
      }
      LF[L_RGU + v * 128 + lane] = sel4(g4, acc[0][0], acc[1][0], acc[2][0], acc[3][0]);
      LF[L_RGU + v * 128 + 64 + lane] = sel4(g4, acc[4][0], acc[5][0], acc[6][0], acc[7][0]);
    }
    if (v == 0) STAMP(28);
    BARRIER();
    if (v == 0) STAMP(29);
    {
      const float G = rbf((LF[L_RGU + lane] + LF[L_RGU + 128 + lane]) + (LF[L_RGU + 256 + lane] + LF[L_RGU + 384 + lane]));
      const float U = rbf((LF[L_RGU + 64 + lane] + LF[L_RGU + 192 + lane]) + (LF[L_RGU + 320 + lane] + LF[L_RGU + 448 + lane]));
      ((LAS bf16_t*)(L + L_ACT + 32 * v))[lane] = (bf16_t)f2bf(rbf(silu(G)) * U);
      if (v == 0) STAMP(14);
      const v4u A0 = *(const LAS v4u*)(L + L_ACT + 32 * v + 4 * g4), A1 = *(const LAS v4u*)(L + L_ACT + 32 * v + 16 + 4 * g4);
      f32x4 acc[10];
#pragma unroll
      for (int cg = 0; cg < 10; ++cg) { acc[cg] = mfma(A0, wd[0][cg], f32x4{0.f, 0.f, 0.f, 0.f}); acc[cg] = mfma(A1, wd[1][cg], acc[cg]); }
      stg(dq, gr_f24(tag, sel4(g4, acc[0][0], acc[1][0], acc[2][0], acc[3][0])));
      stg(dq + 4 * 512, gr_f24(tag, sel4(g4, acc[4][0], acc[5][0], acc[6][0], acc[7][0])));
      if (lane < 32) stg(dq + 8 * 512, gr_f24(tag, g4 == 0 ? acc[8][0] : acc[9][0]));
    }
    if (v == 0) STAMP(17);
  }
  if (v == 0) STAMP_DUMP();
}

extern "C" void kernel_launch(void* const* d_in, const int* in_sizes, int n_in, void* d_out, int out_size, void* d_ws, size_t ws_size, hipStream_t stream) {
  Params p;
  p.x = (const bf16_t*)d_in[0]; p.kc = (const bf16_t*)d_in[1]; p.vc = (const bf16_t*)d_in[2]; p.wq = (const bf16_t*)d_in[3]; p.wo = (const bf16_t*)d_in[4];
  p.wg = (const bf16_t*)d_in[6]; p.wu = (const bf16_t*)d_in[7]; p.wd = (const bf16_t*)d_in[8]; p.c = (const bf16_t*)d_in[9]; p.wmod = (const bf16_t*)d_in[10];
  p.out = (bf16_t*)d_out; p.ws = (unsigned char*)d_ws;
  (void)hipFuncSetAttribute((const void*)adaln_block_kernel, hipFuncAttributeMaxDynamicSharedMemorySize, LDS_BYTES);
  adaln_block_kernel<<<NCU, NTHR, LDS_BYTES, stream>>>(p);
}
```

```cpp
#include <hip/hip_runtime.h>
#include <hip/hip_bf16.h>
#include <stdint.h>

#define TD 6
#define PACE 0
#define PACE0 0
#define KVDELAY 0
#define PSLEEP 0
#define TLBWARM 2
#define TLBSTEP 4
#define MAP1 1
#define RLEAD 2
#define WOLEAD 1
#define GATE 1
#define STAG 12
#define QDELAY 64
#define STAMPS 0
#define NUNITS 16
constexpr int NUNIT = NUNITS, NSTAT = 512 - 4 * NUNIT, MAXU = 5, NREC = 8 + NUNIT;
static_assert(NUNIT % 2 == 0 && NUNIT >= 8 && NUNIT <= 16, "H1 is written for 16..24 partials");
#define NT_Q 44
#define NT_M 57
#define NT_F 48
#define NT_P ((NSTAT - 2 * NT_Q - 2 * NT_M - NT_F) / 3)
static_assert(2 * NT_Q + 2 * NT_M + NT_F + 3 * NT_P == NSTAT, "tiles of a head");
#define NT_G0 44
#define NT_G1 58
#define NT_G2 ((NSTAT - 2 * NT_G0 - 2 * NT_G1) / 4)
static_assert(2 * NT_G0 + 2 * NT_G1 + 4 * NT_G2 == NSTAT, "tiles of a head");
#define NT_H0 44
#define NT_H1 ((NSTAT - 2 * NT_H0) / 6)
static_assert(2 * NT_H0 + 6 * NT_H1 == NSTAT, "tiles of a head");
static_assert(NT_F >= 40 && NT_F <= 48 && NT_Q >= 40 && NT_Q <= 48 && NT_G0 >= 40 && NT_G0 <= 48 && NT_H0 >= 40 && NT_H0 <= 48 && NT_P >= 52 && NT_M >= 52 && NT_G1 >= 52 && NT_G2 >= 52 && NT_H1 >= 52,
              "a stream wave takes 10..12 tiles where the CU draws no units (q and FFN CUs), at least 13 where it does (the catch-up schedule)");

typedef unsigned short bf16_t;
typedef unsigned long long u64;
typedef unsigned v4u __attribute__((ext_vector_type(4)));
typedef float f32x4 __attribute__((ext_vector_type(4)));
typedef float f32x2 __attribute__((ext_vector_type(2)));
typedef unsigned v2u __attribute__((ext_vector_type(2)));
typedef __bf16 bf16x2 __attribute__((ext_vector_type(2)));
typedef __bf16 bf16x8 __attribute__((ext_vector_type(8)));
typedef short v4s __attribute__((ext_vector_type(4)));
#define GAS __attribute__((address_space(1)))
#define LAS __attribute__((address_space(3)))
typedef GAS u64 gu64;
typedef GAS unsigned gu32;

constexpr int D = 640, HQ = 32, SK = 8192, DH = 128, F = 1728, NMOD = 6 * D;
constexpr int NCU = 256, NTHR = 512, NFFN = 27;
constexpr float LOG2E = 1.4426950408889634f;

constexpr size_t WS_EPOCH = 0;
constexpr size_t WS_XG = 393216 + 32768;
constexpr size_t WS_MG = 8192;
constexpr size_t WS_QG = 32768;
constexpr size_t WS_YG = 65536;
constexpr size_t WS_DG = 262144;
constexpr size_t WS_PG = 524288;
constexpr size_t WS_TK = 1572864;
constexpr int PG_STRIDE = 160;
constexpr size_t WS_STAMP = 64u << 20;

constexpr int L_FLAG_Q = 0, L_CNT_S1 = 1, L_FLAG_S1 = 2, L_FLAG_E = 3, L_E = 4, L_CNT_M = 5, L_FLAG_H = 6, L_CNT_G = 7, L_CNT_W = 8, L_FLAG_L = 9, L_CNT_OV = 10, L_CNT_Q = 11, L_REQ = 12, L_TK = 16, L_CNT_F = 24;
constexpr int L_Q = 32;
constexpr int L_REC = 96;
constexpr int L_O = 624;
constexpr int L_MR = 704;
constexpr int L_SC = 960;
constexpr int L_H2 = 1600;
constexpr int L_ST = 1920;
constexpr int L_RGU = 1936;
constexpr int L_ACT = 2448;
constexpr int L_STAMP = 2816;
constexpr int B_STG = 12288;
constexpr int B_WO = 28672;
constexpr int B_WD = 49152;
constexpr int B_OV = 28672;
constexpr int LDS_BYTES = 163840;
static_assert(B_OV + 4 * 32768 <= LDS_BYTES && B_WD + 81920 <= LDS_BYTES, "lds");

constexpr int CODE_BYTES = 43 * 1024;
constexpr unsigned SPIN_G = 1u << 15;
constexpr unsigned SPIN_L = 1u << 20;

struct Params {
  const bf16_t* x; const bf16_t* kc; const bf16_t* vc; const bf16_t* wq; const bf16_t* wo;
  const bf16_t* wg; const bf16_t* wu; const bf16_t* wd; const bf16_t* c; const bf16_t* wmod;
  bf16_t* out; unsigned char* ws;
};

__device__ __forceinline__ float bf2f(unsigned b) { return __uint_as_float(b << 16); }
__device__ __forceinline__ float bfhi(unsigned b) { return __uint_as_float(b & 0xffff0000u); }
__device__ __forceinline__ float bflo(unsigned b) { return __uint_as_float(b << 16); }
__device__ __forceinline__ unsigned f2bf(float f) { __bf16 b = (__bf16)f; return (unsigned)__builtin_bit_cast(unsigned short, b); }
__device__ __forceinline__ float rbf(float f) { return bf2f(f2bf(f)); }
__device__ __forceinline__ unsigned pk2(float lo, float hi) { return __builtin_bit_cast(unsigned, __builtin_convertvector(f32x2{lo, hi}, bf16x2)); }
__device__ __forceinline__ float silu(float x) { return x * __builtin_amdgcn_rcpf(1.f + __builtin_amdgcn_exp2f(-x * LOG2E)); }
__device__ __forceinline__ float dot2(unsigned a, unsigned b, float acc) {
  return __builtin_amdgcn_fdot2_f32_bf16(__builtin_bit_cast(bf16x2, a), __builtin_bit_cast(bf16x2, b), acc, false);
}
template <int CTRL> __device__ __forceinline__ float dpp(float x) {
  return __builtin_bit_cast(float, __builtin_amdgcn_mov_dpp(__builtin_bit_cast(int, x), CTRL, 0xf, 0xf, true));
}
template <int CTRL> __device__ __forceinline__ unsigned dppu(unsigned x) { return (unsigned)__builtin_amdgcn_mov_dpp((int)x, CTRL, 0xf, 0xf, true); }
#define MAX_DPP(x, ctrl) ({ float r_; asm("s_nop 1\n\tv_max_f32_dpp %0, %1, %1 " ctrl " row_mask:0xf bank_mask:0xf bound_ctrl:1" : "=v"(r_) : "v"(x)); r_; })
constexpr int XOR1 = 0xB1, XOR2 = 0x4E, XOR7 = 0x141, XOR8 = 0x128, ROR4 = 0x124, BCAST0 = 0x150;
__device__ __forceinline__ float swap32_sum(float a, float b) {
  auto t = __builtin_amdgcn_permlane32_swap(__float_as_uint(a), __float_as_uint(b), false, false);
  return __uint_as_float(t[0]) + __uint_as_float(t[1]);
}
__device__ __forceinline__ float swap16_sum(float a, float b) {
  auto t = __builtin_amdgcn_permlane16_swap(__float_as_uint(a), __float_as_uint(b), false, false);
  return __uint_as_float(t[0]) + __uint_as_float(t[1]);
}
__device__ __forceinline__ float xrow_max(float x) {
  auto s = __builtin_amdgcn_permlane16_swap(__float_as_uint(x), __float_as_uint(x), false, false);
  x = fmaxf(__uint_as_float(s[0]), __uint_as_float(s[1]));
  auto t = __builtin_amdgcn_permlane32_swap(__float_as_uint(x), __float_as_uint(x), false, false);
  return fmaxf(__uint_as_float(t[0]), __uint_as_float(t[1]));
}
__device__ __forceinline__ float xrow_sum(float x) { x = swap16_sum(x, x); return swap32_sum(x, x); }
__device__ __forceinline__ float wave_sum(float x) {
  x += dpp<XOR1>(x); x += dpp<XOR2>(x); x += dpp<XOR7>(x); x += dpp<XOR8>(x);
  return xrow_sum(x);
}
__device__ __forceinline__ void reduce_rows(const float* x, float& e0, float& e1) {
  const float y0 = swap32_sum(x[0], x[4]), y1 = swap32_sum(x[1], x[5]), y2 = swap32_sum(x[2], x[6]), y3 = swap32_sum(x[3], x[7]);
  e0 = swap16_sum(y0, y2); e1 = swap16_sum(y1, y3);
}
__device__ __forceinline__ float reduce_oct(const float* x, int lane) {
  float e0, e1; reduce_rows(x, e0, e1);
  const bool b3 = (lane & 8) != 0;
  return (b3 ? e1 : e0) + dpp<XOR8>(b3 ? e0 : e1);
}

__device__ __forceinline__ u64 ldg(const gu64* p) { return __hip_atomic_load(p, __ATOMIC_RELAXED, __HIP_MEMORY_SCOPE_AGENT); }
__device__ __forceinline__ void stg(gu64* p, u64 v) { __hip_atomic_store(p, v, __ATOMIC_RELAXED, __HIP_MEMORY_SCOPE_AGENT); }
__device__ __forceinline__ u64 gr_pair(unsigned tag, unsigned pay) { return ((u64)pay << 32) | tag; }
__device__ __forceinline__ u64 gr_f24(unsigned tag, float v) { return ((u64)((__float_as_uint(v) + 0x80u) & 0xffffff00u) << 32) | tag; }
__device__ __forceinline__ unsigned bad_pair(u64 g, unsigned tag) { return (unsigned)g ^ tag; }
__device__ __forceinline__ unsigned bad_f24(u64 g, unsigned tag) { return ((unsigned)g ^ tag) | ((unsigned)(g >> 32) & 0xffu); }
__device__ __forceinline__ float val_f24(u64 g) { return __uint_as_float((unsigned)(g >> 32)); }
__device__ __forceinline__ bool none_bad(unsigned bad) { return __builtin_amdgcn_ballot_w64(bad != 0u) == 0ull; }
__device__ __forceinline__ bool ok_pair(u64 g, unsigned tag) { return (unsigned)g == tag; }
__device__ __forceinline__ bool all_ok(bool ok) { return __builtin_amdgcn_ballot_w64(!ok) == 0ull; }
#define PINP(ptr) asm volatile("" : "+v"(ptr))

__device__ __forceinline__ void lds_wait(volatile LAS unsigned* w, unsigned want) {
#define LSLEEP 1
  for (unsigned n = 0; *w != want && n < SPIN_L; ++n) __builtin_amdgcn_s_sleep(LSLEEP);
  asm volatile("" ::: "memory");
}
__device__ __forceinline__ unsigned lds_wait_nz(volatile LAS unsigned* w) {
  unsigned x = *w;
  for (unsigned n = 0; x == 0u && n < SPIN_L; ++n) { __builtin_amdgcn_s_sleep(1); x = *w; }
  asm volatile("" ::: "memory");
  x = __builtin_amdgcn_readfirstlane(x);
  return x == 0u ? 0x80u : x;
}
#define WAIT_LGKM() asm volatile("s_waitcnt lgkmcnt(0)" ::: "memory")
#define BARRIER() do { asm volatile("s_waitcnt lgkmcnt(0)" ::: "memory"); __builtin_amdgcn_s_barrier(); asm volatile("" ::: "memory"); } while (0)

#define STAMP(i) do { } while (0)
#define STAMP_DUMP() do { } while (0)

struct AttnState { float m, l; float acc[8]; };
#define PUBLISH_REC(R_, rec_id) do { \
        const float m0_ = R_[0], m1_ = R_[132], m2_ = R_[264], m3_ = R_[396]; \
        const float MM_ = fmaxf(fmaxf(m0_, m1_), fmaxf(m2_, m3_)); \
        const float w0_ = __builtin_amdgcn_exp2f((m0_ - MM_) * LOG2E), w1_ = __builtin_amdgcn_exp2f((m1_ - MM_) * LOG2E); \
        const float w2_ = __builtin_amdgcn_exp2f((m2_ - MM_) * LOG2E), w3_ = __builtin_amdgcn_exp2f((m3_ - MM_) * LOG2E); \
        const float LL_ = (w0_ * R_[1] + w1_ * R_[133]) + (w2_ * R_[265] + w3_ * R_[397]); \
        const float oa_ = (w0_ * R_[4 + lane] + w1_ * R_[136 + lane]) + (w2_ * R_[268 + lane] + w3_ * R_[400 + lane]); \
        const float ob_ = (w0_ * R_[68 + lane] + w1_ * R_[200 + lane]) + (w2_ * R_[332 + lane] + w3_ * R_[464 + lane]); \
        gu64* rp_ = PG + (size_t)(head * NREC + (rec_id)) * PG_STRIDE; \
        stg(rp_ + lane, gr_f24(tag, oa_)); \
        stg(rp_ + 64 + lane, gr_f24(tag, ob_)); \
        if (lane < 2) stg(rp_ + 128 + lane, gr_f24(tag, lane == 0 ? MM_ : LL_)); \
      } while (0)

__device__ __forceinline__ void attn_tile(const v4u (&K)[4], const v4u (&V)[4], const v4u& q, AttnState& st, int c) {
  float x[4];
#pragma unroll
  for (int i = 0; i < 4; ++i) {
    float d = dot2(K[i].x, q.x, 0.f);
    d = dot2(K[i].y, q.y, d); d = dot2(K[i].z, q.z, d); d = dot2(K[i].w, q.w, d);
    x[i] = d;
  }
  const bool b3 = (c & 8) != 0, b2 = (c & 4) != 0;
  const float k0 = b3 ? x[2] : x[0], s0 = b3 ? x[0] : x[2];
  const float k1 = b3 ? x[3] : x[1], s1 = b3 ? x[1] : x[3];
  const float y0 = k0 + dpp<XOR8>(s0), y1 = k1 + dpp<XOR8>(s1);
  const float k2 = b2 ? y1 : y0, s2 = b2 ? y0 : y1;
  float z = k2 + dpp<XOR7>(s2);
  z += dpp<XOR2>(z); z += dpp<XOR1>(z);
  const float s = rbf(rbf(z) * 0.08838834764831845f);
  float pm = MAX_DPP(s, "row_ror:4"); pm = MAX_DPP(pm, "row_ror:8");
  const float mn = fmaxf(st.m, pm);
  if (__builtin_amdgcn_ballot_w64(mn > st.m) != 0ull) {
    const float al = __builtin_amdgcn_exp2f((st.m - mn) * LOG2E);
    st.l *= al;
#pragma unroll
    for (int j = 0; j < 8; ++j) st.acc[j] *= al;
    st.m = mn;
  }
  const float pr = __builtin_amdgcn_exp2f((s - st.m) * LOG2E);
  st.l += pr;
  const unsigned plo = f2bf(pr), phi = plo << 16;
#define PV(i) { const unsigned a = dppu<BCAST0 + 4 * i>(plo), b = dppu<BCAST0 + 4 * i>(phi); \
    st.acc[0] = dot2(V[i].x, a, st.acc[0]); st.acc[1] = dot2(V[i].x, b, st.acc[1]); st.acc[2] = dot2(V[i].y, a, st.acc[2]); st.acc[3] = dot2(V[i].y, b, st.acc[3]); \
    st.acc[4] = dot2(V[i].z, a, st.acc[4]); st.acc[5] = dot2(V[i].z, b, st.acc[5]); st.acc[6] = dot2(V[i].w, a, st.acc[6]); st.acc[7] = dot2(V[i].w, b, st.acc[7]); }
  PV(0) PV(1) PV(2) PV(3)
#undef PV
}

__device__ __forceinline__ void interleave(const v4u& r0, const v4u& r1, unsigned* o) {
#pragma unroll
  for (int j = 0; j < 4; ++j) { o[2 * j] = __builtin_amdgcn_perm(r1[j], r0[j], 0x05040100u); o[2 * j + 1] = __builtin_amdgcn_perm(r1[j], r0[j], 0x07060302u); }
}
__device__ __forceinline__ f32x4 mfma(const v4u& a, const v4u& b, const f32x4& c) {
  return __builtin_amdgcn_mfma_f32_16x16x32_bf16(__builtin_bit_cast(bf16x8, a), __builtin_bit_cast(bf16x8, b), c, 0, 0, 0);
}
__device__ __forceinline__ v4u tr_frag(const LAS unsigned char* p, int stride4) {
  const v2u lo = __builtin_bit_cast(v2u, __builtin_amdgcn_ds_read_tr16_b64_v4i16((LAS v4s*)p));
  const v2u hi = __builtin_bit_cast(v2u, __builtin_amdgcn_ds_read_tr16_b64_v4i16((LAS v4s*)(p + stride4)));
  return v4u{lo.x, lo.y, hi.x, hi.y};
}
#define GLDS(gsrc, ldst, aux) __builtin_amdgcn_global_load_lds((const GAS void*)(gsrc), (LAS void*)(ldst), 16, 0, aux)
__device__ __forceinline__ float sel4(int q4, float a, float b, float c, float d) { return q4 == 0 ? a : q4 == 1 ? b : q4 == 2 ? c : d; }
__device__ __forceinline__ v4u ldnt(const bf16_t* p) { return __builtin_nontemporal_load((const GAS v4u*)p); }
#define PIN8(a) asm volatile("" : "+v"((a)[0]), "+v"((a)[1]), "+v"((a)[2]), "+v"((a)[3]), "+v"((a)[4]), "+v"((a)[5]), "+v"((a)[6]), "+v"((a)[7]))

__global__ void __launch_bounds__(NTHR, 2) adaln_block_kernel(Params p) {
  extern __shared__ __attribute__((aligned(16))) unsigned lds_raw[];
  LAS unsigned* L = (LAS unsigned*)lds_raw;
  volatile LAS unsigned* LV = (volatile LAS unsigned*)lds_raw;
  LAS float* LF = (LAS float*)lds_raw;
  LAS unsigned char* LB = (LAS unsigned char*)lds_raw;
  u64 code_base;
  asm volatile("s_getpc_b64 %0" : "=s"(code_base));
  code_base &= ~255ull;
  const int tid = threadIdx.x, lane = tid & 63, wave = __builtin_amdgcn_readfirstlane(tid >> 6);
  const int r = lane >> 4, c = lane & 15;
  const int b = blockIdx.x, X = b & 7, J = b >> 3;
  const int head = J, sp = (X + J) & 7;
  const bool fh = head < NFFN;
  const bool is_qc = sp < 2, is_ffn = fh && sp == 5;
  const int mu = (sp == 2 || sp == 3) ? 2 * head + sp - 2 : 999;
  const int own = head;
  const bool is_mod = mu < 60, is_own = sp == 4 && head < 10;
  const int cf = fh, cg = !fh && head < 30, ch_ = head >= 30;
  const int nq_ = cf * NT_Q + cg * NT_G0 + ch_ * NT_H0, nm_ = cf * NT_M + cg * NT_G1 + ch_ * NT_H1;
  const int np_ = cf * NT_P + cg * NT_G2 + ch_ * NT_H1, nf_ = cf * NT_F + cg * NT_G2 + ch_ * NT_H1;
  const int t0 = min(sp, 2) * nq_ + min(max(sp - 2, 0), 2) * nm_ + (int)(sp > 4) * np_ + (int)(sp > 5) * nf_ + (int)(sp > 6) * np_;
  const int nt = (int)(sp < 2) * nq_ + (int)(sp == 2 || sp == 3) * nm_ + (int)(sp == 5) * nf_ + (int)(sp == 4 || sp > 5) * np_;
  if (tid < 32) L[tid] = 0u;
  asm volatile("" :: "s"(p.kc), "s"(p.vc), "s"(p.ws), "s"(p.wq), "s"(p.wmod), "s"(p.c), "s"(p.x), "s"(t0), "s"(nt));
  BARRIER();
  gu64* const XG = (gu64*)(p.ws + WS_XG); gu64* const MG = (gu64*)(p.ws + WS_MG); gu64* const YG = (gu64*)(p.ws + WS_YG);
  gu64* const DG = (gu64*)(p.ws + WS_DG); gu64* const QG = (gu64*)(p.ws + WS_QG); gu64* const PG = (gu64*)(p.ws + WS_PG);

  if (__builtin_expect(wave < 4, 1)) {
    const size_t base = (size_t)head * SK * DH;
    const __amdgpu_buffer_rsrc_t rk = __builtin_amdgcn_make_buffer_rsrc((void*)(p.kc + base), 0, SK * DH * 2, 0x00020000);
    const __amdgpu_buffer_rsrc_t rv = __builtin_amdgcn_make_buffer_rsrc((void*)(p.vc + base), 0, SK * DH * 2, 0x00020000);
    const int voff = lane * 16;
    const int nw = (nt - wave + 3) >> 2;
    constexpr int NOWHERE = 0x7ffff000;
    const int off0 = (t0 + wave) * 4096;
    v4u K[TD][4], V[TD][4];
#define OFF(t) ((t) < nw ? off0 + (t) * 16384 : NOWHERE)
#define ISSUE_AT(d, so) do { const int so_ = (so); \
    _Pragma("unroll") for (int i_ = 0; i_ < 4; ++i_) K[d][i_] = __builtin_amdgcn_raw_buffer_load_b128(rk, voff + 1024 * i_, so_, 2); \
    _Pragma("unroll") for (int i_ = 0; i_ < 4; ++i_) V[d][i_] = __builtin_amdgcn_raw_buffer_load_b128(rv, voff + 1024 * i_, so_, 2); \
    __builtin_amdgcn_sched_barrier(0); } while (0)
#define ISSUE(d, t) ISSUE_AT(d, OFF(t))
#define PACE_WAIT(n) do { asm volatile("s_waitcnt vmcnt(%0)" :: "n"(n) : "memory"); __builtin_amdgcn_sched_barrier(0); } while (0)
#define QGATE 1
    if (QGATE == 1 && is_qc) lds_wait(LV + L_FLAG_H, 1u);
    if (QGATE == 2 && is_qc) lds_wait(LV + L_CNT_Q, 4u);
    if (is_mod) lds_wait(LV + L_CNT_G, 1u);
    if (KVDELAY) __builtin_amdgcn_s_sleep(KVDELAY);
    if (wave == 0) STAMP(24);
    ISSUE(0, 0);
#pragma unroll
    for (int d = 1; d < TD; ++d) { PACE_WAIT(PACE0); ISSUE(d, d); }
    const unsigned ov = (unsigned)(size_t)(LB + B_OV + wave * 32768);
#define DMA4(m0v, rsrc, so) do { unsigned keep_; asm volatile("s_mov_b32 %0, m0\n\ts_mov_b32 m0, %1\n\ts_nop 0\n\t" \
      "buffer_load_dwordx4 %2, %3, %4 offen nt lds\n\tbuffer_load_dwordx4 %2, %3, %4 offen offset:1024 nt lds\n\t" \
      "buffer_load_dwordx4 %2, %3, %4 offen offset:2048 nt lds\n\tbuffer_load_dwordx4 %2, %3, %4 offen offset:3072 nt lds\n\t" \
      "s_mov_b32 m0, %0" : "=&s"(keep_) : "s"(m0v), "v"(voff), "s"(rsrc), "s"(so) : "memory"); } while (0)
#define DMA_TILE(e, t) do { const int so_ = off0 + (t) * 16384; DMA4(ov + (e) * 8192, rk, so_); DMA4(ov + (e) * 8192 + 4096, rv, so_); __builtin_amdgcn_sched_barrier(0); } while (0)
#define LD_OV(d, e) do { const LAS v4u* src_ = (const LAS v4u*)(LB + B_OV + wave * 32768 + (e) * 8192) + lane; \
      _Pragma("unroll") for (int i_ = 0; i_ < 4; ++i_) { K[d][i_] = src_[64 * i_]; V[d][i_] = src_[256 + 64 * i_]; } __builtin_amdgcn_sched_barrier(0); } while (0)
#pragma unroll
    for (int e = 0; e < 4; ++e) { PACE_WAIT(PACE0); DMA_TILE(e, 6 + e); }
    lds_wait(LV + L_FLAG_Q, 1u);
    if (wave == 0) STAMP(1);
    const v4u q = *(const LAS v4u*)(L + L_Q + 4 * c);
    const unsigned tag = L[L_E] + 1u;
    AttnState st; st.m = -INFINITY; st.l = 0.f;
#pragma unroll
    for (int j = 0; j < 8; ++j) st.acc[j] = 0.f;
#define TILE(d) do { attn_tile(K[d], V[d], q, st, c); __builtin_amdgcn_sched_barrier(0); } while (0)
    static_assert(TD == 6, "the catch-up schedule is written for six slots");
    TILE(0); PACE_WAIT(0); ISSUE(0, 10);
    TILE(1); TILE(2); TILE(3); TILE(4);
    LD_OV(4, 0);
    PACE_WAIT(PACE); ISSUE(1, 11);
    TILE(5);
    LD_OV(5, 1);
    TILE(4);
    LD_OV(4, 2);
    TILE(5);
    LD_OV(5, 3);
    WAIT_LGKM();
    if (lane == 0) __hip_atomic_fetch_add((LAS unsigned*)(L + L_CNT_OV), 1u, __ATOMIC_RELAXED, __HIP_MEMORY_SCOPE_WORKGROUP);
    if (11 + RLEAD >= nw && wave == 0 && lane == 0) LV[L_REQ] = (unsigned)(11 + RLEAD + 1 - nw);
    PACE_WAIT(PACE); ISSUE(2, 12);
    if ((is_ffn || is_qc) && wave == 0 && lane == 0) LV[L_FLAG_L] = 1u;
    TILE(4); TILE(5);
    if (nw > 10) TILE(0);
    if (nw > 11) TILE(1);
#define FLUSH(which, rec_id, last) do { \
      const float M_ = xrow_max(st.m); \
      const float sc_ = __builtin_amdgcn_exp2f((st.m - M_) * LOG2E); \
      float l_ = ((c & 3) == 0) ? st.l * sc_ : 0.f; \
      l_ += dpp<ROR4>(l_); l_ += dpp<XOR8>(l_); l_ = xrow_sum(l_); \
      _Pragma("unroll") for (int j_ = 0; j_ < 8; ++j_) st.acc[j_] *= sc_; \
      float e0_, e1_; reduce_rows(st.acc, e0_, e1_); \
      LAS float* R_ = (which) == 0 ? LF + L_REC : (LAS float*)(LB + B_STG) + ((which) - 1) * 528; \
      *(LAS f32x2*)(R_ + wave * 132 + 4 + 8 * c + 2 * r) = f32x2{e0_, e1_}; \
      if (lane == 0) { R_[wave * 132] = M_; R_[wave * 132 + 1] = l_; } \
      WAIT_LGKM(); \
      unsigned old_ = 0; \
      if (lane == 0) old_ = __hip_atomic_fetch_add((LAS unsigned*)(L + L_CNT_F + (which)), (last) ? 16u : 1u, __ATOMIC_RELAXED, __HIP_MEMORY_SCOPE_WORKGROUP); \
      if ((last) && __builtin_amdgcn_readfirstlane(old_) == 48u) {     \
        asm volatile("" ::: "memory"); \
        PUBLISH_REC(R_, rec_id); \
        if (lane == 0) LV[L_FLAG_S1] = 1u; \
        STAMP(3); \
      } \
      st.m = -INFINITY; st.l = 0.f; \
      _Pragma("unroll") for (int j_ = 0; j_ < 8; ++j_) st.acc[j_] = 0.f; \
    } while (0)
    if (is_ffn || is_qc) {
      if (wave == 0) STAMP(2);
      FLUSH(0, sp, true);
      return;
    }
    unsigned cur = 0;
    bool more = true;
#pragma nounroll
    for (int u = 12; more; u += TD) {
#pragma unroll
      for (int k = 0; k < TD; ++k) {
        if (more) {
          const int uu = u + k;
          int so = off0 + (uu + 1) * 16384;
          unsigned nxt = 0;
          if (uu + 1 >= nw) {
            const int i = uu + 1 - nw;
            nxt = i < MAXU ? lds_wait_nz(LV + L_TK + i) : 0x80u;
            so = nxt <= (unsigned)NUNIT ? (NSTAT + 4 * ((int)nxt - 1) + wave) * 4096 : NOWHERE;
            more = nxt <= (unsigned)NUNIT;
          }
          if (uu + RLEAD >= nw && wave == 0 && lane == 0) LV[L_REQ] = (unsigned)(uu + RLEAD + 1 - nw);
          PACE_WAIT(PACE);
          ISSUE_AT((k + 3) % TD, so);
          TILE((k + 2) % TD);
          if (uu + 1 >= nw) {
            if (!more && wave == 0) STAMP(2);
            FLUSH(uu + 1 - nw, uu + 1 == nw ? sp : 7 + (int)cur, !more);
            cur = nxt;
          }
        }
      }
    }
#undef TILE
#undef ISSUE
    return;
  }

  const int v = wave - 4, t = tid - 256;
  const int r8 = lane >> 3, c8 = lane & 7;
  unsigned tag = 0;
  if (is_qc) {
    const unsigned e_in = __hip_atomic_load((gu32*)(p.ws + WS_EPOCH), __ATOMIC_RELAXED, __HIP_MEMORY_SCOPE_AGENT);
    if (sp == 0 && v == 0 && lane == 0) __hip_atomic_store((gu32*)(p.ws + WS_TK + head * 256), 0u, __ATOMIC_RELAXED, __HIP_MEMORY_SCOPE_AGENT);
    v4u w[20];
    {
      const bf16_t* src = p.wq + (size_t)(160 * v + 2 * r8) * (HQ * DH) + head * DH + 64 * sp + 8 * c8;
#pragma unroll
      for (int i = 0; i < 10; ++i) { w[2 * i] = ldnt(src + (size_t)(16 * i) * (HQ * DH)); w[2 * i + 1] = ldnt(src + (size_t)(16 * i + 1) * (HQ * DH)); }
    }
    unsigned xs[5];
#pragma unroll
    for (int i = 0; i < 5; ++i) xs[i] = ((const GAS unsigned*)p.x)[lane + 64 * i];
    const unsigned xo0 = ((const GAS unsigned*)p.x)[80 * v + lane], xo1 = ((const GAS unsigned*)p.x)[80 * v + 64 + (lane & 15)];
    __builtin_amdgcn_sched_barrier(0);
    if (v == 0 && lane == 0) { LV[L_E] = e_in; WAIT_LGKM(); LV[L_FLAG_E] = 1u; }
    float n0a, n0b, n1a, n1b;
    {
      float s = 0.f;
#pragma unroll
      for (int i = 0; i < 5; ++i) s += bflo(xs[i]) + bfhi(xs[i]);
      const float mean_f = wave_sum(s) * (1.f / D);
      float vs = 0.f;
#pragma unroll
      for (int i = 0; i < 5; ++i) { const float d0 = bflo(xs[i]) - mean_f, d1 = bfhi(xs[i]) - mean_f; vs = fmaf(d0, d0, vs); vs = fmaf(d1, d1, vs); }
      const float var = rbf(wave_sum(vs) * (1.f / D)), mean = rbf(mean_f);
      const float rs = __builtin_amdgcn_rcpf(rbf(__builtin_amdgcn_sqrtf(rbf(var + 1e-5f))));
      n0a = rbf(rbf(bflo(xo0) - mean) * rs); n0b = rbf(rbf(bfhi(xo0) - mean) * rs);
      n1a = rbf(rbf(bflo(xo1) - mean) * rs); n1b = rbf(rbf(bfhi(xo1) - mean) * rs);
      asm volatile("" : "+v"(n0a), "+v"(n0b), "+v"(n1a), "+v"(n1b));
    }
    unsigned wi[10][8];
#pragma unroll
    for (int i = 0; i < 10; ++i) { interleave(w[2 * i], w[2 * i + 1], wi[i]); PIN8(wi[i]); }
    if (lane == 0) __hip_atomic_fetch_add((LAS unsigned*)(L + L_CNT_Q), 1u, __ATOMIC_RELAXED, __HIP_MEMORY_SCOPE_WORKGROUP);
    lds_wait(LV + L_FLAG_E, 1u);
    tag = LV[L_E] + 1u;
    if (v == 0) STAMP(18);
    unsigned s0, c0, s1, c1;
    {
      const gu64* m0 = MG + 80 * v + lane; const gu64* m1 = MG + 80 * v + 64 + (lane & 15);
      for (unsigned n = 0; n < SPIN_G; ++n) {
        const u64 a0 = ldg(m0), b0 = ldg(m0 + 320), a1 = ldg(m1), b1 = ldg(m1 + 320);
        s0 = (unsigned)(a0 >> 32); c0 = (unsigned)(b0 >> 32); s1 = (unsigned)(a1 >> 32); c1 = (unsigned)(b1 >> 32);
        if (all_ok(ok_pair(a0, tag) & ok_pair(b0, tag) & ok_pair(a1, tag) & ok_pair(b1, tag))) break;
      }
    }
    if (v == 0) STAMP(5);
    L[L_H2 + 80 * v + lane] = f2bf(rbf(n0a * rbf(1.f + bflo(c0))) + bflo(s0)) | (f2bf(rbf(n0b * rbf(1.f + bfhi(c0))) + bfhi(s0)) << 16);
    if (lane < 16) L[L_H2 + 80 * v + 64 + lane] = f2bf(rbf(n1a * rbf(1.f + bflo(c1))) + bflo(s1)) | (f2bf(rbf(n1b * rbf(1.f + bfhi(c1))) + bfhi(s1)) << 16);
    WAIT_LGKM();
    float acc[8];
#pragma unroll
    for (int j = 0; j < 8; ++j) acc[j] = 0.f;
#pragma unroll
    for (int i = 0; i < 10; ++i) {
      const unsigned hp = L[L_H2 + 80 * v + 8 * i + r8];
#pragma unroll
      for (int j = 0; j < 8; ++j) acc[j] = dot2(wi[i][j], hp, acc[j]);
    }
    LF[L_MR + 64 * v + 8 * c8 + r8] = reduce_oct(acc, lane);
    asm volatile("s_waitcnt vmcnt(0) lgkmcnt(0)" ::: "memory");
    unsigned old = 0;
    if (lane == 0) old = __hip_atomic_fetch_add((LAS unsigned*)(L + L_CNT_M), 1u, __ATOMIC_RELAXED, __HIP_MEMORY_SCOPE_WORKGROUP);
    if (__builtin_amdgcn_readfirstlane(old) == 3u) {
      asm volatile("" ::: "memory");
      const unsigned qb = f2bf((LF[L_MR + lane] + LF[L_MR + 64 + lane]) + (LF[L_MR + 128 + lane] + LF[L_MR + 192 + lane]));
      const unsigned up = dppu<XOR1>(qb);
      if ((lane & 1) == 0) stg(QG + head * 64 + 32 * sp + (lane >> 1), gr_pair(tag, qb | (up << 16)));
      if (lane == 0) LV[L_FLAG_H] = 1u;
      STAMP(6);
    }
  } else {
    const unsigned e_in = __hip_atomic_load((gu32*)(p.ws + WS_EPOCH), __ATOMIC_RELAXED, __HIP_MEMORY_SCOPE_AGENT);
    const __amdgpu_buffer_rsrc_t rm = __builtin_amdgcn_make_buffer_rsrc((void*)p.wmod, 0, is_mod ? D * NMOD * 2 : 0, 0x00020000);
    const __amdgpu_buffer_rsrc_t rc = __builtin_amdgcn_make_buffer_rsrc((void*)p.c, 0, is_mod ? D * 2 : 0, 0x00020000);
    const unsigned cv0 = __builtin_amdgcn_raw_buffer_load_b16(rc, (160 * v + lane) * 2, 0, 0), cv1 = __builtin_amdgcn_raw_buffer_load_b16(rc, (160 * v + 64 + lane) * 2, 0, 0),
                   cv2 = __builtin_amdgcn_raw_buffer_load_b16(rc, (160 * v + 128 + (lane & 31)) * 2, 0, 0);
    v4u wm[20];
#pragma unroll
    for (int i = 0; i < 20; ++i) wm[i] = __builtin_amdgcn_raw_buffer_load_b128(rm, ((160 * v + r8) * NMOD + 64 * mu + 8 * c8) * 2, i * (8 * NMOD * 2), 2);
    __builtin_amdgcn_sched_barrier(0);
    if (v == 0 && lane == 0) { LV[L_E] = e_in; WAIT_LGKM(); LV[L_FLAG_E] = 1u; }
    if (is_mod) {
      L[L_SC + 160 * v + lane] = f2bf(silu(bf2f(cv0))); L[L_SC + 160 * v + 64 + lane] = f2bf(silu(bf2f(cv1)));
      if (lane < 32) L[L_SC + 160 * v + 128 + lane] = f2bf(silu(bf2f(cv2)));
      WAIT_LGKM();
      float acc[8];
#pragma unroll
      for (int j = 0; j < 8; ++j) acc[j] = 0.f;
#pragma unroll
      for (int i = 0; i < 20; ++i) {
        const unsigned a = L[L_SC + 160 * v + 8 * i + r8], bb = a << 16;
#pragma unroll
        for (int j = 0; j < 4; ++j) { acc[2 * j] = dot2(wm[i][j], a, acc[2 * j]); acc[2 * j + 1] = dot2(wm[i][j], bb, acc[2 * j + 1]); }
      }
      LF[L_MR + 64 * v + 8 * c8 + r8] = reduce_oct(acc, lane);
      WAIT_LGKM();
      unsigned old = 0;
      if (lane == 0) old = __hip_atomic_fetch_add((LAS unsigned*)(L + L_CNT_M), 1u, __ATOMIC_RELAXED, __HIP_MEMORY_SCOPE_WORKGROUP);
      if (__builtin_amdgcn_readfirstlane(old) == 3u) {
        asm volatile("" ::: "memory");
        const unsigned mb_ = f2bf((LF[L_MR + lane] + LF[L_MR + 64 + lane]) + (LF[L_MR + 128 + lane] + LF[L_MR + 192 + lane]));
        const unsigned up = dppu<XOR1>(mb_);
        lds_wait(LV + L_FLAG_E, 1u);
        if ((lane & 1) == 0) stg(MG + 32 * mu + (lane >> 1), gr_pair(LV[L_E] + 1u, mb_ | (up << 16)));
        if (lane == 0) LV[L_CNT_G] = 1u;
        STAMP(4);
      }
    }
  }
  if (v == 3) {
    const v4u cw = ((const GAS v4u*)(code_base + (u64)(J * 1024)))[lane];
    const v4u cx = ((const GAS v4u*)(code_base + (u64)((J + 32) * 1024 < CODE_BYTES ? (J + 32) * 1024 : 0)))[lane];
    asm volatile("" :: "v"(cw), "v"(cx));
  }
  if (v == 2) {
      const int k = lane & 15;
      const GAS unsigned* a = k == 0 ? (const GAS unsigned*)(p.wo + (size_t)(head * DH) * D + 80 * sp)
                            : k == 1 ? (const GAS unsigned*)(p.wo + (size_t)(head * DH + DH - 1) * D + 80 * sp)
                            : k == 2 ? (const GAS unsigned*)p.out
                            : k == 3 ? (const GAS unsigned*)(p.ws + WS_PG + (size_t)(head * NREC) * PG_STRIDE * 8)
                            : k == 4 ? (const GAS unsigned*)(p.ws + WS_YG + (size_t)(5 * sp) * 4096)
                            : k == 5 ? (const GAS unsigned*)(p.ws + WS_DG)
                            : k == 6 ? (const GAS unsigned*)(p.ws + WS_DG + 131072)
                            : k == 7 ? (const GAS unsigned*)(p.ws + (lane & 16 ? WS_TK + head * 256 : WS_XG))
                            : k == 8 ? (const GAS unsigned*)(p.kc + ((size_t)head * SK + (size_t)NSTAT * 16) * DH)
                            : k == 9 ? (const GAS unsigned*)(p.vc + ((size_t)head * SK + (size_t)NSTAT * 16) * DH)
                            : k == 10 ? (const GAS unsigned*)(p.wo + (size_t)(head * DH + 32) * D + 80 * sp)
                            : k == 11 ? (const GAS unsigned*)(p.wo + (size_t)(head * DH + 64) * D + 80 * sp)
                            : k == 12 ? (const GAS unsigned*)(p.wo + (size_t)(head * DH + 96) * D + 80 * sp)
                            : k == 13 ? (const GAS unsigned*)(p.ws + WS_DG + 65536)
                            : k == 14 ? (const GAS unsigned*)(p.ws + WS_YG + (size_t)(5 * sp + 4) * 4096 + 4088)
                            : (const GAS unsigned*)(p.ws + WS_PG + (size_t)(head * NREC + NREC) * PG_STRIDE * 8 - 8);
      const unsigned w_ = *a; asm volatile("" :: "v"(w_));
      const int st_ = min((lane & 31) * TLBSTEP, nt - 1);
      const bf16_t* kv = lane < 32 ? p.kc : p.vc;
      const unsigned w2_ = *(const GAS unsigned*)(kv + ((size_t)head * SK + (size_t)(t0 + st_) * 16) * DH); asm volatile("" :: "v"(w2_));
  }
  if (v == 1) {
    lds_wait(LV + L_FLAG_E, 1u);
    tag = LV[L_E] + 1u;
    const gu64* qp = QG + head * 64 + lane;
    if (!is_qc) __builtin_amdgcn_s_sleep(QDELAY);
    u64 g0 = ldg(qp); __builtin_amdgcn_s_sleep(STAG);
    u64 g1 = ldg(qp); __builtin_amdgcn_s_sleep(STAG);
    u64 g2 = ldg(qp), g;
    for (unsigned n = 0;; ++n) {
      if (all_ok(ok_pair(g0, tag)) || n >= SPIN_G) { g = g0; break; }
      g0 = ldg(qp);
      if (all_ok(ok_pair(g1, tag))) { g = g1; break; }
      g1 = ldg(qp);
      if (all_ok(ok_pair(g2, tag))) { g = g2; break; }
      g2 = ldg(qp);
    }
    L[L_Q + lane] = (unsigned)(g >> 32);
    WAIT_LGKM();
    if (lane == 0) LV[L_FLAG_Q] = 1u;
    STAMP(7);
  }
  lds_wait(LV + L_FLAG_E, 1u);
  tag = LV[L_E] + 1u;

  const int g4 = lane >> 4, tq = (lane & 15) >> 2, tp = lane & 3;
  v4u wf[5][8];
  unsigned mbA[2], mbB[2];
  if (is_ffn) {
#define FPACE 63
    v4u raw[5][8];
    {
      const size_t ro = (size_t)(160 * v + r8) * F + 64 * head + 8 * c8;
#pragma unroll
      for (int kb = 0; kb < 5; ++kb) {
        if (FPACE < 63) asm volatile("s_waitcnt vmcnt(%0)" :: "n"(FPACE) : "memory");
#pragma unroll
        for (int i = 0; i < 4; ++i) { raw[kb][i] = ldnt(p.wg + ro + (size_t)(32 * kb + 8 * i) * F); raw[kb][4 + i] = ldnt(p.wu + ro + (size_t)(32 * kb + 8 * i) * F); }
        __builtin_amdgcn_sched_barrier(0);
      }
    }
    __builtin_amdgcn_sched_barrier(0);
    LAS unsigned char* stg_w = LB + B_STG + v * 4096 + r8 * 128 + c8 * 16;
    const LAS unsigned char* stg_r = LB + B_STG + v * 4096 + (8 * g4 + tq) * 128 + tp * 8;
#pragma unroll
    for (int kb = 0; kb < 5; ++kb)
#pragma unroll
      for (int m = 0; m < 2; ++m) {
#pragma unroll
        for (int i = 0; i < 4; ++i) *(LAS v4u*)(stg_w + i * 1024) = raw[kb][4 * m + i];
        asm volatile("" ::: "memory");
#pragma unroll
        for (int cg = 0; cg < 4; ++cg) wf[kb][4 * m + cg] = tr_frag(stg_r + cg * 32, 512);
        asm volatile("s_waitcnt lgkmcnt(0)" ::: "memory");
      }
    if (v == 0) STAMP(21);
    if (v == 1) STAMP(18);
    lds_wait(LV + L_CNT_OV, 4u);
    if (v == 0) STAMP(22);
#pragma unroll
    for (int i = 0; i < 20; ++i) {
      const int idx = 64 * i + lane, row = idx / 20, ch = idx - 20 * row;
      GLDS(p.wd + (size_t)(64 * head + row) * D + 160 * v + 8 * ch, LB + B_WD + v * 20480 + 1024 * i, 2);
    }
    {
      const gu64* m0 = MG + 80 * v + lane; const gu64* m1 = MG + 80 * v + 64 + (lane & 15);
      for (unsigned n = 0; n < SPIN_G; ++n) {
        const u64 s0 = ldg(m0 + 960), c0 = ldg(m0 + 1280), s1 = ldg(m1 + 960), c1 = ldg(m1 + 1280);
        const unsigned sp0 = (unsigned)(s0 >> 32), cp0 = (unsigned)(c0 >> 32), sp1 = (unsigned)(s1 >> 32), cp1 = (unsigned)(c1 >> 32);
        mbA[0] = (sp0 & 0xffffu) | (f2bf(1.f + bflo(cp0)) << 16); mbB[0] = (sp0 >> 16) | (f2bf(1.f + bfhi(cp0)) << 16);
        mbA[1] = (sp1 & 0xffffu) | (f2bf(1.f + bflo(cp1)) << 16); mbB[1] = (sp1 >> 16) | (f2bf(1.f + bfhi(cp1)) << 16);
        if (all_ok(ok_pair(s0, tag) & ok_pair(c0, tag) & ok_pair(s1, tag) & ok_pair(c1, tag))) break;
        __builtin_amdgcn_s_sleep(8);
      }
    }
  }
  float own_x = 0.f, own_ga = 0.f, own_gb = 0.f;
  if (is_own) {
    const int col = 64 * own + 16 * v + c;
    const gu64* m = MG + (lane < 32 ? 640 : 1600) + (col >> 1);
    own_x = bf2f(((const GAS bf16_t*)p.x)[col]);
    unsigned g = 0;
    for (unsigned n = SPIN_G; n != 0u; --n) { const u64 a = ldg(m); g = (unsigned)(a >> 32); if (none_bad(bad_pair(a, tag))) break; __builtin_amdgcn_s_sleep(8); }
    const float gv = (c & 1) ? bfhi(g) : bflo(g);
    const float ga_h = lane < 32 ? gv : 0.f, gb_h = lane < 32 ? 0.f : gv;
    own_ga = swap32_sum(ga_h, ga_h); own_gb = swap32_sum(gb_h, gb_h);
  }
  if (v == 2 && !is_ffn && !is_qc) {
    lds_wait(LV + L_FLAG_Q, 1u);
    for (int i = 0; i < MAXU; ++i) {
      for (unsigned n = 0; LV[L_REQ] <= (unsigned)i && n < SPIN_L; ++n) __builtin_amdgcn_s_sleep(1);
      unsigned tk = 0;
      if (lane == 0) tk = __hip_atomic_fetch_add((gu32*)(p.ws + WS_TK + head * 256), 1u, __ATOMIC_RELAXED, __HIP_MEMORY_SCOPE_AGENT);
      tk = __builtin_amdgcn_readfirstlane(tk);
      const bool got = tk < (unsigned)NUNIT;
      if (lane == 0) LV[L_TK + i] = got ? tk + 1u : 0x80u;
      if (!got) break;
    }
    if (lane == 0) LV[L_FLAG_L] = 1u;
  }
#define WO_REQUEST() do { _Pragma("unroll") for (int i = 0; i < 5; ++i) { \
    const int idx = 256 * i + t, row = idx / 10, ch = idx - 10 * row; \
    GLDS(p.wo + (size_t)(head * DH + row) * D + 80 * sp + 8 * ch, LB + B_WO + (256 * i + 64 * v) * 16, 0); } } while (0)
  if (v == 3 && !is_ffn && !is_qc) {
    bool wo_out = false;
    int i = 0;
    for (unsigned n = 0; n < SPIN_L; ++n) {
      if (!wo_out && LV[L_FLAG_L] != 0u) { WO_REQUEST(); wo_out = true; }
      const unsigned cnt = __builtin_amdgcn_readfirstlane(LV[L_CNT_F + i]);
      if (cnt >= 16u) break;
      if (cnt == 4u) {
        asm volatile("" ::: "memory");
        const LAS float* R = i == 0 ? LF + L_REC : (LAS float*)(LB + B_STG) + (i - 1) * 528;
        const int rec = i == 0 ? sp : 7 + (int)LV[L_TK + i - 1];
        PUBLISH_REC(R, rec);
        if (++i > MAXU) break;
      } else __builtin_amdgcn_s_sleep(1);
    }
    if (!wo_out) { lds_wait(LV + L_FLAG_L, 1u); WO_REQUEST(); }
  } else {
    lds_wait(LV + L_FLAG_L, 1u);
    WO_REQUEST();
  }
  asm volatile("s_waitcnt vmcnt(0)" ::: "memory");
  if (lane == 0) __hip_atomic_fetch_add((LAS unsigned*)(L + L_CNT_W), 1u, __ATOMIC_RELAXED, __HIP_MEMORY_SCOPE_WORKGROUP);
  lds_wait(LV + L_CNT_W, 4u);
  if (v == 0) STAMP(8);

  v4u wof[4], wof4[4];
#define WOF_READ() do { const LAS unsigned char* src_ = LB + B_WO + (8 * g4 + tq) * 160 + tp * 8; \
    _Pragma("unroll") for (int kb = 0; kb < 4; ++kb) { wof[kb] = tr_frag(src_ + kb * 32 * 160 + v * 32, 4 * 160); wof4[kb] = tr_frag(src_ + kb * 32 * 160 + 4 * 32, 4 * 160); } } while (0)
  constexpr int NJ = NREC / 2, NJ2 = NJ - 8;
  const int hf = lane >> 5;
  const gu64* hop = PG + (size_t)(head * NREC + hf) * PG_STRIDE + 32 * v + (lane & 31);
  const gu64* hmp = PG + (size_t)(head * NREC + (lane & 14) + hf) * PG_STRIDE + 128 + (lane & 1);
  const bool two = (lane & 15) < 2 * NJ2;
  const gu64* hmq = PG + (size_t)(head * NREC + (two ? 16 + (lane & 14) : 0) + hf) * PG_STRIDE + 128 + (lane & 1);
  gu64* ysp = YG + (size_t)(5 * sp + (lane < 16 ? v : 4)) * 512 + head * 16 + c;
  LAS bf16_t* odst = (LAS bf16_t*)(L + L_O) + 32 * v + (lane & 31);
  PINP(hop); PINP(hmp); PINP(hmq); PINP(ysp);
  lds_wait(LV + L_FLAG_S1, 1u);
  if (v == 0) STAMP(9);
  {
    u64 g[NJ], gm, gq = 0;
    for (unsigned n = SPIN_G; n != 0u; --n) {
      unsigned bad = 0;
#pragma unroll
      for (int j = 0; j < NJ; ++j) { g[j] = ldg(hop + 2 * j * PG_STRIDE); bad |= bad_f24(g[j], tag); }
      gm = ldg(hmp); bad |= bad_f24(gm, tag);
      if (NJ2 > 0) { gq = ldg(hmq); bad |= bad_f24(gq, tag); }
      if (none_bad(bad)) break;
      if (PSLEEP) __builtin_amdgcn_s_sleep(PSLEEP);
    }
    __builtin_amdgcn_sched_barrier(0);
    WOF_READ();
    const float ml = val_f24(gm), mq = val_f24(gq);
    const float mm = (lane & 1) ? -INFINITY : ml, mn = ((lane & 1) || !two || NJ2 == 0) ? -INFINITY : mq;
    float M = NJ2 > 0 ? fmaxf(mm, mn) : mm;
    M = MAX_DPP(M, "quad_perm:[2,3,0,1]"); M = MAX_DPP(M, "row_half_mirror"); M = MAX_DPP(M, "row_ror:8"); M = MAX_DPP(M, "quad_perm:[1,0,3,2]");
    { auto t_ = __builtin_amdgcn_permlane32_swap(__float_as_uint(M), __float_as_uint(M), false, false); M = fmaxf(__uint_as_float(t_[0]), __uint_as_float(t_[1])); }
    const float wgt = __builtin_amdgcn_exp2f((mm - M) * LOG2E), wgu = NJ2 > 0 ? __builtin_amdgcn_exp2f((mn - M) * LOG2E) : 0.f;
    const float wn = dpp<XOR1>(wgt), wo_ = dpp<XOR1>(wgu);
    const float wl = (lane & 1) ? wn * ml + (NJ2 > 0 && two ? wo_ * mq : 0.f) : 0.f;
    float Ls = wl + dpp<XOR1>(wl); Ls += dpp<XOR2>(Ls); Ls += dpp<XOR7>(Ls); Ls += dpp<XOR8>(Ls);
    Ls = swap32_sum(Ls, Ls);
    float acc = dpp<BCAST0>(wgt) * val_f24(g[0]);
    acc = fmaf(dpp<BCAST0 + 2>(wgt), val_f24(g[1]), acc); acc = fmaf(dpp<BCAST0 + 4>(wgt), val_f24(g[2]), acc); acc = fmaf(dpp<BCAST0 + 6>(wgt), val_f24(g[3]), acc);
    acc = fmaf(dpp<BCAST0 + 8>(wgt), val_f24(g[4]), acc); acc = fmaf(dpp<BCAST0 + 10>(wgt), val_f24(g[5]), acc);
    acc = fmaf(dpp<BCAST0 + 12>(wgt), val_f24(g[6]), acc); acc = fmaf(dpp<BCAST0 + 14>(wgt), val_f24(g[7]), acc);
    if (NJ2 > 0) acc = fmaf(dpp<BCAST0>(wgu), val_f24(g[NJ > 8 ? 8 : 0]), acc);
    if (NJ2 > 1) acc = fmaf(dpp<BCAST0 + 2>(wgu), val_f24(g[NJ > 9 ? 9 : 0]), acc);
    if (NJ2 > 2) acc = fmaf(dpp<BCAST0 + 4>(wgu), val_f24(g[NJ > 10 ? 10 : 0]), acc);
    if (NJ2 > 3) acc = fmaf(dpp<BCAST0 + 6>(wgu), val_f24(g[NJ > 11 ? 11 : 0]), acc);
    acc = swap32_sum(acc, acc);
    *odst = (bf16_t)f2bf(acc * __builtin_amdgcn_rcpf(Ls));
  }
  BARRIER();
  if (v == 0) STAMP(10);
  {
    v4u A[4];
#pragma unroll
    for (int kb = 0; kb < 4; ++kb) A[kb] = *(const LAS v4u*)(L + L_O + 16 * kb + 4 * g4);
    f32x4 y = {0.f, 0.f, 0.f, 0.f}, y4 = {0.f, 0.f, 0.f, 0.f};
#pragma unroll
    for (int kb = 0; kb < 4; ++kb) y = mfma(A[kb], wof[kb], y);
    if (v == 0) {
#pragma unroll
      for (int kb = 0; kb < 4; ++kb) y4 = mfma(A[kb], wof4[kb], y4);
    }
    if (lane < 16 || (v == 0 && lane < 32)) stg(ysp, gr_f24(tag, lane < 16 ? y[0] : y4[0]));
  }
  if (v == 0) STAMP(11);
  if (is_own) {
    const int og = 4 * own + v;
    const gu64* yp = YG + (size_t)og * 512 + lane;
    const gu64* dp = DG + (size_t)og * 512 + lane;
    const gu64* dp6 = dp + (lane < 48 ? 384 : 0);
    gu64* xp = XG + 16 * og + (c >> 1);
    GAS bf16_t* op = (GAS bf16_t*)p.out + 16 * og + c;
    PINP(yp); PINP(dp); PINP(dp6); PINP(xp); PINP(op);
    u64 g[8];
    for (unsigned n = SPIN_G; n != 0u; --n) {
      unsigned bad = 0;
#pragma unroll
      for (int i = 0; i < 8; ++i) { g[i] = ldg(yp + 64 * i); bad |= bad_f24(g[i], tag); }
      if (none_bad(bad)) break;
      if (PSLEEP) __builtin_amdgcn_s_sleep(PSLEEP);
    }
    if (v == 0) STAMP(30);
    float s = ((val_f24(g[0]) + val_f24(g[1])) + (val_f24(g[2]) + val_f24(g[3]))) + ((val_f24(g[4]) + val_f24(g[5])) + (val_f24(g[6]) + val_f24(g[7])));
    s = xrow_sum(s);
    const float x1 = rbf(own_x + rbf(own_ga * rbf(s)));
    const unsigned xb = f2bf(x1), pair = xb | (dppu<XOR1>(xb) << 16);
    if (lane < 16 && (lane & 1) == 0) stg(xp, gr_pair(tag, pair));
    if (v == 0) STAMP(12);
    for (unsigned n = SPIN_G; n != 0u; --n) {
      unsigned bad = 0;
#pragma unroll
      for (int i = 0; i < 6; ++i) { g[i] = ldg(dp + 64 * i); bad |= bad_f24(g[i], tag); }
      g[6] = ldg(dp6); bad |= bad_f24(g[6], tag);
      if (none_bad(bad)) break;
      if (PSLEEP) __builtin_amdgcn_s_sleep(PSLEEP);
    }
    if (v == 0) STAMP(15);
    s = ((val_f24(g[0]) + val_f24(g[1])) + (val_f24(g[2]) + val_f24(g[3]))) + ((val_f24(g[4]) + val_f24(g[5])) + (lane < 48 ? val_f24(g[6]) : 0.f));
    s = xrow_sum(s);
    if (lane < 16) __hip_atomic_store(op, (bf16_t)f2bf(x1 + rbf(own_gb * rbf(s))), __ATOMIC_RELAXED, __HIP_MEMORY_SCOPE_AGENT);
    if (og == 0 && lane == 0) __hip_atomic_store((gu32*)(p.ws + WS_EPOCH), tag, __ATOMIC_RELAXED, __HIP_MEMORY_SCOPE_AGENT);
    if (v == 0) STAMP(16);
  }
  if (is_ffn) {
    const int ua = 80 * v + lane, ub = 80 * v + 64 + (lane & 15);
    const gu64* xa = XG + (ua >> 3) * 16 + (ua & 7); const gu64* xb = XG + (ub >> 3) * 16 + (ub & 7);
    gu64* dq = DG + (size_t)(10 * v + g4) * 512 + head * 16 + c;
    PINP(xa); PINP(xb); PINP(dq);
    unsigned xo0, xo1;
    for (unsigned n = SPIN_G; n != 0u; --n) {
      const u64 g0 = ldg(xa), g1 = ldg(xb);
      xo0 = (unsigned)(g0 >> 32); xo1 = (unsigned)(g1 >> 32);
      if (none_bad(bad_pair(g0, tag) | bad_pair(g1, tag))) break;
      if (PSLEEP) __builtin_amdgcn_s_sleep(PSLEEP);
    }
    if (v == 0) STAMP(13);
    {
      const unsigned xm = lane < 16 ? xo1 : 0u;
      float s1 = dot2(xm, 0x3f803f80u, dot2(xo0, 0x3f803f80u, 0.f)), s2 = dot2(xm, xm, dot2(xo0, xo0, 0.f));
      s1 += dpp<XOR1>(s1); s2 += dpp<XOR1>(s2); s1 += dpp<XOR2>(s1); s2 += dpp<XOR2>(s2);
      s1 += dpp<XOR7>(s1); s2 += dpp<XOR7>(s2); s1 += dpp<XOR8>(s1); s2 += dpp<XOR8>(s2);
      s1 = xrow_sum(s1); s2 = xrow_sum(s2);
      if (lane == 0) *(LAS f32x2*)(LF + L_ST + 2 * v) = f32x2{s1, s2};
    }
    if (v == 0) STAMP(25);
    BARRIER();
    if (v == 0) STAMP(26);
    {
      const f32x4 sa = *(const LAS f32x4*)(LF + L_ST), sb = *(const LAS f32x4*)(LF + L_ST + 4);
      const float mean_f = ((sa[0] + sa[2]) + (sb[0] + sb[2])) * (1.f / D);
      const float ex2 = ((sa[1] + sa[3]) + (sb[1] + sb[3])) * (1.f / D);
      float var_f = fmaf(-mean_f, mean_f, ex2);
      if (__builtin_amdgcn_ballot_w64(var_f * 256.f < ex2) != 0ull) {
        float vs = 0.f;
#pragma unroll
        for (int i = 0; i < 5; ++i) { const int u_ = lane + 64 * i; const unsigned xp = (unsigned)(ldg(XG + (u_ >> 3) * 16 + (u_ & 7)) >> 32); const float d0 = bflo(xp) - mean_f, d1 = bfhi(xp) - mean_f; vs = fmaf(d0, d0, vs); vs = fmaf(d1, d1, vs); }
        var_f = wave_sum(vs) * (1.f / D);
      }
      const float var = rbf(var_f), mean = rbf(mean_f);
      const float rs = __builtin_amdgcn_rcpf(rbf(__builtin_amdgcn_sqrtf(rbf(var + 1e-5f))));
#define LNPAIR(xp, ma, mb_) ({ const unsigned d_ = pk2(bflo(xp) - mean, bfhi(xp) - mean); const unsigned n_ = pk2(bflo(d_) * rs, bfhi(d_) * rs); \
        const unsigned m_ = pk2(bflo(n_) * bfhi(ma), bfhi(n_) * bfhi(mb_)); pk2(bflo(m_) + bflo(ma), bfhi(m_) + bflo(mb_)); })
      L[L_H2 + 80 * v + lane] = LNPAIR(xo0, mbA[0], mbB[0]);
      const unsigned h1 = LNPAIR(xo1, mbA[1], mbB[1]);
      if (lane < 16) L[L_H2 + 80 * v + 64 + lane] = h1;
#undef LNPAIR
    }
    if (v == 0) STAMP(27);
    v4u wd[2][10];
    {
      v4u A[5];
#pragma unroll
      for (int kb = 0; kb < 5; ++kb) A[kb] = *(const LAS v4u*)(L + L_H2 + 80 * v + 16 * kb + 4 * g4);
      f32x4 acc[8];
#pragma unroll
      for (int cg = 0; cg < 8; ++cg) acc[cg] = f32x4{0.f, 0.f, 0.f, 0.f};
      const LAS unsigned char* src = LB + B_WD + v * 20480 + (8 * g4 + tq) * 320 + tp * 8;
#pragma unroll
      for (int kb = 0; kb < 5; ++kb) {
#pragma unroll
        for (int cg = 0; cg < 8; ++cg) acc[cg] = mfma(A[kb], wf[kb][cg], acc[cg]);
        __builtin_amdgcn_sched_barrier(0);
        if (kb < 4) {
#pragma unroll
          for (int j = 0; j < 5; ++j) { const int f = 5 * kb + j; wd[f / 10][f % 10] = tr_frag(src + (f / 10) * 32 * 320 + (f % 10) * 32, 4 * 320); }
        }
        __builtin_amdgcn_sched_barrier(0);
      }
      LF[L_RGU + v * 128 + lane] = sel4(g4, acc[0][0], acc[1][0], acc[2][0], acc[3][0]);
      LF[L_RGU + v * 128 + 64 + lane] = sel4(g4, acc[4][0], acc[5][0], acc[6][0], acc[7][0]);
    }
    if (v == 0) STAMP(28);
    BARRIER();
    if (v == 0) STAMP(29);
    {
      const float G = rbf((LF[L_RGU + lane] + LF[L_RGU + 128 + lane]) + (LF[L_RGU + 256 + lane] + LF[L_RGU + 384 + lane]));
      const float U = rbf((LF[L_RGU + 64 + lane] + LF[L_RGU + 192 + lane]) + (LF[L_RGU + 320 + lane] + LF[L_RGU + 448 + lane]));
      ((LAS bf16_t*)(L + L_ACT + 32 * v))[lane] = (bf16_t)f2bf(rbf(silu(G)) * U);
      if (v == 0) STAMP(14);
      const v4u A0 = *(const LAS v4u*)(L + L_ACT + 32 * v + 4 * g4), A1 = *(const LAS v4u*)(L + L_ACT + 32 * v + 16 + 4 * g4);
      f32x4 acc[10];
#pragma unroll
      for (int cg = 0; cg < 10; ++cg) { acc[cg] = mfma(A0, wd[0][cg], f32x4{0.f, 0.f, 0.f, 0.f}); acc[cg] = mfma(A1, wd[1][cg], acc[cg]); }
      stg(dq, gr_f24(tag, sel4(g4, acc[0][0], acc[1][0], acc[2][0], acc[3][0])));
      stg(dq + 4 * 512, gr_f24(tag, sel4(g4, acc[4][0], acc[5][0], acc[6][0], acc[7][0])));
      if (lane < 32) stg(dq + 8 * 512, gr_f24(tag, g4 == 0 ? acc[8][0] : acc[9][0]));
    }
    if (v == 0) STAMP(17);
  }
  if (v == 0) STAMP_DUMP();
}

extern "C" void kernel_launch(void* const* d_in, const int* in_sizes, int n_in, void* d_out, int out_size, void* d_ws, size_t ws_size, hipStream_t stream) {
  Params p;
  p.x = (const bf16_t*)d_in[0]; p.kc = (const bf16_t*)d_in[1]; p.vc = (const bf16_t*)d_in[2]; p.wq = (const bf16_t*)d_in[3]; p.wo = (const bf16_t*)d_in[4];
  p.wg = (const bf16_t*)d_in[6]; p.wu = (const bf16_t*)d_in[7]; p.wd = (const bf16_t*)d_in[8]; p.c = (const bf16_t*)d_in[9]; p.wmod = (const bf16_t*)d_in[10];
  p.out = (bf16_t*)d_out; p.ws = (unsigned char*)d_ws;
  (void)hipFuncSetAttribute((const void*)adaln_block_kernel, hipFuncAttributeMaxDynamicSharedMemorySize, LDS_BYTES);
  adaln_block_kernel<<<NCU, NTHR, LDS_BYTES, stream>>>(p);
}
```

```cpp
#include <hip/hip_runtime.h>
#include <hip/hip_bf16.h>
#include <stdint.h>

#define TD 6
#define PACE 0
#define PACE0 0
#define KVDELAY 0
#define PSLEEP 0
#define TLBWARM 2
#define TLBSTEP 4
#define MAP1 1
#define RLEAD 2
#define WOLEAD 1
#define GATE 1
#define STAG 12
#define QDELAY 64
#define STAMPS 0
constexpr int NSTAT = 480, NUNIT = 8, MAXU = 4;
#define NT_Q 44
#define NT_M 64
#define NT_F 48
#define NT_P ((NSTAT - 2 * NT_Q - 2 * NT_M - NT_F) / 3)
static_assert(2 * NT_Q + 2 * NT_M + NT_F + 3 * NT_P == NSTAT, "tiles of a head");
#define NT_G0 44
#define NT_G1 62
#define NT_G2 ((NSTAT - 2 * NT_G0 - 2 * NT_G1) / 4)
static_assert(2 * NT_G0 + 2 * NT_G1 + 4 * NT_G2 == NSTAT, "tiles of a head");
#define NT_H0 45
#define NT_H1 ((NSTAT - 2 * NT_H0) / 6)
static_assert(2 * NT_H0 + 6 * NT_H1 == NSTAT, "tiles of a head");
static_assert(NT_F >= 40 && NT_F <= 48 && NT_Q >= 40 && NT_Q <= 48 && NT_G0 >= 40 && NT_G0 <= 48 && NT_H0 >= 40 && NT_H0 <= 48 && NT_P >= 52 && NT_M >= 52 && NT_G1 >= 52 && NT_G2 >= 52 && NT_H1 >= 52,
              "a stream wave takes 10..12 tiles where the CU draws no units (q and FFN CUs), at least 13 where it does (the catch-up schedule)");

typedef unsigned short bf16_t;
typedef unsigned long long u64;
typedef unsigned v4u __attribute__((ext_vector_type(4)));
typedef float f32x4 __attribute__((ext_vector_type(4)));
typedef float f32x2 __attribute__((ext_vector_type(2)));
typedef unsigned v2u __attribute__((ext_vector_type(2)));
typedef __bf16 bf16x2 __attribute__((ext_vector_type(2)));
typedef __bf16 bf16x8 __attribute__((ext_vector_type(8)));
typedef short v4s __attribute__((ext_vector_type(4)));
#define GAS __attribute__((address_space(1)))
#define LAS __attribute__((address_space(3)))
typedef GAS u64 gu64;
typedef GAS unsigned gu32;

constexpr int D = 640, HQ = 32, SK = 8192, DH = 128, F = 1728, NMOD = 6 * D;
constexpr int NCU = 256, NTHR = 512, NFFN = 27;
constexpr float LOG2E = 1.4426950408889634f;

constexpr size_t WS_EPOCH = 0;
constexpr size_t WS_XG = 393216 + 32768;
constexpr size_t WS_MG = 8192;
constexpr size_t WS_QG = 32768;
constexpr size_t WS_YG = 65536;
constexpr size_t WS_DG = 262144;
constexpr size_t WS_PG = 524288;
constexpr size_t WS_TK = 1310720;
constexpr int PG_STRIDE = 160;
constexpr size_t WS_STAMP = 64u << 20;

constexpr int L_FLAG_Q = 0, L_CNT_S1 = 1, L_FLAG_S1 = 2, L_FLAG_E = 3, L_E = 4, L_CNT_M = 5, L_FLAG_H = 6, L_CNT_G = 7, L_CNT_W = 8, L_FLAG_L = 9, L_CNT_OV = 10, L_CNT_Q = 11, L_REQ = 12, L_TK = 16, L_CNT_F = 24;
constexpr int L_Q = 32;
constexpr int L_REC = 96;
constexpr int L_O = 624;
constexpr int L_MR = 704;
constexpr int L_SC = 960;
constexpr int L_H2 = 1600;
constexpr int L_ST = 1920;
constexpr int L_RGU = 1936;
constexpr int L_ACT = 2448;
constexpr int L_STAMP = 2816;
constexpr int B_STG = 12288;
constexpr int B_WO = 28672;
constexpr int B_WD = 49152;
constexpr int B_OV = 28672;
constexpr int LDS_BYTES = 163840;
static_assert(B_OV + 4 * 32768 <= LDS_BYTES && B_WD + 81920 <= LDS_BYTES, "lds");

constexpr int CODE_BYTES = 43 * 1024;
constexpr unsigned SPIN_G = 1u << 15;
constexpr unsigned SPIN_L = 1u << 20;

struct Params {
  const bf16_t* x; const bf16_t* kc; const bf16_t* vc; const bf16_t* wq; const bf16_t* wo;
  const bf16_t* wg; const bf16_t* wu; const bf16_t* wd; const bf16_t* c; const bf16_t* wmod;
  bf16_t* out; unsigned char* ws;
};

__device__ __forceinline__ float bf2f(unsigned b) { return __uint_as_float(b << 16); }
__device__ __forceinline__ float bfhi(unsigned b) { return __uint_as_float(b & 0xffff0000u); }
__device__ __forceinline__ float bflo(unsigned b) { return __uint_as_float(b << 16); }
__device__ __forceinline__ unsigned f2bf(float f) { __bf16 b = (__bf16)f; return (unsigned)__builtin_bit_cast(unsigned short, b); }
__device__ __forceinline__ float rbf(float f) { return bf2f(f2bf(f)); }
__device__ __forceinline__ unsigned pk2(float lo, float hi) { return __builtin_bit_cast(unsigned, __builtin_convertvector(f32x2{lo, hi}, bf16x2)); }
__device__ __forceinline__ float silu(float x) { return x * __builtin_amdgcn_rcpf(1.f + __builtin_amdgcn_exp2f(-x * LOG2E)); }
__device__ __forceinline__ float dot2(unsigned a, unsigned b, float acc) {
  return __builtin_amdgcn_fdot2_f32_bf16(__builtin_bit_cast(bf16x2, a), __builtin_bit_cast(bf16x2, b), acc, false);
}
template <int CTRL> __device__ __forceinline__ float dpp(float x) {
  return __builtin_bit_cast(float, __builtin_amdgcn_mov_dpp(__builtin_bit_cast(int, x), CTRL, 0xf, 0xf, true));
}
template <int CTRL> __device__ __forceinline__ unsigned dppu(unsigned x) { return (unsigned)__builtin_amdgcn_mov_dpp((int)x, CTRL, 0xf, 0xf, true); }
#define MAX_DPP(x, ctrl) ({ float r_; asm("s_nop 1\n\tv_max_f32_dpp %0, %1, %1 " ctrl " row_mask:0xf bank_mask:0xf bound_ctrl:1" : "=v"(r_) : "v"(x)); r_; })
constexpr int XOR1 = 0xB1, XOR2 = 0x4E, XOR7 = 0x141, XOR8 = 0x128, ROR4 = 0x124, BCAST0 = 0x150;
__device__ __forceinline__ float swap32_sum(float a, float b) {
  auto t = __builtin_amdgcn_permlane32_swap(__float_as_uint(a), __float_as_uint(b), false, false);
  return __uint_as_float(t[0]) + __uint_as_float(t[1]);
}
__device__ __forceinline__ float swap16_sum(float a, float b) {
  auto t = __builtin_amdgcn_permlane16_swap(__float_as_uint(a), __float_as_uint(b), false, false);
  return __uint_as_float(t[0]) + __uint_as_float(t[1]);
}
__device__ __forceinline__ float xrow_max(float x) {
  auto s = __builtin_amdgcn_permlane16_swap(__float_as_uint(x), __float_as_uint(x), false, false);
  x = fmaxf(__uint_as_float(s[0]), __uint_as_float(s[1]));
  auto t = __builtin_amdgcn_permlane32_swap(__float_as_uint(x), __float_as_uint(x), false, false);
  return fmaxf(__uint_as_float(t[0]), __uint_as_float(t[1]));
}
__device__ __forceinline__ float xrow_sum(float x) { x = swap16_sum(x, x); return swap32_sum(x, x); }
__device__ __forceinline__ float wave_sum(float x) {
  x += dpp<XOR1>(x); x += dpp<XOR2>(x); x += dpp<XOR7>(x); x += dpp<XOR8>(x);
  return xrow_sum(x);
}
__device__ __forceinline__ void reduce_rows(const float* x, float& e0, float& e1) {
  const float y0 = swap32_sum(x[0], x[4]), y1 = swap32_sum(x[1], x[5]), y2 = swap32_sum(x[2], x[6]), y3 = swap32_sum(x[3], x[7]);
  e0 = swap16_sum(y0, y2); e1 = swap16_sum(y1, y3);
}
__device__ __forceinline__ float reduce_oct(const float* x, int lane) {
  float e0, e1; reduce_rows(x, e0, e1);
  const bool b3 = (lane & 8) != 0;
  return (b3 ? e1 : e0) + dpp<XOR8>(b3 ? e0 : e1);
}

__device__ __forceinline__ u64 ldg(const gu64* p) { return __hip_atomic_load(p, __ATOMIC_RELAXED, __HIP_MEMORY_SCOPE_AGENT); }
__device__ __forceinline__ void stg(gu64* p, u64 v) { __hip_atomic_store(p, v, __ATOMIC_RELAXED, __HIP_MEMORY_SCOPE_AGENT); }
__device__ __forceinline__ u64 gr_pair(unsigned tag, unsigned pay) { return ((u64)pay << 32) | tag; }
__device__ __forceinline__ u64 gr_f24(unsigned tag, float v) { return ((u64)((__float_as_uint(v) + 0x80u) & 0xffffff00u) << 32) | tag; }
__device__ __forceinline__ unsigned bad_pair(u64 g, unsigned tag) { return (unsigned)g ^ tag; }
__device__ __forceinline__ unsigned bad_f24(u64 g, unsigned tag) { return ((unsigned)g ^ tag) | ((unsigned)(g >> 32) & 0xffu); }
__device__ __forceinline__ float val_f24(u64 g) { return __uint_as_float((unsigned)(g >> 32)); }
__device__ __forceinline__ bool none_bad(unsigned bad) { return __builtin_amdgcn_ballot_w64(bad != 0u) == 0ull; }
__device__ __forceinline__ bool ok_pair(u64 g, unsigned tag) { return (unsigned)g == tag; }
__device__ __forceinline__ bool all_ok(bool ok) { return __builtin_amdgcn_ballot_w64(!ok) == 0ull; }
#define PINP(ptr) asm volatile("" : "+v"(ptr))

__device__ __forceinline__ void lds_wait(volatile LAS unsigned* w, unsigned want) {
#define LSLEEP 1
  for (unsigned n = 0; *w != want && n < SPIN_L; ++n) __builtin_amdgcn_s_sleep(LSLEEP);
  asm volatile("" ::: "memory");
}
__device__ __forceinline__ unsigned lds_wait_nz(volatile LAS unsigned* w) {
  unsigned x = *w;
  for (unsigned n = 0; x == 0u && n < SPIN_L; ++n) { __builtin_amdgcn_s_sleep(1); x = *w; }
  asm volatile("" ::: "memory");
  x = __builtin_amdgcn_readfirstlane(x);
  return x == 0u ? 0x80u : x;
}
#define WAIT_LGKM() asm volatile("s_waitcnt lgkmcnt(0)" ::: "memory")
#define BARRIER() do { asm volatile("s_waitcnt lgkmcnt(0)" ::: "memory"); __builtin_amdgcn_s_barrier(); asm volatile("" ::: "memory"); } while (0)

#define STAMP(i) do { } while (0)
#define STAMP_DUMP() do { } while (0)

struct AttnState { float m, l; float acc[8]; };
#define PUBLISH_REC(R_, rec_id) do { \
        const float m0_ = R_[0], m1_ = R_[132], m2_ = R_[264], m3_ = R_[396]; \
        const float MM_ = fmaxf(fmaxf(m0_, m1_), fmaxf(m2_, m3_)); \
        const float w0_ = __builtin_amdgcn_exp2f((m0_ - MM_) * LOG2E), w1_ = __builtin_amdgcn_exp2f((m1_ - MM_) * LOG2E); \
        const float w2_ = __builtin_amdgcn_exp2f((m2_ - MM_) * LOG2E), w3_ = __builtin_amdgcn_exp2f((m3_ - MM_) * LOG2E); \
        const float LL_ = (w0_ * R_[1] + w1_ * R_[133]) + (w2_ * R_[265] + w3_ * R_[397]); \
        const float oa_ = (w0_ * R_[4 + lane] + w1_ * R_[136 + lane]) + (w2_ * R_[268 + lane] + w3_ * R_[400 + lane]); \
        const float ob_ = (w0_ * R_[68 + lane] + w1_ * R_[200 + lane]) + (w2_ * R_[332 + lane] + w3_ * R_[464 + lane]); \
        gu64* rp_ = PG + (size_t)(head * 16 + (rec_id)) * PG_STRIDE; \
        stg(rp_ + lane, gr_f24(tag, oa_)); \
        stg(rp_ + 64 + lane, gr_f24(tag, ob_)); \
        if (lane < 2) stg(rp_ + 128 + lane, gr_f24(tag, lane == 0 ? MM_ : LL_)); \
      } while (0)

__device__ __forceinline__ void attn_tile(const v4u (&K)[4], const v4u (&V)[4], const v4u& q, AttnState& st, int c) {
  float x[4];
#pragma unroll
  for (int i = 0; i < 4; ++i) {
    float d = dot2(K[i].x, q.x, 0.f);
    d = dot2(K[i].y, q.y, d); d = dot2(K[i].z, q.z, d); d = dot2(K[i].w, q.w, d);
    x[i] = d;
  }
  const bool b3 = (c & 8) != 0, b2 = (c & 4) != 0;
  const float k0 = b3 ? x[2] : x[0], s0 = b3 ? x[0] : x[2];
  const float k1 = b3 ? x[3] : x[1], s1 = b3 ? x[1] : x[3];
  const float y0 = k0 + dpp<XOR8>(s0), y1 = k1 + dpp<XOR8>(s1);
  const float k2 = b2 ? y1 : y0, s2 = b2 ? y0 : y1;
  float z = k2 + dpp<XOR7>(s2);
  z += dpp<XOR2>(z); z += dpp<XOR1>(z);
  const float s = rbf(rbf(z) * 0.08838834764831845f);
  float pm = MAX_DPP(s, "row_ror:4"); pm = MAX_DPP(pm, "row_ror:8");
  const float mn = fmaxf(st.m, pm);
  if (__builtin_amdgcn_ballot_w64(mn > st.m) != 0ull) {
    const float al = __builtin_amdgcn_exp2f((st.m - mn) * LOG2E);
    st.l *= al;
#pragma unroll
    for (int j = 0; j < 8; ++j) st.acc[j] *= al;
    st.m = mn;
  }
  const float pr = __builtin_amdgcn_exp2f((s - st.m) * LOG2E);
  st.l += pr;
  const unsigned plo = f2bf(pr), phi = plo << 16;
#define PV(i) { const unsigned a = dppu<BCAST0 + 4 * i>(plo), b = dppu<BCAST0 + 4 * i>(phi); \
    st.acc[0] = dot2(V[i].x, a, st.acc[0]); st.acc[1] = dot2(V[i].x, b, st.acc[1]); st.acc[2] = dot2(V[i].y, a, st.acc[2]); st.acc[3] = dot2(V[i].y, b, st.acc[3]); \
    st.acc[4] = dot2(V[i].z, a, st.acc[4]); st.acc[5] = dot2(V[i].z, b, st.acc[5]); st.acc[6] = dot2(V[i].w, a, st.acc[6]); st.acc[7] = dot2(V[i].w, b, st.acc[7]); }
  PV(0) PV(1) PV(2) PV(3)
#undef PV
}

__device__ __forceinline__ void interleave(const v4u& r0, const v4u& r1, unsigned* o) {
#pragma unroll
  for (int j = 0; j < 4; ++j) { o[2 * j] = __builtin_amdgcn_perm(r1[j], r0[j], 0x05040100u); o[2 * j + 1] = __builtin_amdgcn_perm(r1[j], r0[j], 0x07060302u); }
}
__device__ __forceinline__ f32x4 mfma(const v4u& a, const v4u& b, const f32x4& c) {
  return __builtin_amdgcn_mfma_f32_16x16x32_bf16(__builtin_bit_cast(bf16x8, a), __builtin_bit_cast(bf16x8, b), c, 0, 0, 0);
}
__device__ __forceinline__ v4u tr_frag(const LAS unsigned char* p, int stride4) {
  const v2u lo = __builtin_bit_cast(v2u, __builtin_amdgcn_ds_read_tr16_b64_v4i16((LAS v4s*)p));
  const v2u hi = __builtin_bit_cast(v2u, __builtin_amdgcn_ds_read_tr16_b64_v4i16((LAS v4s*)(p + stride4)));
  return v4u{lo.x, lo.y, hi.x, hi.y};
}
#define GLDS(gsrc, ldst, aux) __builtin_amdgcn_global_load_lds((const GAS void*)(gsrc), (LAS void*)(ldst), 16, 0, aux)
__device__ __forceinline__ float sel4(int q4, float a, float b, float c, float d) { return q4 == 0 ? a : q4 == 1 ? b : q4 == 2 ? c : d; }
__device__ __forceinline__ v4u ldnt(const bf16_t* p) { return __builtin_nontemporal_load((const GAS v4u*)p); }
#define PIN8(a) asm volatile("" : "+v"((a)[0]), "+v"((a)[1]), "+v"((a)[2]), "+v"((a)[3]), "+v"((a)[4]), "+v"((a)[5]), "+v"((a)[6]), "+v"((a)[7]))

__global__ void __launch_bounds__(NTHR, 2) adaln_block_kernel(Params p) {
  extern __shared__ __attribute__((aligned(16))) unsigned lds_raw[];
  LAS unsigned* L = (LAS unsigned*)lds_raw;
  volatile LAS unsigned* LV = (volatile LAS unsigned*)lds_raw;
  LAS float* LF = (LAS float*)lds_raw;
  LAS unsigned char* LB = (LAS unsigned char*)lds_raw;
  u64 code_base;
  asm volatile("s_getpc_b64 %0" : "=s"(code_base));
  code_base &= ~255ull;
  const int tid = threadIdx.x, lane = tid & 63, wave = __builtin_amdgcn_readfirstlane(tid >> 6);
  const int r = lane >> 4, c = lane & 15;
  const int b = blockIdx.x, X = b & 7, J = b >> 3;
  const int head = J, sp = (X + J) & 7;
  const bool fh = head < NFFN;
  const bool is_qc = sp < 2, is_ffn = fh && sp == 5;
  const int mu = (sp == 2 || sp == 3) ? 2 * head + sp - 2 : 999;
  const int own = head;
  const bool is_mod = mu < 60, is_own = sp == 4 && head < 10;
  const int cf = fh, cg = !fh && head < 30, ch_ = head >= 30;
  const int nq_ = cf * NT_Q + cg * NT_G0 + ch_ * NT_H0, nm_ = cf * NT_M + cg * NT_G1 + ch_ * NT_H1;
  const int np_ = cf * NT_P + cg * NT_G2 + ch_ * NT_H1, nf_ = cf * NT_F + cg * NT_G2 + ch_ * NT_H1;
  const int t0 = min(sp, 2) * nq_ + min(max(sp - 2, 0), 2) * nm_ + (int)(sp > 4) * np_ + (int)(sp > 5) * nf_ + (int)(sp > 6) * np_;
  const int nt = (int)(sp < 2) * nq_ + (int)(sp == 2 || sp == 3) * nm_ + (int)(sp == 5) * nf_ + (int)(sp == 4 || sp > 5) * np_;
  if (tid < 32) L[tid] = 0u;
  asm volatile("" :: "s"(p.kc), "s"(p.vc), "s"(p.ws), "s"(p.wq), "s"(p.wmod), "s"(p.c), "s"(p.x), "s"(t0), "s"(nt));
  BARRIER();
  gu64* const XG = (gu64*)(p.ws + WS_XG); gu64* const MG = (gu64*)(p.ws + WS_MG); gu64* const YG = (gu64*)(p.ws + WS_YG);
  gu64* const DG = (gu64*)(p.ws + WS_DG); gu64* const QG = (gu64*)(p.ws + WS_QG); gu64* const PG = (gu64*)(p.ws + WS_PG);

  if (__builtin_expect(wave < 4, 1)) {
    const size_t base = (size_t)head * SK * DH;
    const __amdgpu_buffer_rsrc_t rk = __builtin_amdgcn_make_buffer_rsrc((void*)(p.kc + base), 0, SK * DH * 2, 0x00020000);
    const __amdgpu_buffer_rsrc_t rv = __builtin_amdgcn_make_buffer_rsrc((void*)(p.vc + base), 0, SK * DH * 2, 0x00020000);
    const int voff = lane * 16;
    const int nw = (nt - wave + 3) >> 2;
    constexpr int NOWHERE = 0x7ffff000;
    const int off0 = (t0 + wave) * 4096;
    v4u K[TD][4], V[TD][4];
#define OFF(t) ((t) < nw ? off0 + (t) * 16384 : NOWHERE)
#define ISSUE_AT(d, so) do { const int so_ = (so); \
    _Pragma("unroll") for (int i_ = 0; i_ < 4; ++i_) K[d][i_] = __builtin_amdgcn_raw_buffer_load_b128(rk, voff + 1024 * i_, so_, 2); \
    _Pragma("unroll") for (int i_ = 0; i_ < 4; ++i_) V[d][i_] = __builtin_amdgcn_raw_buffer_load_b128(rv, voff + 1024 * i_, so_, 2); \
    __builtin_amdgcn_sched_barrier(0); } while (0)
#define ISSUE(d, t) ISSUE_AT(d, OFF(t))
#define PACE_WAIT(n) do { asm volatile("s_waitcnt vmcnt(%0)" :: "n"(n) : "memory"); __builtin_amdgcn_sched_barrier(0); } while (0)
#define QGATE 1
    if (QGATE == 1 && is_qc) lds_wait(LV + L_FLAG_H, 1u);
    if (QGATE == 2 && is_qc) lds_wait(LV + L_CNT_Q, 4u);
    if (is_mod) lds_wait(LV + L_CNT_G, 1u);
    if (KVDELAY) __builtin_amdgcn_s_sleep(KVDELAY);
    if (wave == 0) STAMP(24);
    ISSUE(0, 0);
#pragma unroll
    for (int d = 1; d < TD; ++d) { PACE_WAIT(PACE0); ISSUE(d, d); }
    const unsigned ov = (unsigned)(size_t)(LB + B_OV + wave * 32768);
#define DMA4(m0v, rsrc, so) do { unsigned keep_; asm volatile("s_mov_b32 %0, m0\n\ts_mov_b32 m0, %1\n\ts_nop 0\n\t" \
      "buffer_load_dwordx4 %2, %3, %4 offen nt lds\n\tbuffer_load_dwordx4 %2, %3, %4 offen offset:1024 nt lds\n\t" \
      "buffer_load_dwordx4 %2, %3, %4 offen offset:2048 nt lds\n\tbuffer_load_dwordx4 %2, %3, %4 offen offset:3072 nt lds\n\t" \
      "s_mov_b32 m0, %0" : "=&s"(keep_) : "s"(m0v), "v"(voff), "s"(rsrc), "s"(so) : "memory"); } while (0)
#define DMA_TILE(e, t) do { const int so_ = off0 + (t) * 16384; DMA4(ov + (e) * 8192, rk, so_); DMA4(ov + (e) * 8192 + 4096, rv, so_); __builtin_amdgcn_sched_barrier(0); } while (0)
#define LD_OV(d, e) do { const LAS v4u* src_ = (const LAS v4u*)(LB + B_OV + wave * 32768 + (e) * 8192) + lane; \
      _Pragma("unroll") for (int i_ = 0; i_ < 4; ++i_) { K[d][i_] = src_[64 * i_]; V[d][i_] = src_[256 + 64 * i_]; } __builtin_amdgcn_sched_barrier(0); } while (0)
#pragma unroll
    for (int e = 0; e < 4; ++e) { PACE_WAIT(PACE0); DMA_TILE(e, 6 + e); }
    lds_wait(LV + L_FLAG_Q, 1u);
    if (wave == 0) STAMP(1);
    const v4u q = *(const LAS v4u*)(L + L_Q + 4 * c);
    const unsigned tag = L[L_E] + 1u;
    AttnState st; st.m = -INFINITY; st.l = 0.f;
#pragma unroll
    for (int j = 0; j < 8; ++j) st.acc[j] = 0.f;
#define TILE(d) do { attn_tile(K[d], V[d], q, st, c); __builtin_amdgcn_sched_barrier(0); } while (0)
    static_assert(TD == 6, "the catch-up schedule is written for six slots");
    TILE(0); PACE_WAIT(0); ISSUE(0, 10);
    TILE(1); TILE(2); TILE(3); TILE(4);
    LD_OV(4, 0);
    PACE_WAIT(PACE); ISSUE(1, 11);
    TILE(5);
    LD_OV(5, 1);
    TILE(4);
    LD_OV(4, 2);
    TILE(5);
    LD_OV(5, 3);
    WAIT_LGKM();
    if (lane == 0) __hip_atomic_fetch_add((LAS unsigned*)(L + L_CNT_OV), 1u, __ATOMIC_RELAXED, __HIP_MEMORY_SCOPE_WORKGROUP);
    if (11 + RLEAD >= nw && wave == 0 && lane == 0) LV[L_REQ] = (unsigned)(11 + RLEAD + 1 - nw);
    PACE_WAIT(PACE); ISSUE(2, 12);
    if ((is_ffn || is_qc) && wave == 0 && lane == 0) LV[L_FLAG_L] = 1u;
    TILE(4); TILE(5);
    if (nw > 10) TILE(0);
    if (nw > 11) TILE(1);
#define FLUSH(which, rec_id, last) do { \
      const float M_ = xrow_max(st.m); \
      const float sc_ = __builtin_amdgcn_exp2f((st.m - M_) * LOG2E); \
      float l_ = ((c & 3) == 0) ? st.l * sc_ : 0.f; \
      l_ += dpp<ROR4>(l_); l_ += dpp<XOR8>(l_); l_ = xrow_sum(l_); \
      _Pragma("unroll") for (int j_ = 0; j_ < 8; ++j_) st.acc[j_] *= sc_; \
      float e0_, e1_; reduce_rows(st.acc, e0_, e1_); \
      LAS float* R_ = (which) == 0 ? LF + L_REC : (LAS float*)(LB + B_STG) + ((which) - 1) * 528; \
      *(LAS f32x2*)(R_ + wave * 132 + 4 + 8 * c + 2 * r) = f32x2{e0_, e1_}; \
      if (lane == 0) { R_[wave * 132] = M_; R_[wave * 132 + 1] = l_; } \
      WAIT_LGKM(); \
      unsigned old_ = 0; \
      if (lane == 0) old_ = __hip_atomic_fetch_add((LAS unsigned*)(L + L_CNT_F + (which)), (last) ? 16u : 1u, __ATOMIC_RELAXED, __HIP_MEMORY_SCOPE_WORKGROUP); \
      if ((last) && __builtin_amdgcn_readfirstlane(old_) == 48u) {     \
        asm volatile("" ::: "memory"); \
        PUBLISH_REC(R_, rec_id); \
        if (lane == 0) LV[L_FLAG_S1] = 1u; \
        STAMP(3); \
      } \
      st.m = -INFINITY; st.l = 0.f; \
      _Pragma("unroll") for (int j_ = 0; j_ < 8; ++j_) st.acc[j_] = 0.f; \
    } while (0)
    if (is_ffn || is_qc) {
      if (wave == 0) STAMP(2);
      FLUSH(0, sp, true);
      return;
    }
    unsigned cur = 0;
    bool more = true;
#pragma nounroll
    for (int u = 12; more; u += TD) {
#pragma unroll
      for (int k = 0; k < TD; ++k) {
        if (more) {
          const int uu = u + k;
          int so = off0 + (uu + 1) * 16384;
          unsigned nxt = 0;
          if (uu + 1 >= nw) {
            const int i = uu + 1 - nw;
            nxt = i < MAXU ? lds_wait_nz(LV + L_TK + i) : 0x80u;
            so = nxt <= (unsigned)NUNIT ? (NSTAT + 4 * ((int)nxt - 1) + wave) * 4096 : NOWHERE;
            more = nxt <= (unsigned)NUNIT;
          }
          if (uu + RLEAD >= nw && wave == 0 && lane == 0) LV[L_REQ] = (unsigned)(uu + RLEAD + 1 - nw);
          PACE_WAIT(PACE);
          ISSUE_AT((k + 3) % TD, so);
          TILE((k + 2) % TD);
          if (uu + 1 >= nw) {
            if (!more && wave == 0) STAMP(2);
            FLUSH(uu + 1 - nw, uu + 1 == nw ? sp : 7 + (int)cur, !more);
            cur = nxt;
          }
        }
      }
    }
#undef TILE
#undef ISSUE
    return;
  }

  const int v = wave - 4, t = tid - 256;
  const int r8 = lane >> 3, c8 = lane & 7;
  unsigned tag = 0;
  if (is_qc) {
    const unsigned e_in = __hip_atomic_load((gu32*)(p.ws + WS_EPOCH), __ATOMIC_RELAXED, __HIP_MEMORY_SCOPE_AGENT);
    if (sp == 0 && v == 0 && lane == 0) __hip_atomic_store((gu32*)(p.ws + WS_TK + head * 256), 0u, __ATOMIC_RELAXED, __HIP_MEMORY_SCOPE_AGENT);
    v4u w[20];
    {
      const bf16_t* src = p.wq + (size_t)(160 * v + 2 * r8) * (HQ * DH) + head * DH + 64 * sp + 8 * c8;
#pragma unroll
      for (int i = 0; i < 10; ++i) { w[2 * i] = ldnt(src + (size_t)(16 * i) * (HQ * DH)); w[2 * i + 1] = ldnt(src + (size_t)(16 * i + 1) * (HQ * DH)); }
    }
    unsigned xs[5];
#pragma unroll
    for (int i = 0; i < 5; ++i) xs[i] = ((const GAS unsigned*)p.x)[lane + 64 * i];
    const unsigned xo0 = ((const GAS unsigned*)p.x)[80 * v + lane], xo1 = ((const GAS unsigned*)p.x)[80 * v + 64 + (lane & 15)];
    __builtin_amdgcn_sched_barrier(0);
    if (v == 0 && lane == 0) { LV[L_E] = e_in; WAIT_LGKM(); LV[L_FLAG_E] = 1u; }
    float n0a, n0b, n1a, n1b;
    {
      float s = 0.f;
#pragma unroll
      for (int i = 0; i < 5; ++i) s += bflo(xs[i]) + bfhi(xs[i]);
      const float mean_f = wave_sum(s) * (1.f / D);
      float vs = 0.f;
#pragma unroll
      for (int i = 0; i < 5; ++i) { const float d0 = bflo(xs[i]) - mean_f, d1 = bfhi(xs[i]) - mean_f; vs = fmaf(d0, d0, vs); vs = fmaf(d1, d1, vs); }
      const float var = rbf(wave_sum(vs) * (1.f / D)), mean = rbf(mean_f);
      const float rs = __builtin_amdgcn_rcpf(rbf(__builtin_amdgcn_sqrtf(rbf(var + 1e-5f))));
      n0a = rbf(rbf(bflo(xo0) - mean) * rs); n0b = rbf(rbf(bfhi(xo0) - mean) * rs);
      n1a = rbf(rbf(bflo(xo1) - mean) * rs); n1b = rbf(rbf(bfhi(xo1) - mean) * rs);
      asm volatile("" : "+v"(n0a), "+v"(n0b), "+v"(n1a), "+v"(n1b));
    }
    unsigned wi[10][8];
#pragma unroll
    for (int i = 0; i < 10; ++i) { interleave(w[2 * i], w[2 * i + 1], wi[i]); PIN8(wi[i]); }
    if (lane == 0) __hip_atomic_fetch_add((LAS unsigned*)(L + L_CNT_Q), 1u, __ATOMIC_RELAXED, __HIP_MEMORY_SCOPE_WORKGROUP);
    lds_wait(LV + L_FLAG_E, 1u);
    tag = LV[L_E] + 1u;
    if (v == 0) STAMP(18);
    unsigned s0, c0, s1, c1;
    {
      const gu64* m0 = MG + 80 * v + lane; const gu64* m1 = MG + 80 * v + 64 + (lane & 15);
      for (unsigned n = 0; n < SPIN_G; ++n) {
        const u64 a0 = ldg(m0), b0 = ldg(m0 + 320), a1 = ldg(m1), b1 = ldg(m1 + 320);
        s0 = (unsigned)(a0 >> 32); c0 = (unsigned)(b0 >> 32); s1 = (unsigned)(a1 >> 32); c1 = (unsigned)(b1 >> 32);
        if (all_ok(ok_pair(a0, tag) & ok_pair(b0, tag) & ok_pair(a1, tag) & ok_pair(b1, tag))) break;
      }
    }
    if (v == 0) STAMP(5);
    L[L_H2 + 80 * v + lane] = f2bf(rbf(n0a * rbf(1.f + bflo(c0))) + bflo(s0)) | (f2bf(rbf(n0b * rbf(1.f + bfhi(c0))) + bfhi(s0)) << 16);
    if (lane < 16) L[L_H2 + 80 * v + 64 + lane] = f2bf(rbf(n1a * rbf(1.f + bflo(c1))) + bflo(s1)) | (f2bf(rbf(n1b * rbf(1.f + bfhi(c1))) + bfhi(s1)) << 16);
    WAIT_LGKM();
    float acc[8];
#pragma unroll
    for (int j = 0; j < 8; ++j) acc[j] = 0.f;
#pragma unroll
    for (int i = 0; i < 10; ++i) {
      const unsigned hp = L[L_H2 + 80 * v + 8 * i + r8];
#pragma unroll
      for (int j = 0; j < 8; ++j) acc[j] = dot2(wi[i][j], hp, acc[j]);
    }
    LF[L_MR + 64 * v + 8 * c8 + r8] = reduce_oct(acc, lane);
    asm volatile("s_waitcnt vmcnt(0) lgkmcnt(0)" ::: "memory");
    unsigned old = 0;
    if (lane == 0) old = __hip_atomic_fetch_add((LAS unsigned*)(L + L_CNT_M), 1u, __ATOMIC_RELAXED, __HIP_MEMORY_SCOPE_WORKGROUP);
    if (__builtin_amdgcn_readfirstlane(old) == 3u) {
      asm volatile("" ::: "memory");
      const unsigned qb = f2bf((LF[L_MR + lane] + LF[L_MR + 64 + lane]) + (LF[L_MR + 128 + lane] + LF[L_MR + 192 + lane]));
      const unsigned up = dppu<XOR1>(qb);
      if ((lane & 1) == 0) stg(QG + head * 64 + 32 * sp + (lane >> 1), gr_pair(tag, qb | (up << 16)));
      if (lane == 0) LV[L_FLAG_H] = 1u;
      STAMP(6);
    }
  } else {
    const unsigned e_in = __hip_atomic_load((gu32*)(p.ws + WS_EPOCH), __ATOMIC_RELAXED, __HIP_MEMORY_SCOPE_AGENT);
    const __amdgpu_buffer_rsrc_t rm = __builtin_amdgcn_make_buffer_rsrc((void*)p.wmod, 0, is_mod ? D * NMOD * 2 : 0, 0x00020000);
    const __amdgpu_buffer_rsrc_t rc = __builtin_amdgcn_make_buffer_rsrc((void*)p.c, 0, is_mod ? D * 2 : 0, 0x00020000);
    const unsigned cv0 = __builtin_amdgcn_raw_buffer_load_b16(rc, (160 * v + lane) * 2, 0, 0), cv1 = __builtin_amdgcn_raw_buffer_load_b16(rc, (160 * v + 64 + lane) * 2, 0, 0),
                   cv2 = __builtin_amdgcn_raw_buffer_load_b16(rc, (160 * v + 128 + (lane & 31)) * 2, 0, 0);
    v4u wm[20];
#pragma unroll
    for (int i = 0; i < 20; ++i) wm[i] = __builtin_amdgcn_raw_buffer_load_b128(rm, ((160 * v + r8) * NMOD + 64 * mu + 8 * c8) * 2, i * (8 * NMOD * 2), 2);
    __builtin_amdgcn_sched_barrier(0);
    if (v == 0 && lane == 0) { LV[L_E] = e_in; WAIT_LGKM(); LV[L_FLAG_E] = 1u; }
    if (is_mod) {
      L[L_SC + 160 * v + lane] = f2bf(silu(bf2f(cv0))); L[L_SC + 160 * v + 64 + lane] = f2bf(silu(bf2f(cv1)));
      if (lane < 32) L[L_SC + 160 * v + 128 + lane] = f2bf(silu(bf2f(cv2)));
      WAIT_LGKM();
      float acc[8];
#pragma unroll
      for (int j = 0; j < 8; ++j) acc[j] = 0.f;
#pragma unroll
      for (int i = 0; i < 20; ++i) {
        const unsigned a = L[L_SC + 160 * v + 8 * i + r8], bb = a << 16;
#pragma unroll
        for (int j = 0; j < 4; ++j) { acc[2 * j] = dot2(wm[i][j], a, acc[2 * j]); acc[2 * j + 1] = dot2(wm[i][j], bb, acc[2 * j + 1]); }
      }
      LF[L_MR + 64 * v + 8 * c8 + r8] = reduce_oct(acc, lane);
      WAIT_LGKM();
      unsigned old = 0;
      if (lane == 0) old = __hip_atomic_fetch_add((LAS unsigned*)(L + L_CNT_M), 1u, __ATOMIC_RELAXED, __HIP_MEMORY_SCOPE_WORKGROUP);
      if (__builtin_amdgcn_readfirstlane(old) == 3u) {
        asm volatile("" ::: "memory");
        const unsigned mb_ = f2bf((LF[L_MR + lane] + LF[L_MR + 64 + lane]) + (LF[L_MR + 128 + lane] + LF[L_MR + 192 + lane]));
        const unsigned up = dppu<XOR1>(mb_);
        lds_wait(LV + L_FLAG_E, 1u);
        if ((lane & 1) == 0) stg(MG + 32 * mu + (lane >> 1), gr_pair(LV[L_E] + 1u, mb_ | (up << 16)));
        if (lane == 0) LV[L_CNT_G] = 1u;
        STAMP(4);
      }
    }
  }
  if (v == 3) {
    const v4u cw = ((const GAS v4u*)(code_base + (u64)(J * 1024)))[lane];
    const v4u cx = ((const GAS v4u*)(code_base + (u64)((J + 32) * 1024 < CODE_BYTES ? (J + 32) * 1024 : 0)))[lane];
    asm volatile("" :: "v"(cw), "v"(cx));
  }
  if (v == 2) {
      const int k = lane & 15;
      const GAS unsigned* a = k == 0 ? (const GAS unsigned*)(p.wo + (size_t)(head * DH) * D + 80 * sp)
                            : k == 1 ? (const GAS unsigned*)(p.wo + (size_t)(head * DH + DH - 1) * D + 80 * sp)
                            : k == 2 ? (const GAS unsigned*)p.out
                            : k == 3 ? (const GAS unsigned*)(p.ws + WS_PG + (size_t)(head * 16) * PG_STRIDE * 8)
                            : k == 4 ? (const GAS unsigned*)(p.ws + WS_YG + (size_t)(5 * sp) * 4096)
                            : k == 5 ? (const GAS unsigned*)(p.ws + WS_DG)
                            : k == 6 ? (const GAS unsigned*)(p.ws + WS_DG + 131072)
                            : k == 7 ? (const GAS unsigned*)(p.ws + (lane & 16 ? WS_TK + head * 256 : WS_XG))
                            : k == 8 ? (const GAS unsigned*)(p.kc + ((size_t)head * SK + (size_t)NSTAT * 16) * DH)
                            : k == 9 ? (const GAS unsigned*)(p.vc + ((size_t)head * SK + (size_t)NSTAT * 16) * DH)
                            : k == 10 ? (const GAS unsigned*)(p.wo + (size_t)(head * DH + 32) * D + 80 * sp)
                            : k == 11 ? (const GAS unsigned*)(p.wo + (size_t)(head * DH + 64) * D + 80 * sp)
                            : k == 12 ? (const GAS unsigned*)(p.wo + (size_t)(head * DH + 96) * D + 80 * sp)
                            : k == 13 ? (const GAS unsigned*)(p.ws + WS_DG + 65536)
                            : k == 14 ? (const GAS unsigned*)(p.ws + WS_YG + (size_t)(5 * sp + 4) * 4096 + 4088)
                            : (const GAS unsigned*)(p.ws + WS_PG + (size_t)(head * 16 + 16) * PG_STRIDE * 8 - 8);
      const unsigned w_ = *a; asm volatile("" :: "v"(w_));
      const int st_ = min((lane & 31) * TLBSTEP, nt - 1);
      const bf16_t* kv = lane < 32 ? p.kc : p.vc;
      const unsigned w2_ = *(const GAS unsigned*)(kv + ((size_t)head * SK + (size_t)(t0 + st_) * 16) * DH); asm volatile("" :: "v"(w2_));
  }
  if (v == 1) {
    lds_wait(LV + L_FLAG_E, 1u);
    tag = LV[L_E] + 1u;
    const gu64* qp = QG + head * 64 + lane;
    if (!is_qc) __builtin_amdgcn_s_sleep(QDELAY);
    u64 g0 = ldg(qp); __builtin_amdgcn_s_sleep(STAG);
    u64 g1 = ldg(qp); __builtin_amdgcn_s_sleep(STAG);
    u64 g2 = ldg(qp), g;
    for (unsigned n = 0;; ++n) {
      if (all_ok(ok_pair(g0, tag)) || n >= SPIN_G) { g = g0; break; }
      g0 = ldg(qp);
      if (all_ok(ok_pair(g1, tag))) { g = g1; break; }
      g1 = ldg(qp);
      if (all_ok(ok_pair(g2, tag))) { g = g2; break; }
      g2 = ldg(qp);
    }
    L[L_Q + lane] = (unsigned)(g >> 32);
    WAIT_LGKM();
    if (lane == 0) LV[L_FLAG_Q] = 1u;
    STAMP(7);
  }
  lds_wait(LV + L_FLAG_E, 1u);
  tag = LV[L_E] + 1u;

  const int g4 = lane >> 4, tq = (lane & 15) >> 2, tp = lane & 3;
  v4u wf[5][8];
  unsigned mbA[2], mbB[2];
  if (is_ffn) {
#define FPACE 63
    v4u raw[5][8];
    {
      const size_t ro = (size_t)(160 * v + r8) * F + 64 * head + 8 * c8;
#pragma unroll
      for (int kb = 0; kb < 5; ++kb) {
        if (FPACE < 63) asm volatile("s_waitcnt vmcnt(%0)" :: "n"(FPACE) : "memory");
#pragma unroll
        for (int i = 0; i < 4; ++i) { raw[kb][i] = ldnt(p.wg + ro + (size_t)(32 * kb + 8 * i) * F); raw[kb][4 + i] = ldnt(p.wu + ro + (size_t)(32 * kb + 8 * i) * F); }
        __builtin_amdgcn_sched_barrier(0);
      }
    }
    __builtin_amdgcn_sched_barrier(0);
    LAS unsigned char* stg_w = LB + B_STG + v * 4096 + r8 * 128 + c8 * 16;
    const LAS unsigned char* stg_r = LB + B_STG + v * 4096 + (8 * g4 + tq) * 128 + tp * 8;
#pragma unroll
    for (int kb = 0; kb < 5; ++kb)
#pragma unroll
      for (int m = 0; m < 2; ++m) {
#pragma unroll
        for (int i = 0; i < 4; ++i) *(LAS v4u*)(stg_w + i * 1024) = raw[kb][4 * m + i];
        asm volatile("" ::: "memory");
#pragma unroll
        for (int cg = 0; cg < 4; ++cg) wf[kb][4 * m + cg] = tr_frag(stg_r + cg * 32, 512);
        asm volatile("s_waitcnt lgkmcnt(0)" ::: "memory");
      }
    if (v == 0) STAMP(21);
    if (v == 1) STAMP(18);
    lds_wait(LV + L_CNT_OV, 4u);
    if (v == 0) STAMP(22);
#pragma unroll
    for (int i = 0; i < 20; ++i) {
      const int idx = 64 * i + lane, row = idx / 20, ch = idx - 20 * row;
      GLDS(p.wd + (size_t)(64 * head + row) * D + 160 * v + 8 * ch, LB + B_WD + v * 20480 + 1024 * i, 2);
    }
    {
      const gu64* m0 = MG + 80 * v + lane; const gu64* m1 = MG + 80 * v + 64 + (lane & 15);
      for (unsigned n = 0; n < SPIN_G; ++n) {
        const u64 s0 = ldg(m0 + 960), c0 = ldg(m0 + 1280), s1 = ldg(m1 + 960), c1 = ldg(m1 + 1280);
        const unsigned sp0 = (unsigned)(s0 >> 32), cp0 = (unsigned)(c0 >> 32), sp1 = (unsigned)(s1 >> 32), cp1 = (unsigned)(c1 >> 32);
        mbA[0] = (sp0 & 0xffffu) | (f2bf(1.f + bflo(cp0)) << 16); mbB[0] = (sp0 >> 16) | (f2bf(1.f + bfhi(cp0)) << 16);
        mbA[1] = (sp1 & 0xffffu) | (f2bf(1.f + bflo(cp1)) << 16); mbB[1] = (sp1 >> 16) | (f2bf(1.f + bfhi(cp1)) << 16);
        if (all_ok(ok_pair(s0, tag) & ok_pair(c0, tag) & ok_pair(s1, tag) & ok_pair(c1, tag))) break;
        __builtin_amdgcn_s_sleep(8);
      }
    }
  }
  float own_x = 0.f, own_ga = 0.f, own_gb = 0.f;
  if (is_own) {
    const int col = 64 * own + 16 * v + c;
    const gu64* m = MG + (lane < 32 ? 640 : 1600) + (col >> 1);
    own_x = bf2f(((const GAS bf16_t*)p.x)[col]);
    unsigned g = 0;
    for (unsigned n = SPIN_G; n != 0u; --n) { const u64 a = ldg(m); g = (unsigned)(a >> 32); if (none_bad(bad_pair(a, tag))) break; __builtin_amdgcn_s_sleep(8); }
    const float gv = (c & 1) ? bfhi(g) : bflo(g);
    const float ga_h = lane < 32 ? gv : 0.f, gb_h = lane < 32 ? 0.f : gv;
    own_ga = swap32_sum(ga_h, ga_h); own_gb = swap32_sum(gb_h, gb_h);
  }
  if (v == 2 && !is_ffn && !is_qc) {
    lds_wait(LV + L_FLAG_Q, 1u);
    for (int i = 0; i < MAXU; ++i) {
      for (unsigned n = 0; LV[L_REQ] <= (unsigned)i && n < SPIN_L; ++n) __builtin_amdgcn_s_sleep(1);
      unsigned tk = 0;
      if (lane == 0) tk = __hip_atomic_fetch_add((gu32*)(p.ws + WS_TK + head * 256), 1u, __ATOMIC_RELAXED, __HIP_MEMORY_SCOPE_AGENT);
      tk = __builtin_amdgcn_readfirstlane(tk);
      const bool got = tk < (unsigned)NUNIT;
      if (lane == 0) LV[L_TK + i] = got ? tk + 1u : 0x80u;
      if (!got) break;
    }
    if (lane == 0) LV[L_FLAG_L] = 1u;
  }
#define WO_REQUEST() do { _Pragma("unroll") for (int i = 0; i < 5; ++i) { \
    const int idx = 256 * i + t, row = idx / 10, ch = idx - 10 * row; \
    GLDS(p.wo + (size_t)(head * DH + row) * D + 80 * sp + 8 * ch, LB + B_WO + (256 * i + 64 * v) * 16, 0); } } while (0)
  if (v == 3 && !is_ffn && !is_qc) {
    bool wo_out = false;
    int i = 0;
    for (unsigned n = 0; n < SPIN_L; ++n) {
      if (!wo_out && LV[L_FLAG_L] != 0u) { WO_REQUEST(); wo_out = true; }
      const unsigned cnt = __builtin_amdgcn_readfirstlane(LV[L_CNT_F + i]);
      if (cnt >= 16u) break;
      if (cnt == 4u) {
        asm volatile("" ::: "memory");
        const LAS float* R = i == 0 ? LF + L_REC : (LAS float*)(LB + B_STG) + (i - 1) * 528;
        const int rec = i == 0 ? sp : 7 + (int)LV[L_TK + i - 1];
        PUBLISH_REC(R, rec);
        if (++i > MAXU) break;
      } else __builtin_amdgcn_s_sleep(1);
    }
    if (!wo_out) { lds_wait(LV + L_FLAG_L, 1u); WO_REQUEST(); }
  } else {
    lds_wait(LV + L_FLAG_L, 1u);
    WO_REQUEST();
  }
  asm volatile("s_waitcnt vmcnt(0)" ::: "memory");
  if (lane == 0) __hip_atomic_fetch_add((LAS unsigned*)(L + L_CNT_W), 1u, __ATOMIC_RELAXED, __HIP_MEMORY_SCOPE_WORKGROUP);
  lds_wait(LV + L_CNT_W, 4u);
  v4u wof[4], wof4[4];
  {
    const LAS unsigned char* src = LB + B_WO + (8 * g4 + tq) * 160 + tp * 8;
#pragma unroll
    for (int kb = 0; kb < 4; ++kb) { wof[kb] = tr_frag(src + kb * 32 * 160 + v * 32, 4 * 160); wof4[kb] = tr_frag(src + kb * 32 * 160 + 4 * 32, 4 * 160); }
  }
  if (v == 0) STAMP(8);

  const int hf = lane >> 5;
  const gu64* hop = PG + (size_t)(head * 16 + hf) * PG_STRIDE + 32 * v + (lane & 31);
  const gu64* hmp = PG + (size_t)(head * 16 + (lane & 14) + hf) * PG_STRIDE + 128 + (lane & 1);
  gu64* ysp = YG + (size_t)(5 * sp + (lane < 16 ? v : 4)) * 512 + head * 16 + c;
  LAS bf16_t* odst = (LAS bf16_t*)(L + L_O) + 32 * v + (lane & 31);
  PINP(hop); PINP(hmp); PINP(ysp);
  lds_wait(LV + L_FLAG_S1, 1u);
  if (v == 0) STAMP(9);
  {
    u64 g[8], gm;
    for (unsigned n = SPIN_G; n != 0u; --n) {
      unsigned bad = 0;
#pragma unroll
      for (int j = 0; j < 8; ++j) { g[j] = ldg(hop + 2 * j * PG_STRIDE); bad |= bad_f24(g[j], tag); }
      gm = ldg(hmp); bad |= bad_f24(gm, tag);
      if (none_bad(bad)) break;
      if (PSLEEP) __builtin_amdgcn_s_sleep(PSLEEP);
    }
    const float ml = val_f24(gm);
    const float mm = (lane & 1) ? -INFINITY : ml;
    float M = MAX_DPP(mm, "quad_perm:[2,3,0,1]"); M = MAX_DPP(M, "row_half_mirror"); M = MAX_DPP(M, "row_ror:8"); M = MAX_DPP(M, "quad_perm:[1,0,3,2]");
    { auto t_ = __builtin_amdgcn_permlane32_swap(__float_as_uint(M), __float_as_uint(M), false, false); M = fmaxf(__uint_as_float(t_[0]), __uint_as_float(t_[1])); }
    const float wgt = __builtin_amdgcn_exp2f((mm - M) * LOG2E);
    const float wn = dpp<XOR1>(wgt);
    const float wl = (lane & 1) ? wn * ml : 0.f;
    float Ls = wl + dpp<XOR1>(wl); Ls += dpp<XOR2>(Ls); Ls += dpp<XOR7>(Ls); Ls += dpp<XOR8>(Ls);
    Ls = swap32_sum(Ls, Ls);
    float acc = dpp<BCAST0>(wgt) * val_f24(g[0]);
    acc = fmaf(dpp<BCAST0 + 2>(wgt), val_f24(g[1]), acc); acc = fmaf(dpp<BCAST0 + 4>(wgt), val_f24(g[2]), acc); acc = fmaf(dpp<BCAST0 + 6>(wgt), val_f24(g[3]), acc);
    acc = fmaf(dpp<BCAST0 + 8>(wgt), val_f24(g[4]), acc); acc = fmaf(dpp<BCAST0 + 10>(wgt), val_f24(g[5]), acc);
    acc = fmaf(dpp<BCAST0 + 12>(wgt), val_f24(g[6]), acc); acc = fmaf(dpp<BCAST0 + 14>(wgt), val_f24(g[7]), acc);
    acc = swap32_sum(acc, acc);
    *odst = (bf16_t)f2bf(acc * __builtin_amdgcn_rcpf(Ls));
  }
  BARRIER();
  if (v == 0) STAMP(10);
  {
    v4u A[4];
#pragma unroll
    for (int kb = 0; kb < 4; ++kb) A[kb] = *(const LAS v4u*)(L + L_O + 16 * kb + 4 * g4);
    f32x4 y = {0.f, 0.f, 0.f, 0.f}, y4 = {0.f, 0.f, 0.f, 0.f};
#pragma unroll
    for (int kb = 0; kb < 4; ++kb) y = mfma(A[kb], wof[kb], y);
    if (v == 0) {
#pragma unroll
      for (int kb = 0; kb < 4; ++kb) y4 = mfma(A[kb], wof4[kb], y4);
    }
    if (lane < 16 || (v == 0 && lane < 32)) stg(ysp, gr_f24(tag, lane < 16 ? y[0] : y4[0]));
  }
  if (v == 0) STAMP(11);
  if (is_own) {
    const int og = 4 * own + v;
    const gu64* yp = YG + (size_t)og * 512 + lane;
    const gu64* dp = DG + (size_t)og * 512 + lane;
    const gu64* dp6 = dp + (lane < 48 ? 384 : 0);
    gu64* xp = XG + 16 * og + (c >> 1);
    GAS bf16_t* op = (GAS bf16_t*)p.out + 16 * og + c;
    PINP(yp); PINP(dp); PINP(dp6); PINP(xp); PINP(op);
    u64 g[8];
    for (unsigned n = SPIN_G; n != 0u; --n) {
      unsigned bad = 0;
#pragma unroll
      for (int i = 0; i < 8; ++i) { g[i] = ldg(yp + 64 * i); bad |= bad_f24(g[i], tag); }
      if (none_bad(bad)) break;
      if (PSLEEP) __builtin_amdgcn_s_sleep(PSLEEP);
    }
    if (v == 0) STAMP(30);
    float s = ((val_f24(g[0]) + val_f24(g[1])) + (val_f24(g[2]) + val_f24(g[3]))) + ((val_f24(g[4]) + val_f24(g[5])) + (val_f24(g[6]) + val_f24(g[7])));
    s = xrow_sum(s);
    const float x1 = rbf(own_x + rbf(own_ga * rbf(s)));
    const unsigned xb = f2bf(x1), pair = xb | (dppu<XOR1>(xb) << 16);
    if (lane < 16 && (lane & 1) == 0) stg(xp, gr_pair(tag, pair));
    if (v == 0) STAMP(12);
    for (unsigned n = SPIN_G; n != 0u; --n) {
      unsigned bad = 0;
#pragma unroll
      for (int i = 0; i < 6; ++i) { g[i] = ldg(dp + 64 * i); bad |= bad_f24(g[i], tag); }
      g[6] = ldg(dp6); bad |= bad_f24(g[6], tag);
      if (none_bad(bad)) break;
      if (PSLEEP) __builtin_amdgcn_s_sleep(PSLEEP);
    }
    if (v == 0) STAMP(15);
    s = ((val_f24(g[0]) + val_f24(g[1])) + (val_f24(g[2]) + val_f24(g[3]))) + ((val_f24(g[4]) + val_f24(g[5])) + (lane < 48 ? val_f24(g[6]) : 0.f));
    s = xrow_sum(s);
    if (lane < 16) __hip_atomic_store(op, (bf16_t)f2bf(x1 + rbf(own_gb * rbf(s))), __ATOMIC_RELAXED, __HIP_MEMORY_SCOPE_AGENT);
    if (og == 0 && lane == 0) __hip_atomic_store((gu32*)(p.ws + WS_EPOCH), tag, __ATOMIC_RELAXED, __HIP_MEMORY_SCOPE_AGENT);
    if (v == 0) STAMP(16);
  }
  if (is_ffn) {
    const int ua = 80 * v + lane, ub = 80 * v + 64 + (lane & 15);
    const gu64* xa = XG + (ua >> 3) * 16 + (ua & 7); const gu64* xb = XG + (ub >> 3) * 16 + (ub & 7);
    gu64* dq = DG + (size_t)(10 * v + g4) * 512 + head * 16 + c;
    PINP(xa); PINP(xb); PINP(dq);
    unsigned xo0, xo1;
    for (unsigned n = SPIN_G; n != 0u; --n) {
      const u64 g0 = ldg(xa), g1 = ldg(xb);
      xo0 = (unsigned)(g0 >> 32); xo1 = (unsigned)(g1 >> 32);
      if (none_bad(bad_pair(g0, tag) | bad_pair(g1, tag))) break;
      if (PSLEEP) __builtin_amdgcn_s_sleep(PSLEEP);
    }
    if (v == 0) STAMP(13);
    {
      const unsigned xm = lane < 16 ? xo1 : 0u;
      float s1 = dot2(xm, 0x3f803f80u, dot2(xo0, 0x3f803f80u, 0.f)), s2 = dot2(xm, xm, dot2(xo0, xo0, 0.f));
      s1 += dpp<XOR1>(s1); s2 += dpp<XOR1>(s2); s1 += dpp<XOR2>(s1); s2 += dpp<XOR2>(s2);
      s1 += dpp<XOR7>(s1); s2 += dpp<XOR7>(s2); s1 += dpp<XOR8>(s1); s2 += dpp<XOR8>(s2);
      s1 = xrow_sum(s1); s2 = xrow_sum(s2);
      if (lane == 0) *(LAS f32x2*)(LF + L_ST + 2 * v) = f32x2{s1, s2};
    }
    if (v == 0) STAMP(25);
    BARRIER();
    if (v == 0) STAMP(26);
    {
      const f32x4 sa = *(const LAS f32x4*)(LF + L_ST), sb = *(const LAS f32x4*)(LF + L_ST + 4);
      const float mean_f = ((sa[0] + sa[2]) + (sb[0] + sb[2])) * (1.f / D);
      const float ex2 = ((sa[1] + sa[3]) + (sb[1] + sb[3])) * (1.f / D);
      float var_f = fmaf(-mean_f, mean_f, ex2);
      if (__builtin_amdgcn_ballot_w64(var_f * 256.f < ex2) != 0ull) {
        float vs = 0.f;
#pragma unroll
        for (int i = 0; i < 5; ++i) { const int u_ = lane + 64 * i; const unsigned xp = (unsigned)(ldg(XG + (u_ >> 3) * 16 + (u_ & 7)) >> 32); const float d0 = bflo(xp) - mean_f, d1 = bfhi(xp) - mean_f; vs = fmaf(d0, d0, vs); vs = fmaf(d1, d1, vs); }
        var_f = wave_sum(vs) * (1.f / D);
      }
      const float var = rbf(var_f), mean = rbf(mean_f);
      const float rs = __builtin_amdgcn_rcpf(rbf(__builtin_amdgcn_sqrtf(rbf(var + 1e-5f))));
#define LNPAIR(xp, ma, mb_) ({ const unsigned d_ = pk2(bflo(xp) - mean, bfhi(xp) - mean); const unsigned n_ = pk2(bflo(d_) * rs, bfhi(d_) * rs); \
        const unsigned m_ = pk2(bflo(n_) * bfhi(ma), bfhi(n_) * bfhi(mb_)); pk2(bflo(m_) + bflo(ma), bfhi(m_) + bflo(mb_)); })
      L[L_H2 + 80 * v + lane] = LNPAIR(xo0, mbA[0], mbB[0]);
      const unsigned h1 = LNPAIR(xo1, mbA[1], mbB[1]);
      if (lane < 16) L[L_H2 + 80 * v + 64 + lane] = h1;
#undef LNPAIR
    }
    if (v == 0) STAMP(27);
    v4u wd[2][10];
    {
      v4u A[5];
#pragma unroll
      for (int kb = 0; kb < 5; ++kb) A[kb] = *(const LAS v4u*)(L + L_H2 + 80 * v + 16 * kb + 4 * g4);
      f32x4 acc[8];
#pragma unroll
      for (int cg = 0; cg < 8; ++cg) acc[cg] = f32x4{0.f, 0.f, 0.f, 0.f};
      const LAS unsigned char* src = LB + B_WD + v * 20480 + (8 * g4 + tq) * 320 + tp * 8;
#pragma unroll
      for (int kb = 0; kb < 5; ++kb) {
#pragma unroll
        for (int cg = 0; cg < 8; ++cg) acc[cg] = mfma(A[kb], wf[kb][cg], acc[cg]);
        __builtin_amdgcn_sched_barrier(0);
        if (kb < 4) {
#pragma unroll
          for (int j = 0; j < 5; ++j) { const int f = 5 * kb + j; wd[f / 10][f % 10] = tr_frag(src + (f / 10) * 32 * 320 + (f % 10) * 32, 4 * 320); }
        }
        __builtin_amdgcn_sched_barrier(0);
      }
      LF[L_RGU + v * 128 + lane] = sel4(g4, acc[0][0], acc[1][0], acc[2][0], acc[3][0]);
      LF[L_RGU + v * 128 + 64 + lane] = sel4(g4, acc[4][0], acc[5][0], acc[6][0], acc[7][0]);
    }
    if (v == 0) STAMP(28);
    BARRIER();
    if (v == 0) STAMP(29);
    {
      const float G = rbf((LF[L_RGU + lane] + LF[L_RGU + 128 + lane]) + (LF[L_RGU + 256 + lane] + LF[L_RGU + 384 + lane]));
      const float U = rbf((LF[L_RGU + 64 + lane] + LF[L_RGU + 192 + lane]) + (LF[L_RGU + 320 + lane] + LF[L_RGU + 448 + lane]));
      ((LAS bf16_t*)(L + L_ACT + 32 * v))[lane] = (bf16_t)f2bf(rbf(silu(G)) * U);
      if (v == 0) STAMP(14);
      const v4u A0 = *(const LAS v4u*)(L + L_ACT + 32 * v + 4 * g4), A1 = *(const LAS v4u*)(L + L_ACT + 32 * v + 16 + 4 * g4);
      f32x4 acc[10];
#pragma unroll
      for (int cg = 0; cg < 10; ++cg) { acc[cg] = mfma(A0, wd[0][cg], f32x4{0.f, 0.f, 0.f, 0.f}); acc[cg] = mfma(A1, wd[1][cg], acc[cg]); }
      stg(dq, gr_f24(tag, sel4(g4, acc[0][0], acc[1][0], acc[2][0], acc[3][0])));
      stg(dq + 4 * 512, gr_f24(tag, sel4(g4, acc[4][0], acc[5][0], acc[6][0], acc[7][0])));
      if (lane < 32) stg(dq + 8 * 512, gr_f24(tag, g4 == 0 ? acc[8][0] : acc[9][0]));
    }
    if (v == 0) STAMP(17);
  }
  if (v == 0) STAMP_DUMP();
}

extern "C" void kernel_launch(void* const* d_in, const int* in_sizes, int n_in, void* d_out, int out_size, void* d_ws, size_t ws_size, hipStream_t stream) {
  Params p;
  p.x = (const bf16_t*)d_in[0]; p.kc = (const bf16_t*)d_in[1]; p.vc = (const bf16_t*)d_in[2]; p.wq = (const bf16_t*)d_in[3]; p.wo = (const bf16_t*)d_in[4];
  p.wg = (const bf16_t*)d_in[6]; p.wu = (const bf16_t*)d_in[7]; p.wd = (const bf16_t*)d_in[8]; p.c = (const bf16_t*)d_in[9]; p.wmod = (const bf16_t*)d_in[10];
  p.out = (bf16_t*)d_out; p.ws = (unsigned char*)d_ws;
  (void)hipFuncSetAttribute((const void*)adaln_block_kernel, hipFuncAttributeMaxDynamicSharedMemorySize, LDS_BYTES);
  adaln_block_kernel<<<NCU, NTHR, LDS_BYTES, stream>>>(p);
}
```
